```python
import torch

def _softmax(x, axis=-1):
    return torch.softmax(x, dim=axis)
def _gelu(x):
    return torch.nn.functional.gelu(x, approximate="tanh")
def _silu(x):
    return torch.nn.functional.silu(x)
def _sigmoid(x):
    return torch.sigmoid(x)
def _layernorm(x, axis=-1, eps=1e-5):
    m = x.mean(dim=axis, keepdim=True)
    v = x.var(dim=axis, keepdim=True, unbiased=False)
    return (x - m) / torch.sqrt(v + eps)
def _scatter_add(tgt, idx, src, axis=0):
    return tgt.clone().index_add_(axis, idx, src)
def _segment_sum(src, seg, n):
    out = torch.zeros((n,) + src.shape[1:], dtype=src.dtype)
    return out.index_add_(0, seg, src)
def _pad(x, pw):
    flat = tuple(v for pair in reversed(pw) for v in pair)
    return torch.nn.functional.pad(x, flat)

kernel_name = 'recurrent_scan__ssm_conv_s4d__dense_activation_squared_relu__prompt'

def setup_inputs():

    _w = (lambda a: (lambda t: t.to(torch.bfloat16) if t.is_floating_point() else t)(torch.as_tensor(a)))
    def _A_setup():
        Bsz, L, D, N = 1, 2048, 640, 192
        return {
            "u": _w((lambda *s: torch.randn(*s, dtype=torch.bfloat16) * 0.05)(Bsz, L, D)),
            "log_A_real": _w((lambda *s: torch.randn(*s, dtype=torch.bfloat16) * 3.2)(D, N) * 0.5),
            "A_imag": _w((lambda *s: torch.randn(*s, dtype=torch.bfloat16) * 0.05)(D, N) * 3.14),
            "C_real": _w((lambda *s: torch.randn(*s, dtype=torch.bfloat16) * 0.2)(D, N) / N ** 0.5),
            "C_imag": _w((lambda *s: torch.randn(*s, dtype=torch.bfloat16) * 0.2)(D, N) / N ** 0.5),
            "log_dt": _w((lambda *s: torch.randn(*s, dtype=torch.bfloat16) * 0.05)(D) * 0.5 - 4.0),
            "D_skip": _w((lambda *s: torch.randn(*s, dtype=torch.bfloat16) * 0.05)(D)),
        }
    _A_in = _A_setup()

    def _B_setup():
        B, S, D, F = 1, 2048, 640, 1536
        return {
            "x": _w((lambda *s: torch.randn(*s, dtype=torch.bfloat16) * 0.05)(B, S, D)),
            "w1": _w((lambda *s: torch.randn(*s, dtype=torch.bfloat16) * 0.05)(D, F)),
            "b1": _w((lambda *s: torch.randn(*s, dtype=torch.bfloat16) * 0.4)(F)),
            "w2": _w((lambda *s: torch.randn(*s, dtype=torch.bfloat16) * 0.05)(F, D)),
            "b2": _w((lambda *s: torch.randn(*s, dtype=torch.bfloat16) * 1.6)(D)),
        }
    _B_in = _B_setup()

    return {
        "A_u": _A_in["u"],
        "A_log_A_real": _A_in["log_A_real"],
        "A_A_imag": _A_in["A_imag"],
        "A_C_real": _A_in["C_real"],
        "A_C_imag": _A_in["C_imag"],
        "A_log_dt": _A_in["log_dt"],
        "A_D_skip": _A_in["D_skip"],
        "B_x": _B_in["x"],
        "B_w1": _B_in["w1"],
        "B_b1": _B_in["b1"],
        "B_w2": _B_in["w2"],
        "B_b2": _B_in["b2"],
    }

def reference(A_u, A_log_A_real, A_A_imag, A_C_real, A_C_imag, A_log_dt, A_D_skip, B_x, B_w1, B_b1, B_w2, B_b2):
    def _A_ref(u, log_A_real, A_imag, C_real, C_imag, log_dt, D_skip):
        L = u.shape[1]
        dt = torch.exp(log_dt)
        A = -torch.exp(log_A_real) + 1j * A_imag
        C = (C_real + 1j * C_imag) * dt[:, None]
        pos = (lambda x, dt: x.to(dt))(torch.arange(L), log_dt.dtype)
        P = torch.exp((A * dt[:, None])[:, :, None] * pos[None, None, :])
        K = 2.0 * torch.einsum("dn,dnl->dl", C, P).real
        L2 = 2 * L
        u_t = (lambda x, *d: x.permute(*d))(u, 0, 2, 1)
        Kf = (lambda x, *a, **k: torch.fft.rfft(x.float(), *a, **k))(K, L2)
        Uf = (lambda x, *a, **k: torch.fft.rfft(x.float(), *a, **k))(u_t, L2)
        y = (lambda x, *a, **k: torch.fft.irfft(x, *a, **k).to(torch.bfloat16))(Uf * Kf[None, :, :], L2)[:, :, :L]
        y = (lambda x, *d: x.permute(*d))(y, 0, 2, 1)
        return y + u * D_skip[None, None, :]

    def _B_ref(x, w1, b1, w2, b2):
        h = torch.einsum("bsd,df->bsf", x, w1) + b1
        zero = torch.zeros_like(h)
        r = torch.where(h > zero, h, zero)
        h = r * r
        out = torch.einsum("bsf,fd->bsd", h, w2) + b2
        return out

    x = A_u
    xn = _layernorm(x, axis=-1)
    s = _A_ref(xn, A_log_A_real, A_A_imag, A_C_real, A_C_imag, A_log_dt, A_D_skip)
    g = _silu(_B_ref(xn, B_w1, B_b1, B_w2, B_b2))
    return x + s * g
```

```cpp
#include <hip/hip_runtime.h>
#include <hip/hip_bf16.h>
#include <stdint.h>
typedef unsigned short u16;
constexpr int L = 2048, D = 640, N = 192, F = 1536;
__device__ __forceinline__ float bf2f(u16 v) { return __uint_as_float(((unsigned)v) << 16); }
__device__ __forceinline__ u16 f2bf(float f) { unsigned u = __float_as_uint(f); return (u16)((u + 0x7FFF + ((u >> 16) & 1)) >> 16); }
__device__ __forceinline__ float rbf(float f) { return bf2f(f2bf(f)); }

__global__ void k_ln(const u16* x, u16* xn) {
  int row = blockIdx.x, t = threadIdx.x;
  __shared__ float red[64];
  float s = 0;
  for (int i = t; i < D; i += 64) s += bf2f(x[row * D + i]);
  for (int o = 32; o; o >>= 1) s += __shfl_xor(s, o);
  float mean = s / D;
  float q = 0;
  for (int i = t; i < D; i += 64) { float d = bf2f(x[row * D + i]) - mean; q += d * d; }
  for (int o = 32; o; o >>= 1) q += __shfl_xor(q, o);
  float m = rbf(mean), v = rbf(q / D);
  float sd = rbf(sqrtf(rbf(v + 1e-5f)));
  for (int i = t; i < D; i += 64) xn[row * D + i] = f2bf(rbf(bf2f(x[row * D + i]) - m) / sd);
}
__global__ void k_kgen(const u16* la, const u16* ai, const u16* cr, const u16* ci, const u16* ldt, float* K) {
  int d = blockIdx.x;
  float dt = rbf(expf(bf2f(ldt[d])));
  for (int l = threadIdx.x; l < L; l += blockDim.x) {
    float p = rbf((float)l);
    float acc = 0;
    for (int n = 0; n < N; ++n) {
      float ar = -rbf(expf(bf2f(la[d * N + n]))) * dt, aim = bf2f(ai[d * N + n]) * dt;
      float e = expf(ar * p), sn, cs;
      sincosf(aim * p, &sn, &cs);
      acc += bf2f(cr[d * N + n]) * dt * (e * cs) - bf2f(ci[d * N + n]) * dt * (e * sn);
    }
    K[d * L + l] = 2.f * acc;
  }
}
__global__ void k_conv(const u16* xn, const float* K, const u16* dskip, u16* s) {
  int l = blockIdx.x;
  for (int d = threadIdx.x; d < D; d += blockDim.x) {
    float acc = 0;
    for (int j = 0; j <= l; ++j) acc += K[d * L + j] * bf2f(xn[(l - j) * D + d]);
    s[l * D + d] = f2bf(rbf(acc) + rbf(bf2f(xn[l * D + d]) * bf2f(dskip[d])));
  }
}
__global__ void k_g1(const u16* xn, const u16* w1, const u16* b1, u16* h) {
  int l = blockIdx.x;
  __shared__ float xs[D];
  for (int i = threadIdx.x; i < D; i += blockDim.x) xs[i] = bf2f(xn[l * D + i]);
  __syncthreads();
  for (int f = threadIdx.x; f < F; f += blockDim.x) {
    float acc = 0;
    for (int k = 0; k < D; ++k) acc += xs[k] * bf2f(w1[k * F + f]);
    float v = rbf(rbf(acc) + bf2f(b1[f]));
    v = v > 0 ? v : 0;
    h[l * F + f] = f2bf(v * v);
  }
}
__global__ void k_g2(const u16* h, const u16* w2, const u16* b2, const u16* x, const u16* s, u16* out) {
  int l = blockIdx.x;
  __shared__ float hs[F];
  for (int i = threadIdx.x; i < F; i += blockDim.x) hs[i] = bf2f(h[l * F + i]);
  __syncthreads();
  for (int d = threadIdx.x; d < D; d += blockDim.x) {
    float acc = 0;
    for (int k = 0; k < F; ++k) acc += hs[k] * bf2f(w2[k * D + d]);
    float o = rbf(rbf(acc) + bf2f(b2[d]));
    float g = rbf(o / (1.f + expf(-o)));
    out[l * D + d] = f2bf(bf2f(x[l * D + d]) + rbf(bf2f(s[l * D + d]) * g));
  }
}
extern "C" void kernel_launch(void* const* d_in, const int* in_sizes, int n_in,
                              void* d_out, int out_size, void* d_ws, size_t ws_size,
                              hipStream_t stream) {
  const u16* x = (const u16*)d_in[0];
  char* ws = (char*)d_ws;
  u16* xn = (u16*)ws; float* K = (float*)(ws + (4 << 20)); u16* s = (u16*)(ws + (12 << 20)); u16* h = (u16*)(ws + (16 << 20));
  k_ln<<<L, 64, 0, stream>>>(x, xn);
  k_kgen<<<D, 256, 0, stream>>>((const u16*)d_in[1], (const u16*)d_in[2], (const u16*)d_in[3], (const u16*)d_in[4], (const u16*)d_in[5], K);
  k_conv<<<L, 256, 0, stream>>>(xn, K, (const u16*)d_in[6], s);
  k_g1<<<L, 256, 0, stream>>>(xn, (const u16*)d_in[8], (const u16*)d_in[9], h);
  k_g2<<<L, 256, 0, stream>>>(h, (const u16*)d_in[10], (const u16*)d_in[11], x, s, (u16*)d_out);
}
```

```cpp
#include <hip/hip_runtime.h>
#include <stdint.h>

typedef unsigned short u16;
typedef unsigned u32;
typedef unsigned long long u64;
typedef float f32x2 __attribute__((ext_vector_type(2)));
typedef float f32x4 __attribute__((ext_vector_type(4)));
typedef unsigned u32x2 __attribute__((ext_vector_type(2)));
typedef unsigned u32x4 __attribute__((ext_vector_type(4)));
typedef short s16x4 __attribute__((ext_vector_type(4)));
typedef __bf16 bf16x2 __attribute__((ext_vector_type(2)));
typedef __bf16 bf16x8 __attribute__((ext_vector_type(8)));
typedef _Float16 f16x2 __attribute__((ext_vector_type(2)));
typedef _Float16 f16x8 __attribute__((ext_vector_type(8)));
#define LDSP __attribute__((address_space(3)))
#define GLB __attribute__((address_space(1)))

#define STOP_AFTER 99

constexpr int L = 2048, D = 640, N = 192, F = 1536;

constexpr u32 OFF_EPOCH = 0;
constexpr u32 OFF_F0 = 4096;
constexpr u32 OFF_FW = 8192;
constexpr u32 OFF_FH = 12288;
constexpr u32 OFF_FS = 16384;
constexpr u32 OFF_STAMP = 32768;
constexpr u32 OFF_XNF = 1u << 20;
constexpr u32 OFF_XNG = 4u << 20;
constexpr u32 OFF_W2F = 8u << 20;
constexpr u32 OFF_HF = 12u << 20;
constexpr u32 OFF_ST = 20u << 20;
constexpr u32 OFF_DBG = 24u << 20;
constexpr u32 WS_BYTES = 32u << 20;

constexpr u32 LDS_W1 = 0;
constexpr u32 LDS_STG = 98304;
constexpr u32 LDS_G = 122880;
constexpr u32 LDS_U = 0;
constexpr u32 U_STRIDE = 4640;
constexpr u32 LDS_R = 20480;
constexpr u32 R_COPY = 4224 + 64;
constexpr u32 R_STRIDE = 2 * 4352;
constexpr u32 LDS_RED = 0;
constexpr u32 LDS_S = 81920;
constexpr u32 LDS_BYTES = 163840;

struct Args {
  const u16* x; const u16* logA; const u16* aim; const u16* cre; const u16* cim; const u16* logdt; const u16* dskip;
  const u16* w1; const u16* b1; const u16* w2; const u16* b2; u16* out; char* ws;
};

#define SBAR() __builtin_amdgcn_sched_barrier(0)
#define AUX_SC1 16
typedef __amdgpu_buffer_rsrc_t rsrc_t;

__device__ __forceinline__ float bf_lo(u32 v) { return __uint_as_float(v << 16); }
__device__ __forceinline__ float bf_hi(u32 v) { return __uint_as_float(v & 0xffff0000u); }
__device__ __forceinline__ u32 pk_bf16(float a, float b) {
  return __builtin_bit_cast(u32, __builtin_convertvector(f32x2{a, b}, bf16x2));
}
__device__ __forceinline__ u32 pk_f16(float a, float b) {
  return __builtin_bit_cast(u32, __builtin_convertvector(f32x2{a, b}, f16x2));
}
__device__ __forceinline__ float rbf(float a) { return bf_lo(pk_bf16(a, a)); }

template <int CTRL> __device__ __forceinline__ float dpp(float v) {
  return __int_as_float(__builtin_amdgcn_update_dpp(0, __float_as_int(v), CTRL, 0xf, 0xf, true));
}
__device__ __forceinline__ float sum32(float v) {
  v += dpp<0xB1>(v); v += dpp<0x4E>(v); v += dpp<0x141>(v); v += dpp<0x140>(v);
  auto r = __builtin_amdgcn_permlane16_swap(__float_as_uint(v), __float_as_uint(v), false, false);
  return __uint_as_float(r[0]) + __uint_as_float(r[1]);
}
__device__ __forceinline__ float max64(float v) {
  v = fmaxf(v, dpp<0xB1>(v)); v = fmaxf(v, dpp<0x4E>(v)); v = fmaxf(v, dpp<0x141>(v)); v = fmaxf(v, dpp<0x140>(v));
  auto r = __builtin_amdgcn_permlane16_swap(__float_as_uint(v), __float_as_uint(v), false, false);
  v = fmaxf(__uint_as_float(r[0]), __uint_as_float(r[1]));
  auto t = __builtin_amdgcn_permlane32_swap(__float_as_uint(v), __float_as_uint(v), false, false);
  return fmaxf(__uint_as_float(t[0]), __uint_as_float(t[1]));
}
__device__ __forceinline__ float exp_acc(float x) {
  const float hi = 1.44269502162933349609375f, lo = 1.925963033500011079e-8f;
  float t = x * hi;
  float n = __builtin_rintf(t);
  float f = __builtin_fmaf(x, hi, -n);
  f = __builtin_fmaf(x, lo, f);
  return __builtin_ldexpf(__builtin_amdgcn_exp2f(f), (int)n);
}

__device__ __forceinline__ u32x4 ld16_sc1(rsrc_t r, u32 off) { return __builtin_amdgcn_raw_buffer_load_b128(r, off, 0, AUX_SC1); }
__device__ __forceinline__ void st16_sc1(rsrc_t r, u32 off, u32x4 v) { __builtin_amdgcn_raw_buffer_store_b128(v, r, off, 0, AUX_SC1); }
__device__ __forceinline__ void st8_sc1(rsrc_t r, u32 off, u32x2 v) { __builtin_amdgcn_raw_buffer_store_b64(v, r, off, 0, AUX_SC1); }
__device__ __forceinline__ u32 ld4_sc1(rsrc_t r, u32 off) { return __builtin_amdgcn_raw_buffer_load_b32(r, off, 0, AUX_SC1); }
__device__ __forceinline__ void st4_sc1(rsrc_t r, u32 off, u32 v) { __builtin_amdgcn_raw_buffer_store_b32(v, r, off, 0, AUX_SC1); }

__device__ __forceinline__ void glds16(u32 voff, const void* sbase, u32 lds_base) {
  u32 sv;
  asm volatile("s_mov_b32 %0, m0\n\ts_mov_b32 m0, %3\n\ts_nop 0\n\tglobal_load_lds_dwordx4 %1, %2\n\ts_mov_b32 m0, %0"
               : "=&s"(sv) : "v"(voff), "s"(sbase), "s"(lds_base) : "memory");
}
__device__ __forceinline__ void wait_vm0() { asm volatile("s_waitcnt vmcnt(0)" ::: "memory"); }
__device__ __forceinline__ void bar() { asm volatile("s_waitcnt lgkmcnt(0)\n\ts_barrier" ::: "memory"); }

__device__ __forceinline__ u32x2 lds_tr(LDSP char* p) {
  return __builtin_bit_cast(u32x2, __builtin_amdgcn_ds_read_tr16_b64_v4i16((LDSP s16x4*)p));
}
__device__ __forceinline__ f32x4 mfma_bf(u32x4 a, u32x4 b, f32x4 c) {
  return __builtin_amdgcn_mfma_f32_16x16x32_bf16(__builtin_bit_cast(bf16x8, a), __builtin_bit_cast(bf16x8, b), c, 0, 0, 0);
}
__device__ __forceinline__ f32x4 mfma_h(u32x4 a, u32x4 b, f32x4 c) {
  return __builtin_amdgcn_mfma_f32_16x16x32_f16(__builtin_bit_cast(f16x8, a), __builtin_bit_cast(f16x8, b), c, 0, 0, 0);
}

constexpr u32 SPIN_CAP = 1u << 16;
__device__ __forceinline__ void poll_eq(rsrc_t ws, u32 off, bool active, u32 tag) {
  for (u32 it = 0; it < SPIN_CAP; ++it) {
    u32 v = active ? ld4_sc1(ws, off) : tag;
    __builtin_amdgcn_fence(__ATOMIC_ACQUIRE, "wavefront");
    if (__all(v == tag)) break;
    __builtin_amdgcn_s_sleep(1);
  }
}

struct Cplx { f32x2 v, r; };
__device__ __forceinline__ Cplx mkc(f32x2 v) { Cplx c; c.v = v; c.r = f32x2{-v.y, v.x}; return c; }
__device__ __forceinline__ f32x2 cmul(f32x2 a, const Cplx& w) {
  f32x2 t = f32x2{a.y, a.y} * w.r;
  return __builtin_elementwise_fma(f32x2{a.x, a.x}, w.v, t);
}
__device__ __forceinline__ f32x2 cexp_p(float a2, float th, float p) {
  float mag = __builtin_amdgcn_exp2f(a2 * p);
  float fr = __builtin_amdgcn_fractf(th * p);
  return f32x2{mag * __builtin_amdgcn_cosf(fr), mag * __builtin_amdgcn_sinf(fr)};
}

#define STAMP(i) do { } while (0)

__global__ __launch_bounds__(256, 1) void fused_kernel(Args a) {
  __shared__ __attribute__((aligned(16))) char smem_[LDS_BYTES];
  LDSP char* sm = (LDSP char*)smem_;
  const u32 lds0 = (u32)(uintptr_t)sm;
  const int tid = threadIdx.x, lane = tid & 63;
  const int w = __builtin_amdgcn_readfirstlane(tid >> 6);
  const int b = blockIdx.x, xcd = b & 7, rk = b >> 3;
  const rsrc_t ws = __builtin_amdgcn_make_buffer_rsrc((void*)a.ws, 0, WS_BYTES, 0x00020000);
  const int g4 = lane >> 4, l16 = lane & 15;
  STAMP(0);

  const u32 epoch_v = ld4_sc1(ws, OFF_EPOCH);
  u32x2 xr[5];
  {
    const u16* xp = a.x + (size_t)(8 * b + 2 * w + (lane >> 5)) * D + (lane & 31) * 4;
#pragma unroll
    for (int i = 0; i < 5; ++i) xr[i] = *(const u32x2*)(xp + i * 128);
  }
  const bool is_conv = b < 160;
  const int ch = is_conv ? 4 * b + w : 0;
  u32 pa[4]; u32 pb[4];
  {
    const u16* src[4] = {a.logA, a.aim, a.cre, a.cim};
#pragma unroll
    for (int i = 0; i < 4; ++i) {
      pa[i] = *(const u32*)(src[i] + ch * N + 2 * lane);
      pb[i] = src[i][ch * N + 128 + lane];
    }
  }
  const u32 ldt_raw = a.logdt[ch];
  const u32 dsk_raw = a.dskip[ch];

  {
    float v[20];
#pragma unroll
    for (int i = 0; i < 5; ++i) {
      v[4 * i] = bf_lo(xr[i].x); v[4 * i + 1] = bf_hi(xr[i].x); v[4 * i + 2] = bf_lo(xr[i].y); v[4 * i + 3] = bf_hi(xr[i].y);
    }
    float s0 = 0.f, s1 = 0.f;
#pragma unroll
    for (int i = 0; i < 10; ++i) { s0 += v[2 * i]; s1 += v[2 * i + 1]; }
    const float mean = sum32(s0 + s1) * (1.0f / D);
    float q0 = 0.f, q1 = 0.f;
#pragma unroll
    for (int i = 0; i < 10; ++i) {
      float d0 = v[2 * i] - mean, d1 = v[2 * i + 1] - mean;
      q0 = __builtin_fmaf(d0, d0, q0); q1 = __builtin_fmaf(d1, d1, q1);
    }
    const float var = sum32(q0 + q1) * (1.0f / D);
    const float m = rbf(mean);
    const float sd = rbf(__builtin_amdgcn_sqrtf(rbf(rbf(var) + 1e-5f)));
    const float rs = 1.0f / sd;
    const float mrs = -m * rs;
    LDSP char* dst = sm + LDS_STG + (2 * w + (lane >> 5)) * 1312 + (lane & 31) * 8;
#pragma unroll
    for (int i = 0; i < 5; ++i) {
      u32x2 o;
      o.x = pk_bf16((v[4 * i] - m) * rs, (v[4 * i + 1] - m) * rs);
      o.y = pk_bf16((v[4 * i + 2] - m) * rs, (v[4 * i + 3] - m) * rs);
      *(LDSP u32x2*)(dst + i * 256) = o;
    }
    (void)mrs;
  }
  bar();
  const u32 tag = __builtin_amdgcn_readfirstlane(epoch_v) + 1u;
  {
#pragma unroll
    for (int i = 0; i < 3; ++i) {
      const int j = w + 4 * i;
      if (j < 10) {
        const int pidx = 8 * j + (lane >> 3), tk = lane & 7, s = pidx >> 2, kg = pidx & 3;
        LDSP char* p = sm + LDS_STG + tk * 1312 + s * 64 + kg * 8;
        u32x2 lo = *(LDSP u32x2*)p, hi = *(LDSP u32x2*)(p + 32);
        st16_sc1(ws, OFF_XNF + ((b >> 1) * 20 + s) * 1024 + kg * 256 + ((b & 1) * 8 + tk) * 16, u32x4{lo.x, lo.y, hi.x, hi.y});
      }
    }
#pragma unroll
    for (int i = 0; i < 3; ++i) {
      const int j = (3 - w) + 4 * i;
      if (j < 10) {
        LDSP char* p = sm + LDS_STG + ((lane >> 2) & 3) * 1312 + (64 * j + 16 * g4 + 4 * (lane & 3)) * 2;
        u32x2 t0 = lds_tr(p), t1 = lds_tr(p + 4 * 1312);
        u32x4 o;
        o.x = pk_f16(bf_lo(t0.x), bf_hi(t0.x)); o.y = pk_f16(bf_lo(t0.y), bf_hi(t0.y));
        o.z = pk_f16(bf_lo(t1.x), bf_hi(t1.x)); o.w = pk_f16(bf_lo(t1.y), bf_hi(t1.y));
        const int d = 64 * j + lane;
        st16_sc1(ws, OFF_XNG + (d >> 2) * 16384 + b * 64 + (d & 3) * 16, o);
      }
    }
  }
  wait_vm0();
  bar();
  if (tid == 0) st4_sc1(ws, OFF_F0 + (b >> 4) * 128 + (b & 15) * 4, tag);
  STAMP(1);

  const int tt1 = (xcd >> 1) * 4 + (rk & 3), ft1 = (xcd & 1) * 8 + (rk >> 2);
  u32 w1off[3];
#pragma unroll
  for (int t = 0; t < 3; ++t) {
    const int Q = 64 * t + lane, row = Q / 12, cpos = Q - row * 12;
    w1off[t] = row * (F * 2) + (cpos ^ (((row >> 2) & 1) << 1)) * 16;
  }
  const char* w1base = (const char*)a.w1 + ft1 * 192;
#pragma unroll
  for (int i = 0; i < 8; ++i) {
    const int G = w + 4 * i;
#pragma unroll
    for (int t = 0; t < 3; ++t) glds16(w1off[t], w1base + (size_t)G * 16 * F * 2, lds0 + LDS_W1 + G * 3072 + t * 1024);
  }

  f32x4 kacc[3];
  float yscale = 0.f;
  if (is_conv) {
    const float dt = rbf(exp_acc(bf_lo(ldt_raw)));
    float cr[3], ci[3], a2[3], th[3];
    {
      const u32 la[3] = {pa[0] << 16, pa[0] & 0xffff0000u, pb[0] << 16};
      const u32 ai[3] = {pa[1] << 16, pa[1] & 0xffff0000u, pb[1] << 16};
      const u32 c0[3] = {pa[2] << 16, pa[2] & 0xffff0000u, pb[2] << 16};
      const u32 c1[3] = {pa[3] << 16, pa[3] & 0xffff0000u, pb[3] << 16};
      float mx = 0.f;
#pragma unroll
      for (int r = 0; r < 3; ++r) {
        const float ar = -rbf(exp_acc(__uint_as_float(la[r]))) * dt;
        a2[r] = ar * 1.44269504088896341f;
        th[r] = (__uint_as_float(ai[r]) * dt) * 0.15915494309189535f;
        cr[r] = __uint_as_float(c0[r]) * dt; ci[r] = __uint_as_float(c1[r]) * dt;
        mx = fmaxf(mx, fmaxf(fabsf(cr[r]), fabsf(ci[r])));
      }
      mx = max64(mx);
      const int ec = __builtin_amdgcn_frexp_expf(mx);
#pragma unroll
      for (int r = 0; r < 3; ++r) { cr[r] = __builtin_ldexpf(cr[r], -ec); ci[r] = __builtin_ldexpf(ci[r], -ec); }
      yscale = __builtin_ldexpf(2.0f, ec);
    }
    LDSP char* stg = sm + LDS_STG + w * 16384;
    LDSP char* wr[4];
#pragma unroll
    for (int j = 0; j < 4; ++j) wr[j] = stg + lane * 64 + ((j + (lane >> 1)) & 3) * 16;
    const int tq = (lane >> 2) & 3, tp = lane & 3;
    LDSP char* rd = stg + (2 * g4 + (tq >> 1)) * 64 + ((2 * (tq & 1) + (tp >> 1) + g4) & 3) * 16 + (tp & 1) * 8;
#pragma unroll
    for (int t = 0; t < 3; ++t) kacc[t] = f32x4{0.f, 0.f, 0.f, 0.f};
#pragma unroll 1
    for (int r = 0; r < 3; ++r) {
      const float a2r = r == 0 ? a2[0] : (r == 1 ? a2[1] : a2[2]);
      const float thr = r == 0 ? th[0] : (r == 1 ? th[1] : th[2]);
      const float crr = r == 0 ? cr[0] : (r == 1 ? cr[1] : cr[2]);
      const float cir = r == 0 ? ci[0] : (r == 1 ? ci[1] : ci[2]);
      const Cplx w1 = mkc(cexp_p(a2r, thr, 1.f)), w8 = mkc(cexp_p(a2r, thr, 8.f)), w128 = mkc(cexp_p(a2r, thr, 128.f));
      auto put = [&](int tile, int half, const f32x2* e) {
        u32x4 re, im;
        re.x = pk_f16(e[0].x, e[1].x); re.y = pk_f16(e[2].x, e[3].x); re.z = pk_f16(e[4].x, e[5].x); re.w = pk_f16(e[6].x, e[7].x);
        im.x = pk_f16(e[0].y, e[1].y); im.y = pk_f16(e[2].y, e[3].y); im.z = pk_f16(e[4].y, e[5].y); im.w = pk_f16(e[6].y, e[7].y);
        *(LDSP u32x4*)(wr[half] + tile * 4096) = re;
        *(LDSP u32x4*)(wr[2 + half] + tile * 4096) = im;
      };
      f32x2 e[8];
      {
        const Cplx w8c = mkc(f32x2{w8.v.x, -w8.v.y});
        e[0] = f32x2{1.f, 0.f}; e[1] = w8c.v;
#pragma unroll
        for (int i = 2; i < 8; ++i) e[i] = cmul(e[i - 1], w8c);
        put(3, 0, e);
        e[0] = cmul(e[7], w8c);
#pragma unroll
        for (int i = 1; i < 8; ++i) e[i] = cmul(e[i - 1], w8c);
        put(3, 1, e);
      }
      const f32x2 x0 = f32x2{crr, cir};
      e[0] = x0;
#pragma unroll
      for (int i = 1; i < 8; ++i) e[i] = cmul(e[i - 1], w1);
      put(0, 0, e);
      const f32x2 x128 = cmul(x0, w128);
      e[0] = x128;
#pragma unroll
      for (int i = 1; i < 8; ++i) e[i] = cmul(e[i - 1], w1);
      put(0, 1, e);
      const Cplx w2 = mkc(cmul(w1.v, w1));
      const Cplx w4 = mkc(cmul(w2.v, w2));
      const f32x2 x256 = cmul(x128, w128), x384 = cmul(x256, w128);
      e[0] = x256; e[4] = x384;
#pragma unroll
      for (int i = 1; i < 4; ++i) { e[i] = cmul(e[i - 1], w2); e[4 + i] = cmul(e[3 + i], w2); }
      put(1, 0, e);
      e[0] = cmul(x384, w128); e[2] = cmul(e[0], w128); e[4] = cmul(e[2], w128); e[6] = cmul(e[4], w128);
      e[1] = cmul(e[0], w4); e[3] = cmul(e[2], w4); e[5] = cmul(e[4], w4); e[7] = cmul(e[6], w4);
      const f32x2 x896 = e[6];
      put(1, 1, e);
      e[0] = cmul(x896, w128);
#pragma unroll
      for (int i = 1; i < 8; ++i) e[i] = cmul(e[i - 1], w128);
      const f32x2 x2048 = cmul(e[7], w128);
      put(2, 0, e);
      *(LDSP u32x4*)(wr[1] + 2 * 4096) = u32x4{pk_f16(x2048.x, 0.f), 0u, 0u, 0u};
      *(LDSP u32x4*)(wr[3] + 2 * 4096) = u32x4{pk_f16(x2048.y, 0.f), 0u, 0u, 0u};
#pragma unroll
      for (int ks = 0; ks < 4; ++ks) {
        u32x4 fr[4];
#pragma unroll
        for (int t = 0; t < 4; ++t) {
          u32x2 lo = lds_tr(rd + t * 4096 + ks * 1024), hi = lds_tr(rd + t * 4096 + ks * 1024 + 512);
          fr[t] = u32x4{lo.x, lo.y, hi.x, hi.y};
        }
#pragma unroll
        for (int t = 0; t < 3; ++t) kacc[t] = mfma_h(fr[t], fr[3], kacc[t]);
      }
    }
  } else if (b < 220) {
    const int pw = (b - 160) * 4 + w, s = pw / 5, pc = pw - s * 5;
    const u32 stg = LDS_STG + w * 16384;
#pragma unroll
    for (int t = 0; t < 8; ++t) {
      const int row = 4 * t + g4;
      glds16(row * (D * 2) + ((l16 ^ (2 * (row & 7))) * 16), (const char*)a.w2 + (size_t)s * 32 * D * 2 + pc * 256, lds0 + stg + t * 1024);
    }
    wait_vm0();
    const int tq = (lane >> 2) & 3, tp = lane & 3, row = 4 * g4 + tq;
#pragma unroll
    for (int c = 0; c < 8; ++c) {
      LDSP char* p = sm + stg + row * 256 + (((2 * c + (tp >> 1)) ^ (2 * (row & 7))) * 16) + (tp & 1) * 8;
      u32x2 lo = lds_tr(p), hi = lds_tr(p + 16 * 256);
      st16_sc1(ws, OFF_W2F + ((pc * 8 + c) * 48 + s) * 1024 + lane * 16, u32x4{lo.x, lo.y, hi.x, hi.y});
    }
    wait_vm0();
  }
  bar();
  if (!is_conv && b < 220 && tid == 0) st4_sc1(ws, OFF_FW + (b - 160) * 4, tag);
  STAMP(2);
#pragma unroll
  for (int i = 8; i < 10; ++i) {
    const int G = w + 4 * i;
#pragma unroll
    for (int t = 0; t < 3; ++t) glds16(w1off[t], w1base + (size_t)G * 16 * F * 2, lds0 + LDS_W1 + G * 3072 + t * 1024);
  }
  if (is_conv) {
    float mx = 0.f;
#pragma unroll
    for (int t = 0; t < 3; ++t)
#pragma unroll
      for (int i = 0; i < 4; ++i) mx = fmaxf(mx, fabsf(kacc[t][i]));
    mx = max64(mx);
    const int ef = __builtin_amdgcn_frexp_expf(mx);
    yscale = __builtin_ldexpf(yscale, ef - 14);
#pragma unroll
    for (int t = 0; t < 3; ++t)
#pragma unroll
      for (int i = 0; i < 4; ++i) kacc[t][i] = __builtin_ldexpf(kacc[t][i], 14 - ef);
    LDSP char* G = sm + LDS_G + w * 1536;
    *(LDSP u32x2*)(G + 2 * (8 * l16 + 4 * (g4 & 1) + 128 * (g4 >> 1))) = u32x2{pk_f16(kacc[0][0], kacc[0][1]), pk_f16(kacc[0][2], kacc[0][3])};
    const int i0 = g4 < 2 ? 256 + 64 * g4 + 4 * l16 : 384 + 64 * (g4 - 2) + 2 * l16;
    const int i1 = g4 < 2 ? i0 + 2 : i0 + 32;
    *(LDSP u32*)(G + 2 * i0) = pk_f16(kacc[1][0], kacc[1][1]);
    *(LDSP u32*)(G + 2 * i1) = pk_f16(kacc[1][2], kacc[1][3]);
    const u32 p01 = pk_f16(kacc[2][0], kacc[2][1]), p23 = pk_f16(kacc[2][2], kacc[2][3]);
    LDSP u16* g2 = (LDSP u16*)(G + 2 * (512 + 64 * g4 + l16));
    g2[0] = (u16)p01; g2[16] = (u16)(p01 >> 16); g2[32] = (u16)p23; g2[48] = (u16)(p23 >> 16);
  }

  wait_vm0();
  if (w == 0) poll_eq(ws, OFF_F0 + tt1 * 128 + (lane & 15) * 4, true, tag);
  bar();
  STAMP(3);
  {
    u32x4 xf[2][20];
#pragma unroll
    for (int s = 0; s < 20; ++s)
#pragma unroll
      for (int t = 0; t < 2; ++t) xf[t][s] = ld16_sc1(ws, OFF_XNF + ((tt1 * 8 + 2 * w + t) * 20 + s) * 1024 + lane * 16);
    u32x2 bias[6];
#pragma unroll
    for (int f = 0; f < 6; ++f) bias[f] = *(const u32x2*)(a.b1 + ft1 * 96 + 16 * f + 4 * g4);
    f32x4 acc[2][6];
#pragma unroll
    for (int f = 0; f < 6; ++f) {
      acc[0][f] = f32x4{0.f, 0.f, 0.f, 0.f}; acc[1][f] = acc[0][f];
    }
    const int tq = (lane >> 2) & 3, tp = lane & 3;
    LDSP char* aE = sm + LDS_W1 + (4 * g4 + tq) * 192 + tp * 8 + 32 * (g4 & 1);
    LDSP char* aO = sm + LDS_W1 + (4 * g4 + tq) * 192 + tp * 8 + 32 * (1 - (g4 & 1));
#pragma unroll
    for (int s = 0; s < 20; ++s) {
#pragma unroll
      for (int f = 0; f < 6; ++f) {
        LDSP char* p = ((f & 1) ? aO : aE) + s * 6144 + (f >> 1) * 64;
        u32x2 lo = lds_tr(p), hi = lds_tr(p + 3072);
        const u32x4 af = u32x4{lo.x, lo.y, hi.x, hi.y};
        acc[0][f] = mfma_bf(af, xf[0][s], acc[0][f]);
        acc[1][f] = mfma_bf(af, xf[1][s], acc[1][f]);
      }
    }
    STAMP(4);
#pragma unroll
    for (int t = 0; t < 2; ++t)
#pragma unroll
      for (int sp = 0; sp < 3; ++sp) {
        u32x4 o;
#pragma unroll
        for (int hh = 0; hh < 2; ++hh) {
          const int f = 2 * sp + hh;
          float v0 = acc[t][f][0] + bf_lo(bias[f].x), v1 = acc[t][f][1] + bf_hi(bias[f].x);
          float v2 = acc[t][f][2] + bf_lo(bias[f].y), v3 = acc[t][f][3] + bf_hi(bias[f].y);
          v0 *= fmaxf(v0, 0.f); v1 *= fmaxf(v1, 0.f); v2 *= fmaxf(v2, 0.f); v3 *= fmaxf(v3, 0.f);
          o[2 * hh] = pk_bf16(v0, v1); o[2 * hh + 1] = pk_bf16(v2, v3);
        }
        st16_sc1(ws, OFF_HF + ((tt1 * 8 + 2 * w + t) * 48 + ft1 * 3 + sp) * 1024 + lane * 16, o);
      }
  }
  wait_vm0();
  bar();
  if (tid == 0) st4_sc1(ws, OFF_FH + tt1 * 128 + ft1 * 4, tag);
  STAMP(5);

  if (w == 0) {
    poll_eq(ws, OFF_F0 + (lane >> 2) * 128 + (lane & 3) * 16, true, tag);
    poll_eq(ws, OFF_F0 + (lane >> 2) * 128 + (lane & 3) * 16 + 4, true, tag);
    poll_eq(ws, OFF_F0 + (lane >> 2) * 128 + (lane & 3) * 16 + 8, true, tag);
    poll_eq(ws, OFF_F0 + (lane >> 2) * 128 + (lane & 3) * 16 + 12, true, tag);
    poll_eq(ws, OFF_FW + lane * 4, lane < 60, tag);
    if (b == 255 && lane == 0) st4_sc1(ws, OFF_EPOCH, tag);
  }
  bar();
  STAMP(6);
  const int t2 = xcd * 4 + (rk & 3), j2 = rk >> 2;
  u32x4 w2f[12][5];
#pragma unroll
  for (int s = 0; s < 12; ++s)
#pragma unroll
    for (int c = 0; c < 5; ++c) w2f[s][c] = ld16_sc1(ws, OFF_W2F + ((j2 * 5 + c) * 48 + 12 * w + s) * 1024 + lane * 16);

  if (is_conv) {
    {
      u32x4 ur[4];
#pragma unroll
      for (int j = 0; j < 4; ++j) ur[j] = ld16_sc1(ws, OFF_XNG + b * 16384 + (tid + 256 * j) * 16);
      if (lane < 32) *(LDSP u32x4*)(sm + LDS_U + w * U_STRIDE + lane * 16) = u32x4{0u, 0u, 0u, 0u};
#pragma unroll
      for (int j = 0; j < 4; ++j) {
        const int p = tid + 256 * j;
        *(LDSP u32x4*)(sm + LDS_U + (p & 3) * U_STRIDE + 512 + (p >> 2) * 16) = ur[j];
      }
    }
    {
      LDSP char* G = sm + LDS_G + w * 1536;
      LDSP char* R = sm + LDS_R + w * R_STRIDE;
      auto putR = [&](int B, u32x4 r0, u32x4 r1) {
        const int t = (2056 - B) >> 3;
        *(LDSP u32x4*)(R + t * 16) = r0;
        *(LDSP u32x4*)(R + R_COPY + t * 16) = r1;
      };
      auto perm = [](u32 hi, u32 lo, u32 sel) { return __builtin_amdgcn_perm(hi, lo, sel); };
      constexpr u32 L0 = 0x0100, L1 = 0x0302, H0 = 0x0504, H1 = 0x0706;
      #define SEL(a_, b_) ((a_) | ((b_) << 16))
      if (lane <= 32) {
        const int B = 8 * lane - 8;
        u32x4 d; u32 d4;
        if (B >= 0) { d = *(LDSP u32x4*)(G + 2 * B); } else { d = u32x4{0u, 0u, 0u, 0u}; }
        d4 = *(LDSP u32*)(G + 2 * (B + 8));
        if (B == 248) d4 = perm(d4, d4, SEL(L0, L0));
        u32x4 r0, r1;
        r0.x = perm(d4, d.w, SEL(H0, L1)); r0.y = perm(d.w, d.z, SEL(H0, L1)); r0.z = perm(d.z, d.y, SEL(H0, L1)); r0.w = perm(d.y, d.x, SEL(H0, L1));
        r1.x = perm(d4, d4, SEL(L1, L0)); r1.y = perm(d.w, d.w, SEL(L1, L0)); r1.z = perm(d.z, d.z, SEL(L1, L0)); r1.w = perm(d.y, d.y, SEL(L1, L0));
        putR(B, r0, r1);
      } else if (lane == 33) {
        putR(-16, u32x4{0u, 0u, 0u, 0u}, u32x4{0u, 0u, 0u, 0u});
      }
      if (lane < 32) {
        const int B = 256 + 8 * lane;
        const u32x2 d = *(LDSP u32x2*)(G + 2 * (256 + 4 * lane));
        const u32 d2 = *(LDSP u32*)(G + 2 * (256 + 4 * lane + 4));
        u32x4 r0, r1;
        r0.x = perm(d2, d2, SEL(L0, L0)); r0.y = perm(d.y, d.y, SEL(L1, L0)); r0.z = perm(d.y, d.y, SEL(L0, L0)); r0.w = perm(d.x, d.x, SEL(L1, L0));
        r1.x = r0.x; r1.y = perm(d2, d.y, SEL(H0, L1)); r1.z = r0.z; r1.w = perm(d.y, d.x, SEL(H0, L1));
        putR(B, r0, r1);
      }
      {
        const int B = 512 + 8 * lane;
        const u32 d0 = *(LDSP u32*)(G + 2 * (384 + 2 * lane));
        const u32 d1 = *(LDSP u32*)(G + 2 * (384 + 2 * lane + 2));
        u32x4 r0, r1;
        r0.x = perm(d1, d1, SEL(L0, L0)); r0.y = perm(d1, d0, SEL(H0, L1)); r0.z = perm(d0, d0, SEL(L1, L1)); r0.w = perm(d0, d0, SEL(L0, L0));
        r1.x = r0.x; r1.y = r0.x; r1.z = r0.z; r1.w = perm(d0, d0, SEL(L1, L0));
        putR(B, r0, r1);
      }
#pragma unroll
      for (int k = 0; k < 2; ++k) {
        const int idx = 64 * k + lane, B = 1024 + 8 * idx;
        const LDSP u16* gp = (const LDSP u16*)(G + 2 * (512 + idx));
        const u32 v0 = gp[0], v1 = gp[1];
        const u32 v00 = v0 | (v0 << 16), v11 = v1 | (v1 << 16), v10 = v1 | (v0 << 16);
        const bool odd = idx & 1;
        u32x4 r0, r1;
        r0.x = v11; r0.y = v11; r0.z = odd ? v10 : v00; r0.w = v00;
        r1.x = v11; r1.y = v11; r1.z = odd ? v11 : v10; r1.w = v00;
        putR(B, r0, r1);
      }
    }
    bar();
    STAMP(7);
    f32x4 cacc[8];
#pragma unroll
    for (int P = 0; P < 8; ++P) cacc[P] = f32x4{0.f, 0.f, 0.f, 0.f};
    {
      LDSP char* ra = sm + LDS_R + w * R_STRIDE + (l16 & 1) * R_COPY + 4 * (1024 - (l16 >> 1) + 4 * g4);
      LDSP char* ub = sm + LDS_U + w * U_STRIDE + 2 * (240 + 16 * l16 + 8 * g4);
#pragma unroll 1
      for (int r = 0; r < 8; ++r) {
        u32x4 af[8], bfr[8];
#pragma unroll
        for (int i = 0; i < 8; ++i) {
          const LDSP u32* p = (const LDSP u32*)(ra - 512 * i - 64 * r);
          af[i] = u32x4{p[0], p[1], p[2], p[3]};
          bfr[i] = *(LDSP u32x4*)(ub + 512 * i - 64 * r);
        }
#pragma unroll
        for (int P = 0; P < 8; ++P)
#pragma unroll
          for (int i = 0; i <= P; ++i) cacc[P] = mfma_h(af[i], bfr[P - i], cacc[P]);
      }
    }
    STAMP(8);
    {
      const float dsk = bf_lo(dsk_raw);
#pragma unroll
      for (int P = 0; P < 8; ++P) {
        const int tok = 256 * P + 16 * l16 + 4 * g4;
        const u32x2 uu = *(LDSP u32x2*)(sm + LDS_U + w * U_STRIDE + 512 + tok * 2);
        const u32 uux = uu.x, uuy = uu.y;
        const f16x2 u01 = __builtin_bit_cast(f16x2, uux), u23 = __builtin_bit_cast(f16x2, uuy);
        const float s0 = __builtin_fmaf((float)u01.x, dsk, cacc[P][0] * yscale), s1 = __builtin_fmaf((float)u01.y, dsk, cacc[P][1] * yscale);
        const float s2 = __builtin_fmaf((float)u23.x, dsk, cacc[P][2] * yscale), s3 = __builtin_fmaf((float)u23.y, dsk, cacc[P][3] * yscale);
        st8_sc1(ws, OFF_ST + ch * 4096 + tok * 2, u32x2{pk_bf16(s0, s1), pk_bf16(s2, s3)});
      }
    }
    wait_vm0();
    bar();
    if (tid == 0) st4_sc1(ws, OFF_FS + (b / 20) * 128 + (b % 20) * 4, tag);
  }
  STAMP(9);

  if (w == 0) poll_eq(ws, OFF_FH + (t2 >> 1) * 128 + (lane & 15) * 4, true, tag);
  bar();
  STAMP(10);
  f32x4 oacc[4][5];
  {
    u32x4 hf[4][12];
#pragma unroll
    for (int t = 0; t < 4; ++t)
#pragma unroll
      for (int s = 0; s < 12; ++s) hf[t][s] = ld16_sc1(ws, OFF_HF + ((t2 * 4 + t) * 48 + 12 * w + s) * 1024 + lane * 16);
#pragma unroll
    for (int t = 0; t < 4; ++t)
#pragma unroll
      for (int c = 0; c < 5; ++c) oacc[t][c] = f32x4{0.f, 0.f, 0.f, 0.f};
#pragma unroll
    for (int t = 0; t < 4; ++t)
#pragma unroll
      for (int s = 0; s < 12; ++s)
#pragma unroll
        for (int c = 0; c < 5; ++c) oacc[t][c] = mfma_bf(w2f[s][c], hf[t][s], oacc[t][c]);
  }
  STAMP(11);
  {
#pragma unroll
    for (int t = 0; t < 4; ++t)
#pragma unroll
      for (int c = 0; c < 5; ++c) *(LDSP f32x4*)(sm + LDS_RED + ((w * 4 + t) * 5 + c) * 1024 + lane * 16) = oacc[t][c];
    u32x2 xo[5], b2v[5];
    const int tok = t2 * 64 + w * 16 + l16;
#pragma unroll
    for (int c = 0; c < 5; ++c) {
      xo[c] = *(const u32x2*)(a.x + (size_t)tok * D + j2 * 80 + 16 * c + 4 * g4);
      b2v[c] = *(const u32x2*)(a.b2 + j2 * 80 + 16 * c + 4 * g4);
    }
    if (w == 0) poll_eq(ws, OFF_FS + j2 * 128 + (lane < 20 ? lane : 0) * 4, true, tag);
    bar();
    STAMP(12);
#pragma unroll
    for (int i = 0; i < 3; ++i) {
      const int p = tid + 256 * i;
      if (p < 640) {
        const u32x4 v = ld16_sc1(ws, OFF_ST + (j2 * 80 + (p >> 3)) * 4096 + t2 * 128 + (p & 7) * 16);
        *(LDSP u32x4*)(sm + LDS_S + (p >> 3) * 160 + (p & 7) * 16) = v;
      }
    }
    bar();
    const int tq = (lane >> 2) & 3, tp = lane & 3;
#pragma unroll
    for (int c = 0; c < 5; ++c) {
      f32x4 o = *(LDSP f32x4*)(sm + LDS_RED + ((0 * 4 + w) * 5 + c) * 1024 + lane * 16);
#pragma unroll
      for (int src = 1; src < 4; ++src) o += *(LDSP f32x4*)(sm + LDS_RED + ((src * 4 + w) * 5 + c) * 1024 + lane * 16);
      const u32x2 sv = lds_tr(sm + LDS_S + (16 * c + 4 * g4 + tq) * 160 + w * 32 + tp * 8);
      const float bb[4] = {bf_lo(b2v[c].x), bf_hi(b2v[c].x), bf_lo(b2v[c].y), bf_hi(b2v[c].y)};
      const float ss[4] = {bf_lo(sv.x), bf_hi(sv.x), bf_lo(sv.y), bf_hi(sv.y)};
      const float xx[4] = {bf_lo(xo[c].x), bf_hi(xo[c].x), bf_lo(xo[c].y), bf_hi(xo[c].y)};
      float r[4];
#pragma unroll
      for (int i = 0; i < 4; ++i) {
        const float z = o[i] + bb[i];
        const float g = z * __builtin_amdgcn_rcpf(1.0f + __builtin_amdgcn_exp2f(z * -1.44269504088896341f));
        r[i] = __builtin_fmaf(ss[i], g, xx[i]);
      }
      *(u32x2*)(a.out + (size_t)tok * D + j2 * 80 + 16 * c + 4 * g4) = u32x2{pk_bf16(r[0], r[1]), pk_bf16(r[2], r[3])};
    }
  }
  STAMP(13);
}

extern "C" void kernel_launch(void* const* d_in, const int* in_sizes, int n_in, void* d_out, int out_size, void* d_ws,
                              size_t ws_size, hipStream_t stream) {
  Args a;
  a.x = (const u16*)d_in[0]; a.logA = (const u16*)d_in[1]; a.aim = (const u16*)d_in[2]; a.cre = (const u16*)d_in[3];
  a.cim = (const u16*)d_in[4]; a.logdt = (const u16*)d_in[5]; a.dskip = (const u16*)d_in[6];
  a.w1 = (const u16*)d_in[8]; a.b1 = (const u16*)d_in[9]; a.w2 = (const u16*)d_in[10]; a.b2 = (const u16*)d_in[11];
  a.out = (u16*)d_out; a.ws = (char*)d_ws;
  fused_kernel<<<256, 256, 0, stream>>>(a);
}
```

```cpp
#define STAMPS
#define PROBE 0
#define PROBE_BLK 0
#include <hip/hip_runtime.h>
#include <stdint.h>

typedef unsigned short u16;
typedef unsigned u32;
typedef unsigned long long u64;
typedef float f32x2 __attribute__((ext_vector_type(2)));
typedef float f32x4 __attribute__((ext_vector_type(4)));
typedef unsigned u32x2 __attribute__((ext_vector_type(2)));
typedef unsigned u32x4 __attribute__((ext_vector_type(4)));
typedef short s16x4 __attribute__((ext_vector_type(4)));
typedef __bf16 bf16x2 __attribute__((ext_vector_type(2)));
typedef __bf16 bf16x8 __attribute__((ext_vector_type(8)));
typedef _Float16 f16x2 __attribute__((ext_vector_type(2)));
typedef _Float16 f16x8 __attribute__((ext_vector_type(8)));
#define LDSP __attribute__((address_space(3)))
#define GLB __attribute__((address_space(1)))

#define STOP_AFTER 99

constexpr int L = 2048, D = 640, N = 192, F = 1536;

constexpr u32 OFF_EPOCH = 0;
constexpr u32 OFF_F0 = 4096;
constexpr u32 OFF_FW = 8192;
constexpr u32 OFF_FH = 12288;
constexpr u32 OFF_FS = 16384;
constexpr u32 OFF_STAMP = 32768;
constexpr u32 OFF_XNF = 1u << 20;
constexpr u32 OFF_XNG = 4u << 20;
constexpr u32 OFF_W2F = 8u << 20;
constexpr u32 OFF_HF = 12u << 20;
constexpr u32 OFF_ST = 20u << 20;
constexpr u32 OFF_DBG = 24u << 20;
constexpr u32 WS_BYTES = 32u << 20;

constexpr u32 LDS_W1 = 0;
constexpr u32 LDS_STG = 98304;
constexpr u32 LDS_G = 122880;
constexpr u32 LDS_U = 0;
constexpr u32 U_STRIDE = 4640;
constexpr u32 LDS_R = 20480;
constexpr u32 R_COPY = 4224 + 64;
constexpr u32 R_STRIDE = 2 * 4352;
constexpr u32 LDS_RED = 0;
constexpr u32 LDS_S = 81920;
constexpr u32 LDS_BYTES = 163840;

struct Args {
  const u16* x; const u16* logA; const u16* aim; const u16* cre; const u16* cim; const u16* logdt; const u16* dskip;
  const u16* w1; const u16* b1; const u16* w2; const u16* b2; u16* out; char* ws;
};

#define SBAR() __builtin_amdgcn_sched_barrier(0)
#define AUX_SC1 16
typedef __amdgpu_buffer_rsrc_t rsrc_t;

__device__ __forceinline__ float bf_lo(u32 v) { return __uint_as_float(v << 16); }
__device__ __forceinline__ float bf_hi(u32 v) { return __uint_as_float(v & 0xffff0000u); }
__device__ __forceinline__ u32 pk_bf16(float a, float b) {
  return __builtin_bit_cast(u32, __builtin_convertvector(f32x2{a, b}, bf16x2));
}
__device__ __forceinline__ u32 pk_f16(float a, float b) {
  return __builtin_bit_cast(u32, __builtin_convertvector(f32x2{a, b}, f16x2));
}
__device__ __forceinline__ float rbf(float a) { return bf_lo(pk_bf16(a, a)); }

template <int CTRL> __device__ __forceinline__ float dpp(float v) {
  return __int_as_float(__builtin_amdgcn_update_dpp(0, __float_as_int(v), CTRL, 0xf, 0xf, true));
}
__device__ __forceinline__ float sum32(float v) {
  v += dpp<0xB1>(v); v += dpp<0x4E>(v); v += dpp<0x141>(v); v += dpp<0x140>(v);
  auto r = __builtin_amdgcn_permlane16_swap(__float_as_uint(v), __float_as_uint(v), false, false);
  return __uint_as_float(r[0]) + __uint_as_float(r[1]);
}
__device__ __forceinline__ float max64(float v) {
  v = fmaxf(v, dpp<0xB1>(v)); v = fmaxf(v, dpp<0x4E>(v)); v = fmaxf(v, dpp<0x141>(v)); v = fmaxf(v, dpp<0x140>(v));
  auto r = __builtin_amdgcn_permlane16_swap(__float_as_uint(v), __float_as_uint(v), false, false);
  v = fmaxf(__uint_as_float(r[0]), __uint_as_float(r[1]));
  auto t = __builtin_amdgcn_permlane32_swap(__float_as_uint(v), __float_as_uint(v), false, false);
  return fmaxf(__uint_as_float(t[0]), __uint_as_float(t[1]));
}
__device__ __forceinline__ float exp_acc(float x) {
  const float hi = 1.44269502162933349609375f, lo = 1.925963033500011079e-8f;
  float t = x * hi;
  float n = __builtin_rintf(t);
  float f = __builtin_fmaf(x, hi, -n);
  f = __builtin_fmaf(x, lo, f);
  return __builtin_ldexpf(__builtin_amdgcn_exp2f(f), (int)n);
}

__device__ __forceinline__ u32x4 ld16_sc1(rsrc_t r, u32 off) { return __builtin_amdgcn_raw_buffer_load_b128(r, off, 0, AUX_SC1); }
__device__ __forceinline__ void st16_sc1(rsrc_t r, u32 off, u32x4 v) { __builtin_amdgcn_raw_buffer_store_b128(v, r, off, 0, AUX_SC1); }
__device__ __forceinline__ void st8_sc1(rsrc_t r, u32 off, u32x2 v) { __builtin_amdgcn_raw_buffer_store_b64(v, r, off, 0, AUX_SC1); }
__device__ __forceinline__ u32 ld4_sc1(rsrc_t r, u32 off) { return __builtin_amdgcn_raw_buffer_load_b32(r, off, 0, AUX_SC1); }
__device__ __forceinline__ void st4_sc1(rsrc_t r, u32 off, u32 v) { __builtin_amdgcn_raw_buffer_store_b32(v, r, off, 0, AUX_SC1); }

__device__ __forceinline__ void glds16(u32 voff, const void* sbase, u32 lds_base) {
  u32 sv;
  asm volatile("s_mov_b32 %0, m0\n\ts_mov_b32 m0, %3\n\ts_nop 0\n\tglobal_load_lds_dwordx4 %1, %2\n\ts_mov_b32 m0, %0"
               : "=&s"(sv) : "v"(voff), "s"(sbase), "s"(lds_base) : "memory");
}
__device__ __forceinline__ void wait_vm0() { asm volatile("s_waitcnt vmcnt(0)" ::: "memory"); }
__device__ __forceinline__ void bar() { asm volatile("s_waitcnt lgkmcnt(0)\n\ts_barrier" ::: "memory"); }

__device__ __forceinline__ u32x2 lds_tr(LDSP char* p) {
  return __builtin_bit_cast(u32x2, __builtin_amdgcn_ds_read_tr16_b64_v4i16((LDSP s16x4*)p));
}
__device__ __forceinline__ f32x4 mfma_bf(u32x4 a, u32x4 b, f32x4 c) {
  return __builtin_amdgcn_mfma_f32_16x16x32_bf16(__builtin_bit_cast(bf16x8, a), __builtin_bit_cast(bf16x8, b), c, 0, 0, 0);
}
__device__ __forceinline__ f32x4 mfma_h(u32x4 a, u32x4 b, f32x4 c) {
  return __builtin_amdgcn_mfma_f32_16x16x32_f16(__builtin_bit_cast(f16x8, a), __builtin_bit_cast(f16x8, b), c, 0, 0, 0);
}

constexpr u32 SPIN_CAP = 1u << 16;
__device__ __forceinline__ void poll_eq(rsrc_t ws, u32 off, bool active, u32 tag) {
  for (u32 it = 0; it < SPIN_CAP; ++it) {
    u32 v = active ? ld4_sc1(ws, off) : tag;
    __builtin_amdgcn_fence(__ATOMIC_ACQUIRE, "wavefront");
    if (__all(v == tag)) break;
    __builtin_amdgcn_s_sleep(1);
  }
}

struct Cplx { f32x2 v, r; };
__device__ __forceinline__ Cplx mkc(f32x2 v) { Cplx c; c.v = v; c.r = f32x2{-v.y, v.x}; return c; }
__device__ __forceinline__ f32x2 cmul(f32x2 a, const Cplx& w) {
  f32x2 t = f32x2{a.y, a.y} * w.r;
  return __builtin_elementwise_fma(f32x2{a.x, a.x}, w.v, t);
}
__device__ __forceinline__ f32x2 cexp_p(float a2, float th, float p) {
  float mag = __builtin_amdgcn_exp2f(a2 * p);
  float fr = __builtin_amdgcn_fractf(th * p);
  return f32x2{mag * __builtin_amdgcn_cosf(fr), mag * __builtin_amdgcn_sinf(fr)};
}

#define STAMP(i) do { stamp[i] = __builtin_amdgcn_s_memrealtime(); } while (0)

__global__ __launch_bounds__(256, 1) void fused_kernel(Args a) {
  __shared__ __attribute__((aligned(16))) char smem_[LDS_BYTES];
  LDSP char* sm = (LDSP char*)smem_;
  const u32 lds0 = (u32)(uintptr_t)sm;
  const int tid = threadIdx.x, lane = tid & 63;
  const int w = __builtin_amdgcn_readfirstlane(tid >> 6);
  const int b = blockIdx.x, xcd = b & 7, rk = b >> 3;
  const rsrc_t ws = __builtin_amdgcn_make_buffer_rsrc((void*)a.ws, 0, WS_BYTES, 0x00020000);
  const int g4 = lane >> 4, l16 = lane & 15;
  u64 stamp[16];
#pragma unroll
  for (int i = 0; i < 16; ++i) stamp[i] = 0;
  STAMP(0);
  stamp[14] = __builtin_amdgcn_s_memtime();

  const u32 epoch_v = ld4_sc1(ws, OFF_EPOCH);
  u32x2 xr[5];
  {
    const u16* xp = a.x + (size_t)(8 * b + 2 * w + (lane >> 5)) * D + (lane & 31) * 4;
#pragma unroll
    for (int i = 0; i < 5; ++i) xr[i] = *(const u32x2*)(xp + i * 128);
  }
  const bool is_conv = b < 160;
  const int ch = is_conv ? 4 * b + w : 0;
  u32 pa[4]; u32 pb[4];
  {
    const u16* src[4] = {a.logA, a.aim, a.cre, a.cim};
#pragma unroll
    for (int i = 0; i < 4; ++i) {
      pa[i] = *(const u32*)(src[i] + ch * N + 2 * lane);
      pb[i] = src[i][ch * N + 128 + lane];
    }
  }
  const u32 ldt_raw = a.logdt[ch];
  const u32 dsk_raw = a.dskip[ch];

  {
    float v[20];
#pragma unroll
    for (int i = 0; i < 5; ++i) {
      v[4 * i] = bf_lo(xr[i].x); v[4 * i + 1] = bf_hi(xr[i].x); v[4 * i + 2] = bf_lo(xr[i].y); v[4 * i + 3] = bf_hi(xr[i].y);
    }
    float s0 = 0.f, s1 = 0.f;
#pragma unroll
    for (int i = 0; i < 10; ++i) { s0 += v[2 * i]; s1 += v[2 * i + 1]; }
    const float mean = sum32(s0 + s1) * (1.0f / D);
    float q0 = 0.f, q1 = 0.f;
#pragma unroll
    for (int i = 0; i < 10; ++i) {
      float d0 = v[2 * i] - mean, d1 = v[2 * i + 1] - mean;
      q0 = __builtin_fmaf(d0, d0, q0); q1 = __builtin_fmaf(d1, d1, q1);
    }
    const float var = sum32(q0 + q1) * (1.0f / D);
    const float m = rbf(mean);
    const float sd = rbf(__builtin_amdgcn_sqrtf(rbf(rbf(var) + 1e-5f)));
    const float rs = 1.0f / sd;
    const float mrs = -m * rs;
    LDSP char* dst = sm + LDS_STG + (2 * w + (lane >> 5)) * 1312 + (lane & 31) * 8;
#pragma unroll
    for (int i = 0; i < 5; ++i) {
      u32x2 o;
      o.x = pk_bf16((v[4 * i] - m) * rs, (v[4 * i + 1] - m) * rs);
      o.y = pk_bf16((v[4 * i + 2] - m) * rs, (v[4 * i + 3] - m) * rs);
      *(LDSP u32x2*)(dst + i * 256) = o;
    }
    (void)mrs;
  }
  bar();
  const u32 tag = __builtin_amdgcn_readfirstlane(epoch_v) + 1u;
  {
#pragma unroll
    for (int i = 0; i < 3; ++i) {
      const int j = w + 4 * i;
      if (j < 10) {
        const int pidx = 8 * j + (lane >> 3), tk = lane & 7, s = pidx >> 2, kg = pidx & 3;
        LDSP char* p = sm + LDS_STG + tk * 1312 + s * 64 + kg * 8;
        u32x2 lo = *(LDSP u32x2*)p, hi = *(LDSP u32x2*)(p + 32);
        st16_sc1(ws, OFF_XNF + ((b >> 1) * 20 + s) * 1024 + kg * 256 + ((b & 1) * 8 + tk) * 16, u32x4{lo.x, lo.y, hi.x, hi.y});
      }
    }
#pragma unroll
    for (int i = 0; i < 3; ++i) {
      const int j = (3 - w) + 4 * i;
      if (j < 10) {
        LDSP char* p = sm + LDS_STG + ((lane >> 2) & 3) * 1312 + (64 * j + 16 * g4 + 4 * (lane & 3)) * 2;
        u32x2 t0 = lds_tr(p), t1 = lds_tr(p + 4 * 1312);
        u32x4 o;
        o.x = pk_f16(bf_lo(t0.x), bf_hi(t0.x)); o.y = pk_f16(bf_lo(t0.y), bf_hi(t0.y));
        o.z = pk_f16(bf_lo(t1.x), bf_hi(t1.x)); o.w = pk_f16(bf_lo(t1.y), bf_hi(t1.y));
        const int d = 64 * j + lane;
        st16_sc1(ws, OFF_XNG + (d >> 2) * 16384 + b * 64 + (d & 3) * 16, o);
      }
    }
  }
  wait_vm0();
  bar();
  if (tid == 0) st4_sc1(ws, OFF_F0 + (b >> 4) * 128 + (b & 15) * 4, tag);
  STAMP(1);

  const int tt1 = (xcd >> 1) * 4 + (rk & 3), ft1 = (xcd & 1) * 8 + (rk >> 2);
  u32 w1off[3];
#pragma unroll
  for (int t = 0; t < 3; ++t) {
    const int Q = 64 * t + lane, row = Q / 12, cpos = Q - row * 12;
    w1off[t] = row * (F * 2) + (cpos ^ (((row >> 2) & 1) << 1)) * 16;
  }
  const char* w1base = (const char*)a.w1 + ft1 * 192;
#pragma unroll
  for (int i = 0; i < 8; ++i) {
    const int G = w + 4 * i;
#pragma unroll
    for (int t = 0; t < 3; ++t) glds16(w1off[t], w1base + (size_t)G * 16 * F * 2, lds0 + LDS_W1 + G * 3072 + t * 1024);
  }

  f32x4 kacc[3];
  float yscale = 0.f;
  if (is_conv) {
    const float dt = rbf(exp_acc(bf_lo(ldt_raw)));
    float cr[3], ci[3], a2[3], th[3];
    {
      const u32 la[3] = {pa[0] << 16, pa[0] & 0xffff0000u, pb[0] << 16};
      const u32 ai[3] = {pa[1] << 16, pa[1] & 0xffff0000u, pb[1] << 16};
      const u32 c0[3] = {pa[2] << 16, pa[2] & 0xffff0000u, pb[2] << 16};
      const u32 c1[3] = {pa[3] << 16, pa[3] & 0xffff0000u, pb[3] << 16};
      float mx = 0.f;
#pragma unroll
      for (int r = 0; r < 3; ++r) {
        const float ar = -rbf(exp_acc(__uint_as_float(la[r]))) * dt;
        a2[r] = ar * 1.44269504088896341f;
        th[r] = (__uint_as_float(ai[r]) * dt) * 0.15915494309189535f;
        cr[r] = __uint_as_float(c0[r]) * dt; ci[r] = __uint_as_float(c1[r]) * dt;
        mx = fmaxf(mx, fmaxf(fabsf(cr[r]), fabsf(ci[r])));
      }
      mx = max64(mx);
      const int ec = __builtin_amdgcn_frexp_expf(mx);
#pragma unroll
      for (int r = 0; r < 3; ++r) { cr[r] = __builtin_ldexpf(cr[r], -ec); ci[r] = __builtin_ldexpf(ci[r], -ec); }
      yscale = __builtin_ldexpf(2.0f, ec);
    }
    LDSP char* stg = sm + LDS_STG + w * 16384;
    LDSP char* wr[4];
#pragma unroll
    for (int j = 0; j < 4; ++j) wr[j] = stg + lane * 64 + ((j + (lane >> 1)) & 3) * 16;
    const int tq = (lane >> 2) & 3, tp = lane & 3;
    LDSP char* rd = stg + (2 * g4 + (tq >> 1)) * 64 + ((2 * (tq & 1) + (tp >> 1) + g4) & 3) * 16 + (tp & 1) * 8;
#pragma unroll
    for (int t = 0; t < 3; ++t) kacc[t] = f32x4{0.f, 0.f, 0.f, 0.f};
#pragma unroll 1
    for (int r = 0; r < 3; ++r) {
      const float a2r = r == 0 ? a2[0] : (r == 1 ? a2[1] : a2[2]);
      const float thr = r == 0 ? th[0] : (r == 1 ? th[1] : th[2]);
      const float crr = r == 0 ? cr[0] : (r == 1 ? cr[1] : cr[2]);
      const float cir = r == 0 ? ci[0] : (r == 1 ? ci[1] : ci[2]);
      const Cplx w1 = mkc(cexp_p(a2r, thr, 1.f)), w8 = mkc(cexp_p(a2r, thr, 8.f)), w128 = mkc(cexp_p(a2r, thr, 128.f));
      auto put = [&](int tile, int half, const f32x2* e) {
        u32x4 re, im;
        re.x = pk_f16(e[0].x, e[1].x); re.y = pk_f16(e[2].x, e[3].x); re.z = pk_f16(e[4].x, e[5].x); re.w = pk_f16(e[6].x, e[7].x);
        im.x = pk_f16(e[0].y, e[1].y); im.y = pk_f16(e[2].y, e[3].y); im.z = pk_f16(e[4].y, e[5].y); im.w = pk_f16(e[6].y, e[7].y);
        *(LDSP u32x4*)(wr[half] + tile * 4096) = re;
        *(LDSP u32x4*)(wr[2 + half] + tile * 4096) = im;
      };
      f32x2 e[8];
      {
        const Cplx w8c = mkc(f32x2{w8.v.x, -w8.v.y});
        e[0] = f32x2{1.f, 0.f}; e[1] = w8c.v;
#pragma unroll
        for (int i = 2; i < 8; ++i) e[i] = cmul(e[i - 1], w8c);
        put(3, 0, e);
        e[0] = cmul(e[7], w8c);
#pragma unroll
        for (int i = 1; i < 8; ++i) e[i] = cmul(e[i - 1], w8c);
        put(3, 1, e);
      }
      const f32x2 x0 = f32x2{crr, cir};
      e[0] = x0;
#pragma unroll
      for (int i = 1; i < 8; ++i) e[i] = cmul(e[i - 1], w1);
      put(0, 0, e);
      const f32x2 x128 = cmul(x0, w128);
      e[0] = x128;
#pragma unroll
      for (int i = 1; i < 8; ++i) e[i] = cmul(e[i - 1], w1);
      put(0, 1, e);
      const Cplx w2 = mkc(cmul(w1.v, w1));
      const Cplx w4 = mkc(cmul(w2.v, w2));
      const f32x2 x256 = cmul(x128, w128), x384 = cmul(x256, w128);
      e[0] = x256; e[4] = x384;
#pragma unroll
      for (int i = 1; i < 4; ++i) { e[i] = cmul(e[i - 1], w2); e[4 + i] = cmul(e[3 + i], w2); }
      put(1, 0, e);
      e[0] = cmul(x384, w128); e[2] = cmul(e[0], w128); e[4] = cmul(e[2], w128); e[6] = cmul(e[4], w128);
      e[1] = cmul(e[0], w4); e[3] = cmul(e[2], w4); e[5] = cmul(e[4], w4); e[7] = cmul(e[6], w4);
      const f32x2 x896 = e[6];
      put(1, 1, e);
      e[0] = cmul(x896, w128);
#pragma unroll
      for (int i = 1; i < 8; ++i) e[i] = cmul(e[i - 1], w128);
      const f32x2 x2048 = cmul(e[7], w128);
      put(2, 0, e);
      *(LDSP u32x4*)(wr[1] + 2 * 4096) = u32x4{pk_f16(x2048.x, 0.f), 0u, 0u, 0u};
      *(LDSP u32x4*)(wr[3] + 2 * 4096) = u32x4{pk_f16(x2048.y, 0.f), 0u, 0u, 0u};
#pragma unroll
      for (int ks = 0; ks < 4; ++ks) {
        u32x4 fr[4];
#pragma unroll
        for (int t = 0; t < 4; ++t) {
          u32x2 lo = lds_tr(rd + t * 4096 + ks * 1024), hi = lds_tr(rd + t * 4096 + ks * 1024 + 512);
          fr[t] = u32x4{lo.x, lo.y, hi.x, hi.y};
        }
#pragma unroll
        for (int t = 0; t < 3; ++t) kacc[t] = mfma_h(fr[t], fr[3], kacc[t]);
      }
    }
  } else if (b < 220) {
    const int pw = (b - 160) * 4 + w, s = pw / 5, pc = pw - s * 5;
    const u32 stg = LDS_STG + w * 16384;
#pragma unroll
    for (int t = 0; t < 8; ++t) {
      const int row = 4 * t + g4;
      glds16(row * (D * 2) + ((l16 ^ (2 * (row & 7))) * 16), (const char*)a.w2 + (size_t)s * 32 * D * 2 + pc * 256, lds0 + stg + t * 1024);
    }
    wait_vm0();
    const int tq = (lane >> 2) & 3, tp = lane & 3, row = 4 * g4 + tq;
#pragma unroll
    for (int c = 0; c < 8; ++c) {
      LDSP char* p = sm + stg + row * 256 + (((2 * c + (tp >> 1)) ^ (2 * (row & 7))) * 16) + (tp & 1) * 8;
      u32x2 lo = lds_tr(p), hi = lds_tr(p + 16 * 256);
      st16_sc1(ws, OFF_W2F + ((pc * 8 + c) * 48 + s) * 1024 + lane * 16, u32x4{lo.x, lo.y, hi.x, hi.y});
    }
    wait_vm0();
  }
  bar();
  if (!is_conv && b < 220 && tid == 0) st4_sc1(ws, OFF_FW + (b - 160) * 4, tag);
  STAMP(2);
#pragma unroll
  for (int i = 8; i < 10; ++i) {
    const int G = w + 4 * i;
#pragma unroll
    for (int t = 0; t < 3; ++t) glds16(w1off[t], w1base + (size_t)G * 16 * F * 2, lds0 + LDS_W1 + G * 3072 + t * 1024);
  }
  if (is_conv) {
    float mx = 0.f;
#pragma unroll
    for (int t = 0; t < 3; ++t)
#pragma unroll
      for (int i = 0; i < 4; ++i) mx = fmaxf(mx, fabsf(kacc[t][i]));
    mx = max64(mx);
    const int ef = __builtin_amdgcn_frexp_expf(mx);
    yscale = __builtin_ldexpf(yscale, ef - 14);
#pragma unroll
    for (int t = 0; t < 3; ++t)
#pragma unroll
      for (int i = 0; i < 4; ++i) kacc[t][i] = __builtin_ldexpf(kacc[t][i], 14 - ef);
    LDSP char* G = sm + LDS_G + w * 1536;
    *(LDSP u32x2*)(G + 2 * (8 * l16 + 4 * (g4 & 1) + 128 * (g4 >> 1))) = u32x2{pk_f16(kacc[0][0], kacc[0][1]), pk_f16(kacc[0][2], kacc[0][3])};
    const int i0 = g4 < 2 ? 256 + 64 * g4 + 4 * l16 : 384 + 64 * (g4 - 2) + 2 * l16;
    const int i1 = g4 < 2 ? i0 + 2 : i0 + 32;
    *(LDSP u32*)(G + 2 * i0) = pk_f16(kacc[1][0], kacc[1][1]);
    *(LDSP u32*)(G + 2 * i1) = pk_f16(kacc[1][2], kacc[1][3]);
    const u32 p01 = pk_f16(kacc[2][0], kacc[2][1]), p23 = pk_f16(kacc[2][2], kacc[2][3]);
    LDSP u16* g2 = (LDSP u16*)(G + 2 * (512 + 64 * g4 + l16));
    g2[0] = (u16)p01; g2[16] = (u16)(p01 >> 16); g2[32] = (u16)p23; g2[48] = (u16)(p23 >> 16);
  }

  wait_vm0();
  if (w == 0) poll_eq(ws, OFF_F0 + tt1 * 128 + (lane & 15) * 4, true, tag);
  bar();
  STAMP(3);
  {
    u32x4 xf[2][20];
#pragma unroll
    for (int s = 0; s < 20; ++s)
#pragma unroll
      for (int t = 0; t < 2; ++t) xf[t][s] = ld16_sc1(ws, OFF_XNF + ((tt1 * 8 + 2 * w + t) * 20 + s) * 1024 + lane * 16);
    u32x2 bias[6];
#pragma unroll
    for (int f = 0; f < 6; ++f) bias[f] = *(const u32x2*)(a.b1 + ft1 * 96 + 16 * f + 4 * g4);
    f32x4 acc[2][6];
#pragma unroll
    for (int f = 0; f < 6; ++f) {
      acc[0][f] = f32x4{0.f, 0.f, 0.f, 0.f}; acc[1][f] = acc[0][f];
    }
    const int tq = (lane >> 2) & 3, tp = lane & 3;
    LDSP char* aE = sm + LDS_W1 + (4 * g4 + tq) * 192 + tp * 8 + 32 * (g4 & 1);
    LDSP char* aO = sm + LDS_W1 + (4 * g4 + tq) * 192 + tp * 8 + 32 * (1 - (g4 & 1));
#pragma unroll
    for (int s = 0; s < 20; ++s) {
#pragma unroll
      for (int f = 0; f < 6; ++f) {
        LDSP char* p = ((f & 1) ? aO : aE) + s * 6144 + (f >> 1) * 64;
        u32x2 lo = lds_tr(p), hi = lds_tr(p + 3072);
        const u32x4 af = u32x4{lo.x, lo.y, hi.x, hi.y};
        acc[0][f] = mfma_bf(af, xf[0][s], acc[0][f]);
        acc[1][f] = mfma_bf(af, xf[1][s], acc[1][f]);
      }
    }
    STAMP(4);
#pragma unroll
    for (int t = 0; t < 2; ++t)
#pragma unroll
      for (int sp = 0; sp < 3; ++sp) {
        u32x4 o;
#pragma unroll
        for (int hh = 0; hh < 2; ++hh) {
          const int f = 2 * sp + hh;
          float v0 = acc[t][f][0] + bf_lo(bias[f].x), v1 = acc[t][f][1] + bf_hi(bias[f].x);
          float v2 = acc[t][f][2] + bf_lo(bias[f].y), v3 = acc[t][f][3] + bf_hi(bias[f].y);
          v0 *= fmaxf(v0, 0.f); v1 *= fmaxf(v1, 0.f); v2 *= fmaxf(v2, 0.f); v3 *= fmaxf(v3, 0.f);
          o[2 * hh] = pk_bf16(v0, v1); o[2 * hh + 1] = pk_bf16(v2, v3);
        }
        st16_sc1(ws, OFF_HF + ((tt1 * 8 + 2 * w + t) * 48 + ft1 * 3 + sp) * 1024 + lane * 16, o);
      }
  }
  wait_vm0();
  bar();
  if (tid == 0) st4_sc1(ws, OFF_FH + tt1 * 128 + ft1 * 4, tag);
  STAMP(5);

  if (w == 0) {
    poll_eq(ws, OFF_F0 + (lane >> 2) * 128 + (lane & 3) * 16, true, tag);
    poll_eq(ws, OFF_F0 + (lane >> 2) * 128 + (lane & 3) * 16 + 4, true, tag);
    poll_eq(ws, OFF_F0 + (lane >> 2) * 128 + (lane & 3) * 16 + 8, true, tag);
    poll_eq(ws, OFF_F0 + (lane >> 2) * 128 + (lane & 3) * 16 + 12, true, tag);
    poll_eq(ws, OFF_FW + lane * 4, lane < 60, tag);
    if (b == 255 && lane == 0) st4_sc1(ws, OFF_EPOCH, tag);
  }
  bar();
  STAMP(6);
  const int t2 = xcd * 4 + (rk & 3), j2 = rk >> 2;
  u32x4 w2f[12][5];
#pragma unroll
  for (int s = 0; s < 12; ++s)
#pragma unroll
    for (int c = 0; c < 5; ++c) w2f[s][c] = ld16_sc1(ws, OFF_W2F + ((j2 * 5 + c) * 48 + 12 * w + s) * 1024 + lane * 16);

  if (is_conv) {
    {
      u32x4 ur[4];
#pragma unroll
      for (int j = 0; j < 4; ++j) ur[j] = ld16_sc1(ws, OFF_XNG + b * 16384 + (tid + 256 * j) * 16);
      if (lane < 32) *(LDSP u32x4*)(sm + LDS_U + w * U_STRIDE + lane * 16) = u32x4{0u, 0u, 0u, 0u};
#pragma unroll
      for (int j = 0; j < 4; ++j) {
        const int p = tid + 256 * j;
        *(LDSP u32x4*)(sm + LDS_U + (p & 3) * U_STRIDE + 512 + (p >> 2) * 16) = ur[j];
      }
    }
    {
      LDSP char* G = sm + LDS_G + w * 1536;
      LDSP char* R = sm + LDS_R + w * R_STRIDE;
      auto putR = [&](int B, u32x4 r0, u32x4 r1) {
        const int t = (2056 - B) >> 3;
        *(LDSP u32x4*)(R + t * 16) = r0;
        *(LDSP u32x4*)(R + R_COPY + t * 16) = r1;
      };
      auto perm = [](u32 hi, u32 lo, u32 sel) { return __builtin_amdgcn_perm(hi, lo, sel); };
      constexpr u32 L0 = 0x0100, L1 = 0x0302, H0 = 0x0504, H1 = 0x0706;
      #define SEL(a_, b_) ((a_) | ((b_) << 16))
      if (lane <= 32) {
        const int B = 8 * lane - 8;
        u32x4 d; u32 d4;
        if (B >= 0) { d = *(LDSP u32x4*)(G + 2 * B); } else { d = u32x4{0u, 0u, 0u, 0u}; }
        d4 = *(LDSP u32*)(G + 2 * (B + 8));
        if (B == 248) d4 = perm(d4, d4, SEL(L0, L0));
        u32x4 r0, r1;
        r0.x = perm(d4, d.w, SEL(H0, L1)); r0.y = perm(d.w, d.z, SEL(H0, L1)); r0.z = perm(d.z, d.y, SEL(H0, L1)); r0.w = perm(d.y, d.x, SEL(H0, L1));
        r1.x = perm(d4, d4, SEL(L1, L0)); r1.y = perm(d.w, d.w, SEL(L1, L0)); r1.z = perm(d.z, d.z, SEL(L1, L0)); r1.w = perm(d.y, d.y, SEL(L1, L0));
        putR(B, r0, r1);
      } else if (lane == 33) {
        putR(-16, u32x4{0u, 0u, 0u, 0u}, u32x4{0u, 0u, 0u, 0u});
      }
      if (lane < 32) {
        const int B = 256 + 8 * lane;
        const u32x2 d = *(LDSP u32x2*)(G + 2 * (256 + 4 * lane));
        const u32 d2 = *(LDSP u32*)(G + 2 * (256 + 4 * lane + 4));
        u32x4 r0, r1;
        r0.x = perm(d2, d2, SEL(L0, L0)); r0.y = perm(d.y, d.y, SEL(L1, L0)); r0.z = perm(d.y, d.y, SEL(L0, L0)); r0.w = perm(d.x, d.x, SEL(L1, L0));
        r1.x = r0.x; r1.y = perm(d2, d.y, SEL(H0, L1)); r1.z = r0.z; r1.w = perm(d.y, d.x, SEL(H0, L1));
        putR(B, r0, r1);
      }
      {
        const int B = 512 + 8 * lane;
        const u32 d0 = *(LDSP u32*)(G + 2 * (384 + 2 * lane));
        const u32 d1 = *(LDSP u32*)(G + 2 * (384 + 2 * lane + 2));
        u32x4 r0, r1;
        r0.x = perm(d1, d1, SEL(L0, L0)); r0.y = perm(d1, d0, SEL(H0, L1)); r0.z = perm(d0, d0, SEL(L1, L1)); r0.w = perm(d0, d0, SEL(L0, L0));
        r1.x = r0.x; r1.y = r0.x; r1.z = r0.z; r1.w = perm(d0, d0, SEL(L1, L0));
        putR(B, r0, r1);
      }
#pragma unroll
      for (int k = 0; k < 2; ++k) {
        const int idx = 64 * k + lane, B = 1024 + 8 * idx;
        const LDSP u16* gp = (const LDSP u16*)(G + 2 * (512 + idx));
        const u32 v0 = gp[0], v1 = gp[1];
        const u32 v00 = v0 | (v0 << 16), v11 = v1 | (v1 << 16), v10 = v1 | (v0 << 16);
        const bool odd = idx & 1;
        u32x4 r0, r1;
        r0.x = v11; r0.y = v11; r0.z = odd ? v10 : v00; r0.w = v00;
        r1.x = v11; r1.y = v11; r1.z = odd ? v11 : v10; r1.w = v00;
        putR(B, r0, r1);
      }
    }
    bar();
    STAMP(7);
    f32x4 cacc[8];
#pragma unroll
    for (int P = 0; P < 8; ++P) cacc[P] = f32x4{0.f, 0.f, 0.f, 0.f};
    {
      LDSP char* ra = sm + LDS_R + w * R_STRIDE + (l16 & 1) * R_COPY + 4 * (1024 - (l16 >> 1) + 4 * g4);
      LDSP char* ub = sm + LDS_U + w * U_STRIDE + 2 * (240 + 16 * l16 + 8 * g4);
#pragma unroll 1
      for (int r = 0; r < 8; ++r) {
        u32x4 af[8], bfr[8];
#pragma unroll
        for (int i = 0; i < 8; ++i) {
          const LDSP u32* p = (const LDSP u32*)(ra - 512 * i - 64 * r);
          af[i] = u32x4{p[0], p[1], p[2], p[3]};
          bfr[i] = *(LDSP u32x4*)(ub + 512 * i - 64 * r);
        }
#pragma unroll
        for (int P = 0; P < 8; ++P)
#pragma unroll
          for (int i = 0; i <= P; ++i) cacc[P] = mfma_h(af[i], bfr[P - i], cacc[P]);
      }
    }
    STAMP(8);
    {
      const float dsk = bf_lo(dsk_raw);
#pragma unroll
      for (int P = 0; P < 8; ++P) {
        const int tok = 256 * P + 16 * l16 + 4 * g4;
        const u32x2 uu = *(LDSP u32x2*)(sm + LDS_U + w * U_STRIDE + 512 + tok * 2);
        const u32 uux = uu.x, uuy = uu.y;
        const f16x2 u01 = __builtin_bit_cast(f16x2, uux), u23 = __builtin_bit_cast(f16x2, uuy);
        const float s0 = __builtin_fmaf((float)u01.x, dsk, cacc[P][0] * yscale), s1 = __builtin_fmaf((float)u01.y, dsk, cacc[P][1] * yscale);
        const float s2 = __builtin_fmaf((float)u23.x, dsk, cacc[P][2] * yscale), s3 = __builtin_fmaf((float)u23.y, dsk, cacc[P][3] * yscale);
        st8_sc1(ws, OFF_ST + ch * 4096 + tok * 2, u32x2{pk_bf16(s0, s1), pk_bf16(s2, s3)});
      }
    }
    wait_vm0();
    bar();
    if (tid == 0) st4_sc1(ws, OFF_FS + (b / 20) * 128 + (b % 20) * 4, tag);
  }
  STAMP(9);

  if (w == 0) poll_eq(ws, OFF_FH + (t2 >> 1) * 128 + (lane & 15) * 4, true, tag);
  bar();
  STAMP(10);
  f32x4 oacc[4][5];
  {
    u32x4 hf[4][12];
#pragma unroll
    for (int t = 0; t < 4; ++t)
#pragma unroll
      for (int s = 0; s < 12; ++s) hf[t][s] = ld16_sc1(ws, OFF_HF + ((t2 * 4 + t) * 48 + 12 * w + s) * 1024 + lane * 16);
#pragma unroll
    for (int t = 0; t < 4; ++t)
#pragma unroll
      for (int c = 0; c < 5; ++c) oacc[t][c] = f32x4{0.f, 0.f, 0.f, 0.f};
#pragma unroll
    for (int t = 0; t < 4; ++t)
#pragma unroll
      for (int s = 0; s < 12; ++s)
#pragma unroll
        for (int c = 0; c < 5; ++c) oacc[t][c] = mfma_bf(w2f[s][c], hf[t][s], oacc[t][c]);
  }
  STAMP(11);
  {
#pragma unroll
    for (int t = 0; t < 4; ++t)
#pragma unroll
      for (int c = 0; c < 5; ++c) *(LDSP f32x4*)(sm + LDS_RED + ((w * 4 + t) * 5 + c) * 1024 + lane * 16) = oacc[t][c];
    u32x2 xo[5], b2v[5];
    const int tok = t2 * 64 + w * 16 + l16;
#pragma unroll
    for (int c = 0; c < 5; ++c) {
      xo[c] = *(const u32x2*)(a.x + (size_t)tok * D + j2 * 80 + 16 * c + 4 * g4);
      b2v[c] = *(const u32x2*)(a.b2 + j2 * 80 + 16 * c + 4 * g4);
    }
    if (w == 0) poll_eq(ws, OFF_FS + j2 * 128 + (lane < 20 ? lane : 0) * 4, true, tag);
    bar();
    STAMP(12);
#pragma unroll
    for (int i = 0; i < 3; ++i) {
      const int p = tid + 256 * i;
      if (p < 640) {
        const u32x4 v = ld16_sc1(ws, OFF_ST + (j2 * 80 + (p >> 3)) * 4096 + t2 * 128 + (p & 7) * 16);
        *(LDSP u32x4*)(sm + LDS_S + (p >> 3) * 160 + (p & 7) * 16) = v;
      }
    }
    bar();
    const int tq = (lane >> 2) & 3, tp = lane & 3;
#pragma unroll
    for (int c = 0; c < 5; ++c) {
      f32x4 o = *(LDSP f32x4*)(sm + LDS_RED + ((0 * 4 + w) * 5 + c) * 1024 + lane * 16);
#pragma unroll
      for (int src = 1; src < 4; ++src) o += *(LDSP f32x4*)(sm + LDS_RED + ((src * 4 + w) * 5 + c) * 1024 + lane * 16);
      const u32x2 sv = lds_tr(sm + LDS_S + (16 * c + 4 * g4 + tq) * 160 + w * 32 + tp * 8);
      const float bb[4] = {bf_lo(b2v[c].x), bf_hi(b2v[c].x), bf_lo(b2v[c].y), bf_hi(b2v[c].y)};
      const float ss[4] = {bf_lo(sv.x), bf_hi(sv.x), bf_lo(sv.y), bf_hi(sv.y)};
      const float xx[4] = {bf_lo(xo[c].x), bf_hi(xo[c].x), bf_lo(xo[c].y), bf_hi(xo[c].y)};
      float r[4];
#pragma unroll
      for (int i = 0; i < 4; ++i) {
        const float z = o[i] + bb[i];
        const float g = z * __builtin_amdgcn_rcpf(1.0f + __builtin_amdgcn_exp2f(z * -1.44269504088896341f));
        r[i] = __builtin_fmaf(ss[i], g, xx[i]);
      }
      *(u32x2*)(a.out + (size_t)tok * D + j2 * 80 + 16 * c + 4 * g4) = u32x2{pk_bf16(r[0], r[1]), pk_bf16(r[2], r[3])};
    }
  }
  STAMP(13);
  stamp[15] = __builtin_amdgcn_s_memtime();
  if (lane == 0) {
#pragma unroll
    for (int i = 0; i < 16; ++i) *(u64*)(a.ws + OFF_STAMP + ((b * 4 + w) * 16 + i) * 8) = stamp[i];
  }
  if (b == PROBE_BLK && w == 0) {
    u64 dur = stamp[PROBE] - stamp[0];
    const u64 target = stamp[0] + 6000 + dur;
    for (int it = 0; it < 100000; ++it) {
      if (__builtin_amdgcn_s_memrealtime() >= target) break;
      __builtin_amdgcn_s_sleep(1);
    }
  }
}

extern "C" void kernel_launch(void* const* d_in, const int* in_sizes, int n_in, void* d_out, int out_size, void* d_ws,
                              size_t ws_size, hipStream_t stream) {
  Args a;
  a.x = (const u16*)d_in[0]; a.logA = (const u16*)d_in[1]; a.aim = (const u16*)d_in[2]; a.cre = (const u16*)d_in[3];
  a.cim = (const u16*)d_in[4]; a.logdt = (const u16*)d_in[5]; a.dskip = (const u16*)d_in[6];
  a.w1 = (const u16*)d_in[8]; a.b1 = (const u16*)d_in[9]; a.w2 = (const u16*)d_in[10]; a.b2 = (const u16*)d_in[11];
  a.out = (u16*)d_out; a.ws = (char*)d_ws;
  fused_kernel<<<256, 256, 0, stream>>>(a);
}
```

```cpp
#include <hip/hip_runtime.h>
#include <stdint.h>

typedef unsigned short u16;
typedef unsigned u32;
typedef unsigned long long u64;
typedef float f32x2 __attribute__((ext_vector_type(2)));
typedef float f32x4 __attribute__((ext_vector_type(4)));
typedef unsigned u32x2 __attribute__((ext_vector_type(2)));
typedef unsigned u32x4 __attribute__((ext_vector_type(4)));
typedef short s16x4 __attribute__((ext_vector_type(4)));
typedef __bf16 bf16x2 __attribute__((ext_vector_type(2)));
typedef __bf16 bf16x8 __attribute__((ext_vector_type(8)));
typedef _Float16 f16x2 __attribute__((ext_vector_type(2)));
typedef _Float16 f16x8 __attribute__((ext_vector_type(8)));
#define LDSP __attribute__((address_space(3)))
#define GLB __attribute__((address_space(1)))

#define STOP_AFTER 99

constexpr int L = 2048, D = 640, N = 192, F = 1536;

constexpr u32 OFF_EPOCH = 0;
constexpr u32 OFF_F0 = 4096;
constexpr u32 OFF_FW = 8192;
constexpr u32 OFF_FH = 12288;
constexpr u32 OFF_FS = 16384;
constexpr u32 OFF_STAMP = 32768;
constexpr u32 OFF_XNF = 1u << 20;
constexpr u32 OFF_XNG = 4u << 20;
constexpr u32 OFF_W2F = 8u << 20;
constexpr u32 OFF_HF = 12u << 20;
constexpr u32 OFF_ST = 20u << 20;
constexpr u32 OFF_DBG = 24u << 20;
constexpr u32 WS_BYTES = 32u << 20;

constexpr u32 LDS_W1 = 0;
constexpr u32 LDS_STG = 98304;
constexpr u32 LDS_G = 122880;
constexpr u32 LDS_U = 0;
constexpr u32 U_STRIDE = 4640;
constexpr u32 LDS_R = 20480;
constexpr u32 R_COPY = 4224 + 64;
constexpr u32 R_STRIDE = 2 * 4352;
constexpr u32 LDS_RED = 0;
constexpr u32 LDS_S = 81920;
constexpr u32 LDS_BYTES = 163840;

struct Args {
  const u16* x; const u16* logA; const u16* aim; const u16* cre; const u16* cim; const u16* logdt; const u16* dskip;
  const u16* w1; const u16* b1; const u16* w2; const u16* b2; u16* out; char* ws;
};

#define SBAR() __builtin_amdgcn_sched_barrier(0)
#define AUX_SC1 16
typedef __amdgpu_buffer_rsrc_t rsrc_t;

__device__ __forceinline__ float bf_lo(u32 v) { return __uint_as_float(v << 16); }
__device__ __forceinline__ float bf_hi(u32 v) { return __uint_as_float(v & 0xffff0000u); }
__device__ __forceinline__ u32 pk_bf16(float a, float b) {
  return __builtin_bit_cast(u32, __builtin_convertvector(f32x2{a, b}, bf16x2));
}
__device__ __forceinline__ u32 pk_f16(float a, float b) {
  return __builtin_bit_cast(u32, __builtin_convertvector(f32x2{a, b}, f16x2));
}
__device__ __forceinline__ float rbf(float a) { return bf_lo(pk_bf16(a, a)); }

template <int CTRL> __device__ __forceinline__ float dpp(float v) {
  return __int_as_float(__builtin_amdgcn_update_dpp(0, __float_as_int(v), CTRL, 0xf, 0xf, true));
}
__device__ __forceinline__ float sum32(float v) {
  v += dpp<0xB1>(v); v += dpp<0x4E>(v); v += dpp<0x141>(v); v += dpp<0x140>(v);
  auto r = __builtin_amdgcn_permlane16_swap(__float_as_uint(v), __float_as_uint(v), false, false);
  return __uint_as_float(r[0]) + __uint_as_float(r[1]);
}
__device__ __forceinline__ float max64(float v) {
  v = fmaxf(v, dpp<0xB1>(v)); v = fmaxf(v, dpp<0x4E>(v)); v = fmaxf(v, dpp<0x141>(v)); v = fmaxf(v, dpp<0x140>(v));
  auto r = __builtin_amdgcn_permlane16_swap(__float_as_uint(v), __float_as_uint(v), false, false);
  v = fmaxf(__uint_as_float(r[0]), __uint_as_float(r[1]));
  auto t = __builtin_amdgcn_permlane32_swap(__float_as_uint(v), __float_as_uint(v), false, false);
  return fmaxf(__uint_as_float(t[0]), __uint_as_float(t[1]));
}
__device__ __forceinline__ float exp_acc(float x) {
  const float hi = 1.44269502162933349609375f, lo = 1.925963033500011079e-8f;
  float t = x * hi;
  float n = __builtin_rintf(t);
  float f = __builtin_fmaf(x, hi, -n);
  f = __builtin_fmaf(x, lo, f);
  return __builtin_ldexpf(__builtin_amdgcn_exp2f(f), (int)n);
}

__device__ __forceinline__ u32x4 ld16_sc1(rsrc_t r, u32 off) { return __builtin_amdgcn_raw_buffer_load_b128(r, off, 0, AUX_SC1); }
__device__ __forceinline__ void st16_sc1(rsrc_t r, u32 off, u32x4 v) { __builtin_amdgcn_raw_buffer_store_b128(v, r, off, 0, AUX_SC1); }
__device__ __forceinline__ void st8_sc1(rsrc_t r, u32 off, u32x2 v) { __builtin_amdgcn_raw_buffer_store_b64(v, r, off, 0, AUX_SC1); }
__device__ __forceinline__ u32 ld4_sc1(rsrc_t r, u32 off) { return __builtin_amdgcn_raw_buffer_load_b32(r, off, 0, AUX_SC1); }
__device__ __forceinline__ void st4_sc1(rsrc_t r, u32 off, u32 v) { __builtin_amdgcn_raw_buffer_store_b32(v, r, off, 0, AUX_SC1); }

__device__ __forceinline__ void glds16(u32 voff, const void* sbase, u32 lds_base) {
  u32 sv;
  asm volatile("s_mov_b32 %0, m0\n\ts_mov_b32 m0, %3\n\ts_nop 0\n\tglobal_load_lds_dwordx4 %1, %2\n\ts_mov_b32 m0, %0"
               : "=&s"(sv) : "v"(voff), "s"(sbase), "s"(lds_base) : "memory");
}
__device__ __forceinline__ void wait_vm0() { asm volatile("s_waitcnt vmcnt(0)" ::: "memory"); }
__device__ __forceinline__ void bar() { asm volatile("s_waitcnt lgkmcnt(0)\n\ts_barrier" ::: "memory"); }

__device__ __forceinline__ u32x2 lds_tr(LDSP char* p) {
  return __builtin_bit_cast(u32x2, __builtin_amdgcn_ds_read_tr16_b64_v4i16((LDSP s16x4*)p));
}
__device__ __forceinline__ f32x4 mfma_bf(u32x4 a, u32x4 b, f32x4 c) {
  return __builtin_amdgcn_mfma_f32_16x16x32_bf16(__builtin_bit_cast(bf16x8, a), __builtin_bit_cast(bf16x8, b), c, 0, 0, 0);
}
__device__ __forceinline__ f32x4 mfma_h(u32x4 a, u32x4 b, f32x4 c) {
  return __builtin_amdgcn_mfma_f32_16x16x32_f16(__builtin_bit_cast(f16x8, a), __builtin_bit_cast(f16x8, b), c, 0, 0, 0);
}

constexpr u32 SPIN_CAP = 1u << 16;
__device__ __forceinline__ void poll_eq(rsrc_t ws, u32 off, bool active, u32 tag) {
  for (u32 it = 0; it < SPIN_CAP; ++it) {
    u32 v = active ? ld4_sc1(ws, off) : tag;
    __builtin_amdgcn_fence(__ATOMIC_ACQUIRE, "wavefront");
    if (__all(v == tag)) break;
    __builtin_amdgcn_s_sleep(1);
  }
}

struct Cplx { f32x2 v, r; };
__device__ __forceinline__ Cplx mkc(f32x2 v) { Cplx c; c.v = v; c.r = f32x2{-v.y, v.x}; return c; }
__device__ __forceinline__ f32x2 cmul(f32x2 a, const Cplx& w) {
  f32x2 t = f32x2{a.y, a.y} * w.r;
  return __builtin_elementwise_fma(f32x2{a.x, a.x}, w.v, t);
}
__device__ __forceinline__ f32x2 cexp_p(float a2, float th, float p) {
  float mag = __builtin_amdgcn_exp2f(a2 * p);
  float fr = __builtin_amdgcn_fractf(th * p);
  return f32x2{mag * __builtin_amdgcn_cosf(fr), mag * __builtin_amdgcn_sinf(fr)};
}

#define STAMP(i) do { } while (0)

__global__ __launch_bounds__(256, 1) void fused_kernel(Args a) {
  __shared__ __attribute__((aligned(16))) char smem_[LDS_BYTES];
  LDSP char* sm = (LDSP char*)smem_;
  const u32 lds0 = (u32)(uintptr_t)sm;
  const int tid = threadIdx.x, lane = tid & 63;
  const int w = __builtin_amdgcn_readfirstlane(tid >> 6);
  const int b = blockIdx.x, xcd = b & 7, rk = b >> 3;
  const rsrc_t ws = __builtin_amdgcn_make_buffer_rsrc((void*)a.ws, 0, WS_BYTES, 0x00020000);
  const int g4 = lane >> 4, l16 = lane & 15;
  const rsrc_t outr = __builtin_amdgcn_make_buffer_rsrc((void*)a.out, 0, L * D * 2, 0x00020000);
  STAMP(0);

  const u32 epoch_v = ld4_sc1(ws, OFF_EPOCH);
  u32x2 xr[5];
  {
    const u16* xp = a.x + (size_t)(8 * b + 2 * w + (lane >> 5)) * D + (lane & 31) * 4;
#pragma unroll
    for (int i = 0; i < 5; ++i) xr[i] = *(const u32x2*)(xp + i * 128);
  }
  const bool is_conv = b < 160;
  const int ch = is_conv ? 4 * b + w : 0;
  u32 pa[4]; u32 pb[4];
  {
    const u16* src[4] = {a.logA, a.aim, a.cre, a.cim};
#pragma unroll
    for (int i = 0; i < 4; ++i) {
      pa[i] = *(const u32*)(src[i] + ch * N + 2 * lane);
      pb[i] = src[i][ch * N + 128 + lane];
    }
  }
  const u32 ldt_raw = a.logdt[ch];
  const u32 dsk_raw = a.dskip[ch];

  {
    float v[20];
#pragma unroll
    for (int i = 0; i < 5; ++i) {
      v[4 * i] = bf_lo(xr[i].x); v[4 * i + 1] = bf_hi(xr[i].x); v[4 * i + 2] = bf_lo(xr[i].y); v[4 * i + 3] = bf_hi(xr[i].y);
    }
    float s0 = 0.f, s1 = 0.f;
#pragma unroll
    for (int i = 0; i < 10; ++i) { s0 += v[2 * i]; s1 += v[2 * i + 1]; }
    const float mean = sum32(s0 + s1) * (1.0f / D);
    float q0 = 0.f, q1 = 0.f;
#pragma unroll
    for (int i = 0; i < 10; ++i) {
      float d0 = v[2 * i] - mean, d1 = v[2 * i + 1] - mean;
      q0 = __builtin_fmaf(d0, d0, q0); q1 = __builtin_fmaf(d1, d1, q1);
    }
    const float var = sum32(q0 + q1) * (1.0f / D);
    const float m = rbf(mean);
    const float sd = rbf(__builtin_amdgcn_sqrtf(rbf(rbf(var) + 1e-5f)));
    const float rs = 1.0f / sd;
    const float mrs = -m * rs;
    LDSP char* dst = sm + LDS_STG + (2 * w + (lane >> 5)) * 1312 + (lane & 31) * 8;
#pragma unroll
    for (int i = 0; i < 5; ++i) {
      u32x2 o;
      o.x = pk_bf16((v[4 * i] - m) * rs, (v[4 * i + 1] - m) * rs);
      o.y = pk_bf16((v[4 * i + 2] - m) * rs, (v[4 * i + 3] - m) * rs);
      *(LDSP u32x2*)(dst + i * 256) = o;
    }
    (void)mrs;
  }
  bar();
  const u32 tag = __builtin_amdgcn_readfirstlane(epoch_v) + 1u;
  {
#pragma unroll
    for (int i = 0; i < 3; ++i) {
      const int j = w + 4 * i;
      if (j < 10) {
        const int pidx = 8 * j + (lane >> 3), tk = lane & 7, s = pidx >> 2, kg = pidx & 3;
        LDSP char* p = sm + LDS_STG + tk * 1312 + s * 64 + kg * 8;
        u32x2 lo = *(LDSP u32x2*)p, hi = *(LDSP u32x2*)(p + 32);
        st16_sc1(ws, OFF_XNF + ((b >> 1) * 20 + s) * 1024 + kg * 256 + ((b & 1) * 8 + tk) * 16, u32x4{lo.x, lo.y, hi.x, hi.y});
      }
    }
#pragma unroll
    for (int i = 0; i < 3; ++i) {
      const int j = (3 - w) + 4 * i;
      if (j < 10) {
        LDSP char* p = sm + LDS_STG + ((lane >> 2) & 3) * 1312 + (64 * j + 16 * g4 + 4 * (lane & 3)) * 2;
        u32x2 t0 = lds_tr(p), t1 = lds_tr(p + 4 * 1312);
        u32x4 o;
        o.x = pk_f16(bf_lo(t0.x), bf_hi(t0.x)); o.y = pk_f16(bf_lo(t0.y), bf_hi(t0.y));
        o.z = pk_f16(bf_lo(t1.x), bf_hi(t1.x)); o.w = pk_f16(bf_lo(t1.y), bf_hi(t1.y));
        const int d = 64 * j + lane;
        st16_sc1(ws, OFF_XNG + (d >> 2) * 16384 + b * 64 + (d & 3) * 16, o);
      }
    }
  }
  wait_vm0();
  bar();
  if (tid == 0) st4_sc1(ws, OFF_F0 + (b >> 4) * 128 + (b & 15) * 4, tag);
  STAMP(1);

  const int tt1 = (xcd >> 1) * 4 + (rk & 3), ft1 = (xcd & 1) * 8 + (rk >> 2);
  u32 w1off[3];
#pragma unroll
  for (int t = 0; t < 3; ++t) {
    const int Q = 64 * t + lane, row = Q / 12, cpos = Q - row * 12;
    w1off[t] = row * (F * 2) + (cpos ^ (((row >> 2) & 1) << 1)) * 16;
  }
  const char* w1base = (const char*)a.w1 + ft1 * 192;
#pragma unroll
  for (int i = 0; i < 8; ++i) {
    const int G = w + 4 * i;
#pragma unroll
    for (int t = 0; t < 3; ++t) glds16(w1off[t], w1base + (size_t)G * 16 * F * 2, lds0 + LDS_W1 + G * 3072 + t * 1024);
  }

  f32x4 kacc[3];
  float yscale = 0.f;
  if (is_conv) {
    const float dt = rbf(exp_acc(bf_lo(ldt_raw)));
    float cr[3], ci[3], a2[3], th[3];
    {
      const u32 la[3] = {pa[0] << 16, pa[0] & 0xffff0000u, pb[0] << 16};
      const u32 ai[3] = {pa[1] << 16, pa[1] & 0xffff0000u, pb[1] << 16};
      const u32 c0[3] = {pa[2] << 16, pa[2] & 0xffff0000u, pb[2] << 16};
      const u32 c1[3] = {pa[3] << 16, pa[3] & 0xffff0000u, pb[3] << 16};
      float mx = 0.f;
#pragma unroll
      for (int r = 0; r < 3; ++r) {
        const float ar = -rbf(exp_acc(__uint_as_float(la[r]))) * dt;
        a2[r] = ar * 1.44269504088896341f;
        th[r] = (__uint_as_float(ai[r]) * dt) * 0.15915494309189535f;
        cr[r] = __uint_as_float(c0[r]) * dt; ci[r] = __uint_as_float(c1[r]) * dt;
        mx = fmaxf(mx, fmaxf(fabsf(cr[r]), fabsf(ci[r])));
      }
      mx = max64(mx);
      const int ec = __builtin_amdgcn_frexp_expf(mx);
#pragma unroll
      for (int r = 0; r < 3; ++r) { cr[r] = __builtin_ldexpf(cr[r], -ec); ci[r] = __builtin_ldexpf(ci[r], -ec); }
      yscale = __builtin_ldexpf(2.0f, ec);
    }
    LDSP char* stg = sm + LDS_STG + w * 16384;
    LDSP char* wr[4];
#pragma unroll
    for (int j = 0; j < 4; ++j) wr[j] = stg + lane * 64 + ((j + (lane >> 1)) & 3) * 16;
    const int tq = (lane >> 2) & 3, tp = lane & 3;
    LDSP char* rd = stg + (2 * g4 + (tq >> 1)) * 64 + ((2 * (tq & 1) + (tp >> 1) + g4) & 3) * 16 + (tp & 1) * 8;
#pragma unroll
    for (int t = 0; t < 3; ++t) kacc[t] = f32x4{0.f, 0.f, 0.f, 0.f};
#pragma unroll 1
    for (int r = 0; r < 3; ++r) {
      const float a2r = r == 0 ? a2[0] : (r == 1 ? a2[1] : a2[2]);
      const float thr = r == 0 ? th[0] : (r == 1 ? th[1] : th[2]);
      const float crr = r == 0 ? cr[0] : (r == 1 ? cr[1] : cr[2]);
      const float cir = r == 0 ? ci[0] : (r == 1 ? ci[1] : ci[2]);
      const Cplx w1 = mkc(cexp_p(a2r, thr, 1.f)), w8 = mkc(cexp_p(a2r, thr, 8.f)), w128 = mkc(cexp_p(a2r, thr, 128.f));
      auto put = [&](int tile, int half, const f32x2* e) {
        u32x4 re, im;
        re.x = pk_f16(e[0].x, e[1].x); re.y = pk_f16(e[2].x, e[3].x); re.z = pk_f16(e[4].x, e[5].x); re.w = pk_f16(e[6].x, e[7].x);
        im.x = pk_f16(e[0].y, e[1].y); im.y = pk_f16(e[2].y, e[3].y); im.z = pk_f16(e[4].y, e[5].y); im.w = pk_f16(e[6].y, e[7].y);
        *(LDSP u32x4*)(wr[half] + tile * 4096) = re;
        *(LDSP u32x4*)(wr[2 + half] + tile * 4096) = im;
      };
      f32x2 e[8];
      {
        const Cplx w8c = mkc(f32x2{w8.v.x, -w8.v.y});
        e[0] = f32x2{1.f, 0.f}; e[1] = w8c.v;
#pragma unroll
        for (int i = 2; i < 8; ++i) e[i] = cmul(e[i - 1], w8c);
        put(3, 0, e);
        e[0] = cmul(e[7], w8c);
#pragma unroll
        for (int i = 1; i < 8; ++i) e[i] = cmul(e[i - 1], w8c);
        put(3, 1, e);
      }
      const f32x2 x0 = f32x2{crr, cir};
      e[0] = x0;
#pragma unroll
      for (int i = 1; i < 8; ++i) e[i] = cmul(e[i - 1], w1);
      put(0, 0, e);
      const f32x2 x128 = cmul(x0, w128);
      e[0] = x128;
#pragma unroll
      for (int i = 1; i < 8; ++i) e[i] = cmul(e[i - 1], w1);
      put(0, 1, e);
      const Cplx w2 = mkc(cmul(w1.v, w1));
      const Cplx w4 = mkc(cmul(w2.v, w2));
      const f32x2 x256 = cmul(x128, w128), x384 = cmul(x256, w128);
      e[0] = x256; e[4] = x384;
#pragma unroll
      for (int i = 1; i < 4; ++i) { e[i] = cmul(e[i - 1], w2); e[4 + i] = cmul(e[3 + i], w2); }
      put(1, 0, e);
      e[0] = cmul(x384, w128); e[2] = cmul(e[0], w128); e[4] = cmul(e[2], w128); e[6] = cmul(e[4], w128);
      e[1] = cmul(e[0], w4); e[3] = cmul(e[2], w4); e[5] = cmul(e[4], w4); e[7] = cmul(e[6], w4);
      const f32x2 x896 = e[6];
      put(1, 1, e);
      e[0] = cmul(x896, w128);
#pragma unroll
      for (int i = 1; i < 8; ++i) e[i] = cmul(e[i - 1], w128);
      const f32x2 x2048 = cmul(e[7], w128);
      put(2, 0, e);
      *(LDSP u32x4*)(wr[1] + 2 * 4096) = u32x4{pk_f16(x2048.x, 0.f), 0u, 0u, 0u};
      *(LDSP u32x4*)(wr[3] + 2 * 4096) = u32x4{pk_f16(x2048.y, 0.f), 0u, 0u, 0u};
#pragma unroll
      for (int ks = 0; ks < 4; ++ks) {
        u32x4 fr[4];
#pragma unroll
        for (int t = 0; t < 4; ++t) {
          u32x2 lo = lds_tr(rd + t * 4096 + ks * 1024), hi = lds_tr(rd + t * 4096 + ks * 1024 + 512);
          fr[t] = u32x4{lo.x, lo.y, hi.x, hi.y};
        }
#pragma unroll
        for (int t = 0; t < 3; ++t) kacc[t] = mfma_h(fr[t], fr[3], kacc[t]);
      }
    }
  } else if (b < 220) {
    const int pw = (b - 160) * 4 + w, s = pw / 5, pc = pw - s * 5;
    const u32 stg = LDS_STG + w * 16384;
#pragma unroll
    for (int t = 0; t < 8; ++t) {
      const int row = 4 * t + g4;
      glds16(row * (D * 2) + ((l16 ^ (2 * (row & 7))) * 16), (const char*)a.w2 + (size_t)s * 32 * D * 2 + pc * 256, lds0 + stg + t * 1024);
    }
    wait_vm0();
    const int tq = (lane >> 2) & 3, tp = lane & 3, row = 4 * g4 + tq;
#pragma unroll
    for (int c = 0; c < 8; ++c) {
      LDSP char* p = sm + stg + row * 256 + (((2 * c + (tp >> 1)) ^ (2 * (row & 7))) * 16) + (tp & 1) * 8;
      u32x2 lo = lds_tr(p), hi = lds_tr(p + 16 * 256);
      st16_sc1(ws, OFF_W2F + ((pc * 8 + c) * 48 + s) * 1024 + lane * 16, u32x4{lo.x, lo.y, hi.x, hi.y});
    }
    wait_vm0();
  }
  bar();
  if (!is_conv && b < 220 && tid == 0) st4_sc1(ws, OFF_FW + (b - 160) * 4, tag);
  STAMP(2);
#pragma unroll
  for (int i = 8; i < 10; ++i) {
    const int G = w + 4 * i;
#pragma unroll
    for (int t = 0; t < 3; ++t) glds16(w1off[t], w1base + (size_t)G * 16 * F * 2, lds0 + LDS_W1 + G * 3072 + t * 1024);
  }
  if (is_conv) {
    float mx = 0.f;
#pragma unroll
    for (int t = 0; t < 3; ++t)
#pragma unroll
      for (int i = 0; i < 4; ++i) mx = fmaxf(mx, fabsf(kacc[t][i]));
    mx = max64(mx);
    const int ef = __builtin_amdgcn_frexp_expf(mx);
    yscale = __builtin_ldexpf(yscale, ef - 14);
#pragma unroll
    for (int t = 0; t < 3; ++t)
#pragma unroll
      for (int i = 0; i < 4; ++i) kacc[t][i] = __builtin_ldexpf(kacc[t][i], 14 - ef);
    LDSP char* G = sm + LDS_G + w * 1536;
    *(LDSP u32x2*)(G + 2 * (8 * l16 + 4 * (g4 & 1) + 128 * (g4 >> 1))) = u32x2{pk_f16(kacc[0][0], kacc[0][1]), pk_f16(kacc[0][2], kacc[0][3])};
    const int i0 = g4 < 2 ? 256 + 64 * g4 + 4 * l16 : 384 + 64 * (g4 - 2) + 2 * l16;
    const int i1 = g4 < 2 ? i0 + 2 : i0 + 32;
    *(LDSP u32*)(G + 2 * i0) = pk_f16(kacc[1][0], kacc[1][1]);
    *(LDSP u32*)(G + 2 * i1) = pk_f16(kacc[1][2], kacc[1][3]);
    const u32 p01 = pk_f16(kacc[2][0], kacc[2][1]), p23 = pk_f16(kacc[2][2], kacc[2][3]);
    LDSP u16* g2 = (LDSP u16*)(G + 2 * (512 + 64 * g4 + l16));
    g2[0] = (u16)p01; g2[16] = (u16)(p01 >> 16); g2[32] = (u16)p23; g2[48] = (u16)(p23 >> 16);
  }

  wait_vm0();
  if (w == 0) poll_eq(ws, OFF_F0 + tt1 * 128 + (lane & 15) * 4, true, tag);
  bar();
  STAMP(3);
  {
    u32x4 xf[2][20];
#pragma unroll
    for (int s = 0; s < 20; ++s)
#pragma unroll
      for (int t = 0; t < 2; ++t) xf[t][s] = ld16_sc1(ws, OFF_XNF + ((tt1 * 8 + 2 * w + t) * 20 + s) * 1024 + lane * 16);
    u32x2 bias[6];
#pragma unroll
    for (int f = 0; f < 6; ++f) bias[f] = *(const u32x2*)(a.b1 + ft1 * 96 + 16 * f + 4 * g4);
    f32x4 acc[2][6];
#pragma unroll
    for (int f = 0; f < 6; ++f) {
      acc[0][f] = f32x4{0.f, 0.f, 0.f, 0.f}; acc[1][f] = acc[0][f];
    }
    const int tq = (lane >> 2) & 3, tp = lane & 3;
    LDSP char* aE = sm + LDS_W1 + (4 * g4 + tq) * 192 + tp * 8 + 32 * (g4 & 1);
    LDSP char* aO = sm + LDS_W1 + (4 * g4 + tq) * 192 + tp * 8 + 32 * (1 - (g4 & 1));
#pragma unroll
    for (int s = 0; s < 20; ++s) {
#pragma unroll
      for (int f = 0; f < 6; ++f) {
        LDSP char* p = ((f & 1) ? aO : aE) + s * 6144 + (f >> 1) * 64;
        u32x2 lo = lds_tr(p), hi = lds_tr(p + 3072);
        const u32x4 af = u32x4{lo.x, lo.y, hi.x, hi.y};
        acc[0][f] = mfma_bf(af, xf[0][s], acc[0][f]);
        acc[1][f] = mfma_bf(af, xf[1][s], acc[1][f]);
      }
    }
    STAMP(4);
#pragma unroll
    for (int t = 0; t < 2; ++t)
#pragma unroll
      for (int sp = 0; sp < 3; ++sp) {
        u32x4 o;
#pragma unroll
        for (int hh = 0; hh < 2; ++hh) {
          const int f = 2 * sp + hh;
          float v0 = acc[t][f][0] + bf_lo(bias[f].x), v1 = acc[t][f][1] + bf_hi(bias[f].x);
          float v2 = acc[t][f][2] + bf_lo(bias[f].y), v3 = acc[t][f][3] + bf_hi(bias[f].y);
          v0 *= fmaxf(v0, 0.f); v1 *= fmaxf(v1, 0.f); v2 *= fmaxf(v2, 0.f); v3 *= fmaxf(v3, 0.f);
          o[2 * hh] = pk_bf16(v0, v1); o[2 * hh + 1] = pk_bf16(v2, v3);
        }
        st16_sc1(ws, OFF_HF + ((tt1 * 8 + 2 * w + t) * 48 + ft1 * 3 + sp) * 1024 + lane * 16, o);
      }
  }
  wait_vm0();
  bar();
  if (tid == 0) st4_sc1(ws, OFF_FH + tt1 * 128 + ft1 * 4, tag);
  STAMP(5);

  if (w == 0) {
    poll_eq(ws, OFF_F0 + (lane >> 2) * 128 + (lane & 3) * 16, true, tag);
    poll_eq(ws, OFF_F0 + (lane >> 2) * 128 + (lane & 3) * 16 + 4, true, tag);
    poll_eq(ws, OFF_F0 + (lane >> 2) * 128 + (lane & 3) * 16 + 8, true, tag);
    poll_eq(ws, OFF_F0 + (lane >> 2) * 128 + (lane & 3) * 16 + 12, true, tag);
    poll_eq(ws, OFF_FW + lane * 4, lane < 60, tag);
    if (b == 255 && lane == 0) st4_sc1(ws, OFF_EPOCH, tag);
  }
  bar();
  STAMP(6);
  const int t2 = xcd * 4 + (rk & 3), j2 = rk >> 2;
  u32x4 w2f[12][5];
#pragma unroll
  for (int s = 0; s < 12; ++s)
#pragma unroll
    for (int c = 0; c < 5; ++c) w2f[s][c] = ld16_sc1(ws, OFF_W2F + ((j2 * 5 + c) * 48 + 12 * w + s) * 1024 + lane * 16);

  if (is_conv) {
    {
      u32x4 ur[4];
#pragma unroll
      for (int j = 0; j < 4; ++j) ur[j] = ld16_sc1(ws, OFF_XNG + b * 16384 + (tid + 256 * j) * 16);
      if (lane < 32) *(LDSP u32x4*)(sm + LDS_U + w * U_STRIDE + lane * 16) = u32x4{0u, 0u, 0u, 0u};
#pragma unroll
      for (int j = 0; j < 4; ++j) {
        const int p = tid + 256 * j;
        *(LDSP u32x4*)(sm + LDS_U + (p & 3) * U_STRIDE + 512 + (p >> 2) * 16) = ur[j];
      }
    }
    {
      LDSP char* G = sm + LDS_G + w * 1536;
      LDSP char* R = sm + LDS_R + w * R_STRIDE;
      auto putR = [&](int B, u32x4 r0, u32x4 r1) {
        const int t = (2056 - B) >> 3;
        *(LDSP u32x4*)(R + t * 16) = r0;
        *(LDSP u32x4*)(R + R_COPY + t * 16) = r1;
      };
      auto perm = [](u32 hi, u32 lo, u32 sel) { return __builtin_amdgcn_perm(hi, lo, sel); };
      constexpr u32 L0 = 0x0100, L1 = 0x0302, H0 = 0x0504, H1 = 0x0706;
      #define SEL(a_, b_) ((a_) | ((b_) << 16))
      if (lane <= 32) {
        const int B = 8 * lane - 8;
        u32x4 d; u32 d4;
        if (B >= 0) { d = *(LDSP u32x4*)(G + 2 * B); } else { d = u32x4{0u, 0u, 0u, 0u}; }
        d4 = *(LDSP u32*)(G + 2 * (B + 8));
        if (B == 248) d4 = perm(d4, d4, SEL(L0, L0));
        u32x4 r0, r1;
        r0.x = perm(d4, d.w, SEL(H0, L1)); r0.y = perm(d.w, d.z, SEL(H0, L1)); r0.z = perm(d.z, d.y, SEL(H0, L1)); r0.w = perm(d.y, d.x, SEL(H0, L1));
        r1.x = perm(d4, d4, SEL(L1, L0)); r1.y = perm(d.w, d.w, SEL(L1, L0)); r1.z = perm(d.z, d.z, SEL(L1, L0)); r1.w = perm(d.y, d.y, SEL(L1, L0));
        putR(B, r0, r1);
      } else if (lane == 33) {
        putR(-16, u32x4{0u, 0u, 0u, 0u}, u32x4{0u, 0u, 0u, 0u});
      }
      if (lane < 32) {
        const int B = 256 + 8 * lane;
        const u32x2 d = *(LDSP u32x2*)(G + 2 * (256 + 4 * lane));
        const u32 d2 = *(LDSP u32*)(G + 2 * (256 + 4 * lane + 4));
        u32x4 r0, r1;
        r0.x = perm(d2, d2, SEL(L0, L0)); r0.y = perm(d.y, d.y, SEL(L1, L0)); r0.z = perm(d.y, d.y, SEL(L0, L0)); r0.w = perm(d.x, d.x, SEL(L1, L0));
        r1.x = r0.x; r1.y = perm(d2, d.y, SEL(H0, L1)); r1.z = r0.z; r1.w = perm(d.y, d.x, SEL(H0, L1));
        putR(B, r0, r1);
      }
      {
        const int B = 512 + 8 * lane;
        const u32 d0 = *(LDSP u32*)(G + 2 * (384 + 2 * lane));
        const u32 d1 = *(LDSP u32*)(G + 2 * (384 + 2 * lane + 2));
        u32x4 r0, r1;
        r0.x = perm(d1, d1, SEL(L0, L0)); r0.y = perm(d1, d0, SEL(H0, L1)); r0.z = perm(d0, d0, SEL(L1, L1)); r0.w = perm(d0, d0, SEL(L0, L0));
        r1.x = r0.x; r1.y = r0.x; r1.z = r0.z; r1.w = perm(d0, d0, SEL(L1, L0));
        putR(B, r0, r1);
      }
#pragma unroll
      for (int k = 0; k < 2; ++k) {
        const int idx = 64 * k + lane, B = 1024 + 8 * idx;
        const LDSP u16* gp = (const LDSP u16*)(G + 2 * (512 + idx));
        const u32 v0 = gp[0], v1 = gp[1];
        const u32 v00 = v0 | (v0 << 16), v11 = v1 | (v1 << 16), v10 = v1 | (v0 << 16);
        const bool odd = idx & 1;
        u32x4 r0, r1;
        r0.x = v11; r0.y = v11; r0.z = odd ? v10 : v00; r0.w = v00;
        r1.x = v11; r1.y = v11; r1.z = odd ? v11 : v10; r1.w = v00;
        putR(B, r0, r1);
      }
    }
    bar();
    STAMP(7);
    f32x4 cacc[8];
#pragma unroll
    for (int P = 0; P < 8; ++P) cacc[P] = f32x4{0.f, 0.f, 0.f, 0.f};
    {
      LDSP char* ra = sm + LDS_R + w * R_STRIDE + (l16 & 1) * R_COPY + 4 * (1024 - (l16 >> 1) + 4 * g4);
      LDSP char* ub = sm + LDS_U + w * U_STRIDE + 2 * (240 + 16 * l16 + 8 * g4);
#pragma unroll 1
      for (int r = 0; r < 8; ++r) {
        u32x4 af[8], bfr[8];
#pragma unroll
        for (int i = 0; i < 8; ++i) {
          const LDSP u32* p = (const LDSP u32*)(ra - 512 * i - 64 * r);
          af[i] = u32x4{p[0], p[1], p[2], p[3]};
          bfr[i] = *(LDSP u32x4*)(ub + 512 * i - 64 * r);
        }
#pragma unroll
        for (int P = 0; P < 8; ++P)
#pragma unroll
          for (int i = 0; i <= P; ++i) cacc[P] = mfma_h(af[i], bfr[P - i], cacc[P]);
      }
    }
    STAMP(8);
    {
      const float dsk = bf_lo(dsk_raw);
#pragma unroll
      for (int P = 0; P < 8; ++P) {
        const int tok = 256 * P + 16 * l16 + 4 * g4;
        const u32x2 uu = *(LDSP u32x2*)(sm + LDS_U + w * U_STRIDE + 512 + tok * 2);
        const u32 uux = uu.x, uuy = uu.y;
        const f16x2 u01 = __builtin_bit_cast(f16x2, uux), u23 = __builtin_bit_cast(f16x2, uuy);
        const float s0 = __builtin_fmaf((float)u01.x, dsk, cacc[P][0] * yscale), s1 = __builtin_fmaf((float)u01.y, dsk, cacc[P][1] * yscale);
        const float s2 = __builtin_fmaf((float)u23.x, dsk, cacc[P][2] * yscale), s3 = __builtin_fmaf((float)u23.y, dsk, cacc[P][3] * yscale);
        st8_sc1(ws, OFF_ST + ch * 4096 + tok * 2, u32x2{pk_bf16(s0, s1), pk_bf16(s2, s3)});
      }
    }
    wait_vm0();
    bar();
    if (tid == 0) st4_sc1(ws, OFF_FS + (b / 20) * 128 + (b % 20) * 4, tag);
  }
  STAMP(9);

  if (w == 0) poll_eq(ws, OFF_FH + (t2 >> 1) * 128 + (lane & 15) * 4, true, tag);
  bar();
  STAMP(10);
  f32x4 oacc[4][5];
  {
    u32x4 hf[4][12];
#pragma unroll
    for (int t = 0; t < 4; ++t)
#pragma unroll
      for (int s = 0; s < 12; ++s) hf[t][s] = ld16_sc1(ws, OFF_HF + ((t2 * 4 + t) * 48 + 12 * w + s) * 1024 + lane * 16);
#pragma unroll
    for (int t = 0; t < 4; ++t)
#pragma unroll
      for (int c = 0; c < 5; ++c) oacc[t][c] = f32x4{0.f, 0.f, 0.f, 0.f};
#pragma unroll
    for (int t = 0; t < 4; ++t)
#pragma unroll
      for (int s = 0; s < 12; ++s)
#pragma unroll
        for (int c = 0; c < 5; ++c) oacc[t][c] = mfma_bf(w2f[s][c], hf[t][s], oacc[t][c]);
  }
  STAMP(11);
  {
#pragma unroll
    for (int t = 0; t < 4; ++t)
#pragma unroll
      for (int c = 0; c < 5; ++c) *(LDSP f32x4*)(sm + LDS_RED + ((w * 4 + t) * 5 + c) * 1024 + lane * 16) = oacc[t][c];
    u32x2 xo[5], b2v[5];
    const int tok = t2 * 64 + w * 16 + l16;
#pragma unroll
    for (int c = 0; c < 5; ++c) {
      xo[c] = *(const u32x2*)(a.x + (size_t)tok * D + j2 * 80 + 16 * c + 4 * g4);
      b2v[c] = *(const u32x2*)(a.b2 + j2 * 80 + 16 * c + 4 * g4);
    }
    if (w == 0) poll_eq(ws, OFF_FS + j2 * 128 + (lane < 20 ? lane : 0) * 4, true, tag);
    bar();
    STAMP(12);
#pragma unroll
    for (int i = 0; i < 3; ++i) {
      const int p = tid + 256 * i;
      if (p < 640) {
        const u32x4 v = ld16_sc1(ws, OFF_ST + (j2 * 80 + (p >> 3)) * 4096 + t2 * 128 + (p & 7) * 16);
        *(LDSP u32x4*)(sm + LDS_S + (p >> 3) * 160 + (p & 7) * 16) = v;
      }
    }
    bar();
    const int tq = (lane >> 2) & 3, tp = lane & 3;
#pragma unroll
    for (int c = 0; c < 5; ++c) {
      f32x4 o = *(LDSP f32x4*)(sm + LDS_RED + ((0 * 4 + w) * 5 + c) * 1024 + lane * 16);
#pragma unroll
      for (int src = 1; src < 4; ++src) o += *(LDSP f32x4*)(sm + LDS_RED + ((src * 4 + w) * 5 + c) * 1024 + lane * 16);
      const u32x2 sv = lds_tr(sm + LDS_S + (16 * c + 4 * g4 + tq) * 160 + w * 32 + tp * 8);
      const float bb[4] = {bf_lo(b2v[c].x), bf_hi(b2v[c].x), bf_lo(b2v[c].y), bf_hi(b2v[c].y)};
      const float ss[4] = {bf_lo(sv.x), bf_hi(sv.x), bf_lo(sv.y), bf_hi(sv.y)};
      const float xx[4] = {bf_lo(xo[c].x), bf_hi(xo[c].x), bf_lo(xo[c].y), bf_hi(xo[c].y)};
      float r[4];
#pragma unroll
      for (int i = 0; i < 4; ++i) {
        const float z = o[i] + bb[i];
        const float g = z * __builtin_amdgcn_rcpf(1.0f + __builtin_amdgcn_exp2f(z * -1.44269504088896341f));
        r[i] = __builtin_fmaf(ss[i], g, xx[i]);
      }
      st8_sc1(outr, (tok * D + j2 * 80 + 16 * c + 4 * g4) * 2, u32x2{pk_bf16(r[0], r[1]), pk_bf16(r[2], r[3])});
    }
  }
  STAMP(13);
}

extern "C" void kernel_launch(void* const* d_in, const int* in_sizes, int n_in, void* d_out, int out_size, void* d_ws,
                              size_t ws_size, hipStream_t stream) {
  Args a;
  a.x = (const u16*)d_in[0]; a.logA = (const u16*)d_in[1]; a.aim = (const u16*)d_in[2]; a.cre = (const u16*)d_in[3];
  a.cim = (const u16*)d_in[4]; a.logdt = (const u16*)d_in[5]; a.dskip = (const u16*)d_in[6];
  a.w1 = (const u16*)d_in[8]; a.b1 = (const u16*)d_in[9]; a.w2 = (const u16*)d_in[10]; a.b2 = (const u16*)d_in[11];
  a.out = (u16*)d_out; a.ws = (char*)d_ws;
  fused_kernel<<<256, 256, 0, stream>>>(a);
}
```

```cpp
#include <hip/hip_runtime.h>
#include <stdint.h>

struct Args {
  const void* x; const void* logA; const void* aim; const void* cre; const void* cim; const void* logdt;
  const void* dskip; const void* w1; const void* b1; const void* w2; const void* b2; void* out; void* ws;
};

__global__ __launch_bounds__(256, 1) void fused_kernel(Args args) {
  __shared__ __attribute__((aligned(16))) char smem[163840];
  asm volatile("" ::"v"((unsigned)(uintptr_t)smem) : "memory");
  const uint64_t kernarg = (uint64_t)(uintptr_t)__builtin_amdgcn_kernarg_segment_ptr();
  const unsigned bid = blockIdx.x, tid = threadIdx.x;
  asm volatile(
      ".p2align 6\n"
      "s_getpc_b64 s[98:99]\n"
      "K2_PC0:\n"
      "s_load_dwordx16 s[4:19], %0, 0\n"
      "s_load_dwordx8 s[20:27], %0, 64\n"
      "s_load_dwordx2 s[28:29], %0, 96\n"
      "s_mov_b32 s30, %1\n"
      "v_and_b32 v1, 63, %2\n"
      "v_lshrrev_b32 v9, 6, %2\n"
      "s_nop 0\n"
      "v_readfirstlane_b32 s31, v9\n"
      "v_lshlrev_b32 v2, 4, v1\n"
      "v_lshrrev_b32 v3, 4, v1\n"
      "v_and_b32 v4, 15, v1\n"
      "v_bfe_u32 v7, v1, 2, 2\n"
      "v_and_b32 v8, 3, v1\n"
      "s_and_b32 s57, s30, 7\n"
      "s_lshr_b32 s58, s30, 3\n"
      "s_and_b32 s53, s58, 3\n"
      "s_lshl2_add_u32 s48, s57, s53\n"
      "s_lshr_b32 s49, s58, 2\n"
      "s_and_b32 s52, s49, 3\n"
      "s_add_u32 s52, s52, 4\n"
      "s_lshl2_add_u32 s53, s53, 16\n"
      "s_sub_u32 s54, s58, 20\n"
      "s_lshr_b32 s55, s54, 1\n"
      "s_and_b32 s61, s54, 1\n"
      "s_lshl_b32 s61, s61, 4\n"
      "s_sub_u32 s62, s54, 4\n"
      "s_cmp_lt_u32 s54, 8\n"
      "s_cselect_b32 s55, s55, s62\n"
      "s_cselect_b32 s61, s61, 0\n"
      "s_cmp_lt_u32 s58, 20\n"
      "s_cselect_b32 s55, s52, s55\n"
      "s_cselect_b32 s61, s53, s61\n"
      "s_and_b32 s52, s57, 1\n"
      "s_lshl3_add_u32 s60, s52, s55\n"
      "s_lshr_b32 s52, s57, 1\n"
      "s_lshl_b32 s52, s52, 5\n"
      "s_add_u32 s59, s52, s61\n"
      "v_lshlrev_b32_e64 v11, 7, v1\n"
      "s_mul_i32 s81, s31, 10240\n"
      "s_add_u32 s81, s81, 122880\n"
      "s_mov_b32 s63, 0xffff0000\n"
      "s_mov_b32 s65, 4096\n"
      "s_mov_b32 s66, 8192\n"
      "s_mov_b32 s67, 12288\n"
      "s_mov_b32 s68, 16384\n"
      "s_mov_b32 s69, 20480\n"
      "s_mov_b32 s70, 24576\n"
      "s_mov_b32 s71, 28672\n"
      "s_mov_b32 s72, 32768\n"
      "s_mov_b32 s73, 36864\n"
      "s_mov_b32 s74, 40960\n"
      "s_mov_b32 s75, 45056\n"
      "s_mov_b32 s76, 49152\n"
      "s_mov_b32 s77, 53248\n"
      "s_mov_b32 s78, 57344\n"
      "s_mov_b32 s79, 61440\n"
      "v_add_u32 v16, 0, v1\n"
      "v_mul_u32_u24 v17, 0xaaab, v16\n"
      "v_lshrrev_b32 v17, 19, v17\n"
      "v_mul_u32_u24 v18, 12, v17\n"
      "v_sub_u32 v16, v16, v18\n"
      "v_bfe_u32 v18, v17, 3, 1\n"
      "v_lshlrev_b32 v18, 1, v18\n"
      "v_xor_b32 v16, v16, v18\n"
      "v_mul_u32_u24 v17, 3072, v17\n"
      "v_lshl_add_u32 v16, v16, 4, v17\n"
      "v_subrev_u32 v13, 0, v16\n"
      "v_add_u32 v16, 64, v1\n"
      "v_mul_u32_u24 v17, 0xaaab, v16\n"
      "v_lshrrev_b32 v17, 19, v17\n"
      "v_mul_u32_u24 v18, 12, v17\n"
      "v_sub_u32 v16, v16, v18\n"
      "v_bfe_u32 v18, v17, 3, 1\n"
      "v_lshlrev_b32 v18, 1, v18\n"
      "v_xor_b32_e64 v16, v16, v18\n"
      "v_mul_u32_u24 v17, 3072, v17\n"
      "v_lshl_add_u32 v16, v16, 4, v17\n"
      "v_subrev_u32 v14, 1024, v16\n"
      "v_add_u32 v16, 128, v1\n"
      "v_mul_u32_u24 v17, 0xaaab, v16\n"
      "v_lshrrev_b32 v17, 19, v17\n"
      "v_mul_u32_u24 v18, 12, v17\n"
      "v_sub_u32 v16, v16, v18\n"
      "v_bfe_u32 v18, v17, 3, 1\n"
      "v_lshlrev_b32 v18, 1, v18\n"
      "v_xor_b32_e64 v16, v16, v18\n"
      "v_mul_u32_u24 v17, 3072, v17\n"
      "v_lshl_add_u32 v16, v16, 4, v17\n"
      "v_subrev_u32 v15, 2048, v16\n"
      "s_waitcnt lgkmcnt(0)\n"
      "s_mov_b32 s32, s28\n"
      "s_and_b32 s33, s29, 0xffff\n"
      "s_mov_b32 s34, 33554432\n"
      "s_mov_b32 s35, 0x20000\n"
      "buffer_load_dword v5, off, s[32:35], 0 sc1\n"
      "s_cmp_ge_u32 s30, 160\n"
      "s_cbranch_scc1 K2_NONCONV\n"
      "s_lshl2_add_u32 s80, s30, s31\n"
      "s_mul_i32 s52, s80, 384\n"
      "v_lshl_add_u32 v46, v1, 2, s52\n"
      "v_lshl_add_u32 v47, v1, 1, s52\n"
      "s_lshl_b32 s53, s80, 1\n"
      "v_mov_b32 v48, s53\n"
      "global_load_dword v20, v46, s[6:7]\n"
      "global_load_ushort v24, v47, s[6:7] offset:256\n"
      "global_load_dword v21, v46, s[8:9]\n"
      "global_load_ushort v25, v47, s[8:9] offset:256\n"
      "global_load_dword v22, v46, s[10:11]\n"
      "global_load_ushort v26, v47, s[10:11] offset:256\n"
      "global_load_dword v23, v46, s[12:13]\n"
      "global_load_ushort v27, v47, s[12:13] offset:256\n"
      "global_load_ushort v28, v48, s[14:15]\n"
      "global_load_ushort v9, v48, s[16:17]\n"
      "s_mul_i32 s52, s60, 192\n"
      "v_lshl_add_u32 v254, v3, 3, s52\n"
      "global_load_dwordx2 v[242:243], v254, s[20:21] offset:0\n"
      "global_load_dwordx2 v[244:245], v254, s[20:21] offset:32\n"
      "global_load_dwordx2 v[246:247], v254, s[20:21] offset:64\n"
      "global_load_dwordx2 v[248:249], v254, s[20:21] offset:96\n"
      "global_load_dwordx2 v[250:251], v254, s[20:21] offset:128\n"
      "global_load_dwordx2 v[252:253], v254, s[20:21] offset:160\n"
      "s_mov_b32 s52, K2_END-K2_PC0-4\n"
      "s_lshl_b32 s53, s31, 13\n"
      "v_lshl_add_u32 v16, v1, 7, s53\n"
      "v_min_u32 v16, s52, v16\n"
      "v_and_b32 v16, -4, v16\n"
      "global_load_dword v18, v16, s[98:99]\n"
      "s_lshl_b32 s53, s31, 13\n"
      "s_add_u32 s53, s53, 32768\n"
      "v_lshl_add_u32 v17, v1, 7, s53\n"
      "v_min_u32 v17, s52, v17\n"
      "v_and_b32 v17, -4, v17\n"
      "global_load_dword v19, v17, s[98:99]\n"
      "s_mul_i32 s52, s60, 192\n"
      "s_add_u32 s40, s18, s52\n"
      "s_addc_u32 s41, s19, 0\n"
      "s_and_b32 s41, s41, 0xffff\n"
      "s_mov_b32 s42, 1966080\n"
      "s_mov_b32 s43, 0x20000\n"
      "s_sub_u32 s42, s42, s52\n"
      "s_mul_i32 s50, s31, 49152\n"
      "s_mul_i32 s51, s31, 3072\n"
      "s_cmp_lt_u32 s58, 16\n"
      "s_cselect_b32 s42, s42, 0\n"
      "s_lshl2_add_u32 s52, s58, s31\n"
      "s_mul_i32 s52, s52, 24576\n"
      "s_and_b32 s53, s57, 1\n"
      "s_mul_i32 s53, s53, 1536\n"
      "s_add_u32 s52, s52, s53\n"
      "v_and_b32 v238, 7, v1\n"
      "v_min_u32 v238, 5, v238\n"
      "v_lshlrev_b32 v238, 7, v238\n"
      "v_lshrrev_b32 v239, 3, v1\n"
      "v_mul_u32_u24 v239, 3072, v239\n"
      "v_add3_u32 v238, v238, v239, s52\n"
      "global_load_dword v239, v238, s[18:19]\n"
      "global_load_dword v237, v238, s[18:19] offset:768\n"
      "s_mov_b32 m0, s51\n"
      "s_nop 0\n"
      "buffer_load_dwordx4 v13, s[40:43], s50 offen offset:0 lds\n"
      "buffer_load_dwordx4 v14, s[40:43], s50 offen offset:1024 lds\n"
      "buffer_load_dwordx4 v15, s[40:43], s50 offen offset:2048 lds\n"
      "s_add_u32 s50, s50, 196608\n"
      "s_add_u32 s51, s51, 12288\n"
      "s_mov_b32 m0, s51\n"
      "s_nop 0\n"
      "buffer_load_dwordx4 v13, s[40:43], s50 offen offset:0 lds\n"
      "buffer_load_dwordx4 v14, s[40:43], s50 offen offset:1024 lds\n"
      "buffer_load_dwordx4 v15, s[40:43], s50 offen offset:2048 lds\n"
      "s_add_u32 s50, s50, 196608\n"
      "s_add_u32 s51, s51, 12288\n"
      "s_add_u32 s94, s59, s31\n"
      "s_lshl_b32 s93, s94, 8\n"
      "s_add_u32 s93, s93, 98304\n"
      "s_mul_i32 s53, s94, 20480\n"
      "s_add_u32 s53, s53, 1048576\n"
      "v_add_u32 v241, s53, v2\n"
      "s_mov_b32 s82, 0x3fb8aa3b\n"
      "s_mov_b32 s83, 0x32a57060\n"
      "s_mov_b32 s85, 0x3e22f983\n"
      "s_waitcnt vmcnt(17)\n"
      "v_lshlrev_b32 v28, 16, v28\n"
      "v_lshlrev_b32 v36, 16, v22\n"
      "v_mul_f32 v44, s82, v28\n"
      "v_lshlrev_b32 v37, 16, v23\n"
      "v_rndne_f32 v44, v44\n"
      "v_and_b32 v38, s63, v22\n"
      "v_fma_f32 v45, v28, s82, -v44\n"
      "v_cvt_i32_f32 v44, v44\n"
      "v_fma_f32 v45, v28, s83, v45\n"
      "v_and_b32 v39, s63, v23\n"
      "v_exp_f32 v45, v45\n"
      "v_lshlrev_b32 v40, 16, v26\n"
      "v_lshlrev_b32 v41, 16, v27\n"
      "v_lshlrev_b32 v64, 16, v20\n"
      "v_ldexp_f32 v42, v45, v44\n"
      "v_and_b32 v68, s63, v20\n"
      "v_cvt_pk_bf16_f32 v42, v42, v42\n"
      "v_lshlrev_b32 v72, 16, v24\n"
      "v_and_b32 v42, s63, v42\n"
      "v_lshrrev_b32 v52, 1, v1\n"
      "v_mul_f32 v36, v36, v42\n"
      "v_mul_f32 v37, v37, v42\n"
      "v_mul_f32 v38, v38, v42\n"
      "v_mul_f32 v39, v39, v42\n"
      "v_max3_f32 v43, |v36|, |v37|, 0\n"
      "v_mul_f32 v40, v40, v42\n"
      "v_mul_f32 v41, v41, v42\n"
      "v_max3_f32 v43, |v38|, |v39|, v43\n"
      "v_mul_f32 v65, s82, v64\n"
      "v_max3_f32 v43, |v40|, |v41|, v43\n"
      "v_mul_f32 v69, s82, v68\n"
      "v_mul_f32 v73, s82, v72\n"
      "v_max_f32_dpp v43, v43, v43 quad_perm:[1,0,3,2] row_mask:0xf bank_mask:0xf bound_ctrl:1\n"
      "v_lshlrev_b32 v51, 6, v1\n"
      "v_add_u32 v53, 0, v52\n"
      "v_max_f32_dpp v43, v43, v43 quad_perm:[2,3,0,1] row_mask:0xf bank_mask:0xf bound_ctrl:1\n"
      "v_rndne_f32 v65, v65\n"
      "v_rndne_f32 v69, v69\n"
      "v_max_f32_dpp v43, v43, v43 row_half_mirror row_mask:0xf bank_mask:0xf bound_ctrl:1\n"
      "v_rndne_f32 v73, v73\n"
      "v_add_u32 v51, s81, v51\n"
      "v_max_f32_dpp v43, v43, v43 row_mirror row_mask:0xf bank_mask:0xf bound_ctrl:1\n"
      "v_and_b32 v53, 3, v53\n"
      "v_mov_b32 v45, v43\n"
      "v_fma_f32 v66, v64, s82, -v65\n"
      "v_fma_f32 v70, v68, s82, -v69\n"
      "v_fma_f32 v74, v72, s82, -v73\n"
      "v_permlane16_swap_b32 v45, v43\n"
      "v_lshl_add_u32 v46, v53, 4, v51\n"
      "v_fma_f32 v66, v64, s83, v66\n"
      "v_fma_f32 v70, v68, s83, v70\n"
      "v_fma_f32 v74, v72, s83, v74\n"
      "v_max_f32 v43, v45, v43\n"
      "v_add_u32 v53, 1, v52\n"
      "v_and_b32 v55, 1, v7\n"
      "v_exp_f32 v66, v66\n"
      "v_exp_f32 v70, v70\n"
      "v_exp_f32 v74, v74\n"
      "v_mov_b32 v45, v43\n"
      "v_and_b32 v53, 3, v53\n"
      "v_lshrrev_b32 v54, 1, v7\n"
      "v_lshlrev_b32 v55, 1, v55\n"
      "v_lshrrev_b32 v56, 1, v8\n"
      "v_cvt_i32_f32 v65, v65\n"
      "v_cvt_i32_f32 v69, v69\n"
      "v_cvt_i32_f32 v73, v73\n"
      "v_lshl_add_u32 v47, v53, 4, v51\n"
      "v_permlane32_swap_b32 v45, v43\n"
      "v_add_u32_e64 v53, 2, v52\n"
      "v_lshl_add_u32 v54, v3, 1, v54\n"
      "v_add3_u32 v55, v55, v56, v3\n"
      "v_ldexp_f32 v64, v66, v65\n"
      "v_ldexp_f32 v68, v70, v69\n"
      "v_ldexp_f32 v72, v74, v73\n"
      "v_max_f32 v43, v45, v43\n"
      "v_and_b32 v53, 3, v53\n"
      "v_lshlrev_b32 v54, 6, v54\n"
      "v_and_b32 v55, 3, v55\n"
      "v_cvt_pk_bf16_f32 v64, v64, v64\n"
      "v_cvt_pk_bf16_f32 v68, v68, v68\n"
      "v_cvt_pk_bf16_f32 v72, v72, v72\n"
      "v_frexp_exp_i32_f32 v45, v43\n"
      "v_lshl_add_u32 v48, v53, 4, v51\n"
      "v_lshl_add_u32 v54, v55, 4, v54\n"
      "v_and_b32 v64, s63, v64\n"
      "v_lshlrev_b32 v67, 16, v21\n"
      "v_and_b32 v68, s63, v68\n"
      "v_and_b32 v71, s63, v21\n"
      "v_and_b32 v72, s63, v72\n"
      "v_lshlrev_b32 v75, 16, v25\n"
      "v_add_u32 v53, 3, v52\n"
      "v_and_b32 v55, 1, v8\n"
      "v_ldexp_f32 v10, 2.0, v45\n"
      "v_mul_f32 v64, v64, v42\n"
      "v_mul_f32 v67, v67, v42\n"
      "v_mul_f32 v68, v68, v42\n"
      "v_mul_f32 v71, v71, v42\n"
      "v_mul_f32 v72, v72, v42\n"
      "v_mul_f32 v75, v75, v42\n"
      "v_sub_u32 v45, 0, v45\n"
      "v_and_b32 v53, 3, v53\n"
      "v_lshl_add_u32 v54, v55, 3, v54\n"
      "v_mul_f32 v30, 0xbfb8aa3b, v64\n"
      "v_mul_f32 v33, s85, v67\n"
      "v_mul_f32 v31, 0xbfb8aa3b, v68\n"
      "v_mul_f32 v34, s85, v71\n"
      "v_mul_f32 v32, 0xbfb8aa3b, v72\n"
      "v_mul_f32 v35, s85, v75\n"
      "v_ldexp_f32 v36, v36, v45\n"
      "v_ldexp_f32 v37, v37, v45\n"
      "v_ldexp_f32 v38, v38, v45\n"
      "v_ldexp_f32 v39, v39, v45\n"
      "v_ldexp_f32 v40, v40, v45\n"
      "v_ldexp_f32 v41, v41, v45\n"
      "s_waitcnt vmcnt(16)\n"
      "v_lshlrev_b32 v9, 16, v9\n"
      "v_lshl_add_u32 v49, v53, 4, v51\n"
      "v_add_u32 v50, s81, v54\n"
      "v_mul_f32 v97, 0x41000000, v33\n"
      "v_mul_f32 v96, 0x41000000, v30\n"
      "v_fract_f32 v97, v97\n"
      "v_exp_f32 v96, v96\n"
      "v_cos_f32 v78, v97\n"
      "v_sin_f32 v79, v97\n"
      "v_fract_f32 v99, v33\n"
      "v_exp_f32 v98, v30\n"
      "v_cos_f32 v76, v99\n"
      "v_pk_mul_f32 v[78:79], v[78:79], v[96:97] op_sel_hi:[1,0] neg_hi:[1,0]\n"
      "v_sin_f32_e64 v77, v99\n"
      "v_pk_mul_f32 v[66:67], v[78:79], v[78:79] op_sel:[1,1] op_sel_hi:[1,0] neg_lo:[1,0]\n"
      "v_mul_f32 v101, 0x43000000, v33\n"
      "v_pk_fma_f32 v[186:187], v[78:79], v[78:79], v[66:67] op_sel:[0,0,0] op_sel_hi:[0,1,1]\n"
      "v_pk_mul_f32 v[76:77], v[76:77], v[98:99] op_sel_hi:[1,0]\n"
      "v_pk_mul_f32 v[68:69], v[186:187], v[78:79] op_sel:[1,1] op_sel_hi:[1,0] neg_lo:[1,0]\n"
      "v_mul_f32 v100, 0x43000000, v30\n"
      "v_pk_fma_f32 v[188:189], v[186:187], v[78:79], v[68:69] op_sel:[0,0,0] op_sel_hi:[0,1,1]\n"
      "v_fract_f32 v101, v101\n"
      "v_pk_mul_f32 v[70:71], v[188:189], v[78:79] op_sel:[1,1] op_sel_hi:[1,0] neg_lo:[1,0]\n"
      "s_mov_b32 m0, s51\n"
      "s_nop 0\n"
      "buffer_load_dwordx4 v13, s[40:43], s50 offen offset:0 lds\n"
      "buffer_load_dwordx4 v14, s[40:43], s50 offen offset:1024 lds\n"
      "buffer_load_dwordx4 v15, s[40:43], s50 offen offset:2048 lds\n"
      "s_add_u32 s50, s50, 196608\n"
      "s_add_u32 s51, s51, 12288\n"
      "v_exp_f32 v100, v100\n"
      "v_pk_fma_f32 v[190:191], v[188:189], v[78:79], v[70:71] op_sel:[0,0,0] op_sel_hi:[0,1,1]\n"
      "v_cos_f32 v80, v101\n"
      "v_pk_mul_f32 v[72:73], v[190:191], v[78:79] op_sel:[1,1] op_sel_hi:[1,0] neg_lo:[1,0]\n"
      "v_sin_f32 v81, v101\n"
      "v_pk_fma_f32 v[192:193], v[190:191], v[78:79], v[72:73] op_sel:[0,0,0] op_sel_hi:[0,1,1]\n"
      "v_cvt_pk_f16_f32 v103, v186, v188\n"
      "v_pk_mul_f32 v[74:75], v[192:193], v[78:79] op_sel:[1,1] op_sel_hi:[1,0] neg_lo:[1,0]\n"
      "v_pk_mul_f32 v[80:81], v[80:81], v[100:101] op_sel_hi:[1,0]\n"
      "v_pk_fma_f32 v[194:195], v[192:193], v[78:79], v[74:75] op_sel:[0,0,0] op_sel_hi:[0,1,1]\n"
      "v_cvt_pk_f16_f32 v107, v187, v189\n"
      "v_pk_mul_f32 v[64:65], v[194:195], v[78:79] op_sel:[1,1] op_sel_hi:[1,0] neg_lo:[1,0]\n"
      "v_cvt_pk_f16_f32 v104, v190, v192\n"
      "v_pk_fma_f32 v[196:197], v[194:195], v[78:79], v[64:65] op_sel:[0,0,0] op_sel_hi:[0,1,1]\n"
      "v_cvt_pk_f16_f32 v108, v191, v193\n"
      "v_pk_mul_f32 v[66:67], v[196:197], v[78:79] op_sel:[1,1] op_sel_hi:[1,0] neg_lo:[1,0]\n"
      "v_cvt_pk_f16_f32 v105, v194, v196\n"
      "v_pk_fma_f32 v[198:199], v[196:197], v[78:79], v[66:67] op_sel:[0,0,0] op_sel_hi:[0,1,1]\n"
      "v_cvt_pk_f16_f32 v109, v195, v197\n"
      "v_pk_mul_f32 v[68:69], v[198:199], v[78:79] op_sel:[1,1] op_sel_hi:[1,0] neg_lo:[1,0]\n"
      "v_mul_f32 v97, 0x41000000, v34\n"
      "v_pk_fma_f32 v[200:201], v[198:199], v[78:79], v[68:69] op_sel:[0,0,0] op_sel_hi:[0,1,1]\n"
      "v_mul_f32 v96, 0x41000000, v31\n"
      "v_pk_mul_f32 v[70:71], v[200:201], v[78:79] op_sel:[1,1] op_sel_hi:[1,0] neg_lo:[1,0]\n"
      "v_cvt_pk_f16_f32 v118, v198, v200\n"
      "v_pk_fma_f32 v[202:203], v[200:201], v[78:79], v[70:71] op_sel:[0,0,0] op_sel_hi:[0,1,1]\n"
      "v_pk_mul_f32 v[70:71], v[36:37], v[76:77] op_sel:[1,1] op_sel_hi:[1,0] neg_lo:[1,0]\n"
      "v_pk_mul_f32 v[72:73], v[202:203], v[78:79] op_sel:[1,1] op_sel_hi:[1,0] neg_lo:[1,0]\n"
      "v_pk_fma_f32 v[216:217], v[36:37], v[76:77], v[70:71] op_sel:[0,0,0] op_sel_hi:[0,1,1]\n"
      "v_pk_fma_f32 v[204:205], v[202:203], v[78:79], v[72:73] op_sel:[0,0,0] op_sel_hi:[0,1,1]\n"
      "v_pk_mul_f32 v[72:73], v[216:217], v[76:77] op_sel:[1,1] op_sel_hi:[1,0] neg_lo:[1,0]\n"
      "v_pk_mul_f32 v[74:75], v[204:205], v[78:79] op_sel:[1,1] op_sel_hi:[1,0] neg_lo:[1,0]\n"
      "v_pk_fma_f32 v[218:219], v[216:217], v[76:77], v[72:73] op_sel:[0,0,0] op_sel_hi:[0,1,1]\n"
      "v_pk_fma_f32 v[206:207], v[204:205], v[78:79], v[74:75] op_sel:[0,0,0] op_sel_hi:[0,1,1]\n"
      "v_pk_mul_f32 v[72:73], v[36:37], v[80:81] op_sel:[1,1] op_sel_hi:[1,0] neg_lo:[1,0]\n"
      "v_pk_mul_f32 v[74:75], v[218:219], v[76:77] op_sel:[1,1] op_sel_hi:[1,0] neg_lo:[1,0]\n"
      "v_pk_mul_f32 v[64:65], v[206:207], v[78:79] op_sel:[1,1] op_sel_hi:[1,0] neg_lo:[1,0]\n"
      "v_pk_fma_f32 v[214:215], v[36:37], v[80:81], v[72:73] op_sel:[0,0,0] op_sel_hi:[0,1,1]\n"
      "v_pk_fma_f32 v[220:221], v[218:219], v[76:77], v[74:75] op_sel:[0,0,0] op_sel_hi:[0,1,1]\n"
      "v_pk_fma_f32 v[208:209], v[206:207], v[78:79], v[64:65] op_sel:[0,0,0] op_sel_hi:[0,1,1]\n"
      "v_pk_mul_f32 v[74:75], v[214:215], v[76:77] op_sel:[1,1] op_sel_hi:[1,0] neg_lo:[1,0]\n"
      "v_pk_mul_f32 v[64:65], v[220:221], v[76:77] op_sel:[1,1] op_sel_hi:[1,0] neg_lo:[1,0]\n"
      "v_pk_mul_f32 v[66:67], v[208:209], v[78:79] op_sel:[1,1] op_sel_hi:[1,0] neg_lo:[1,0]\n"
      "v_pk_fma_f32 v[184:185], v[214:215], v[76:77], v[74:75] op_sel:[0,0,0] op_sel_hi:[0,1,1]\n"
      "v_pk_fma_f32 v[222:223], v[220:221], v[76:77], v[64:65] op_sel:[0,0,0] op_sel_hi:[0,1,1]\n"
      "v_pk_fma_f32 v[210:211], v[208:209], v[78:79], v[66:67] op_sel:[0,0,0] op_sel_hi:[0,1,1]\n"
      "v_pk_mul_f32 v[64:65], v[184:185], v[76:77] op_sel:[1,1] op_sel_hi:[1,0] neg_lo:[1,0]\n"
      "v_pk_mul_f32 v[66:67], v[222:223], v[76:77] op_sel:[1,1] op_sel_hi:[1,0] neg_lo:[1,0]\n"
      "v_pk_mul_f32 v[68:69], v[210:211], v[78:79] op_sel:[1,1] op_sel_hi:[1,0] neg_lo:[1,0]\n"
      "v_pk_fma_f32 v[186:187], v[184:185], v[76:77], v[64:65] op_sel:[0,0,0] op_sel_hi:[0,1,1]\n"
      "v_pk_fma_f32 v[224:225], v[222:223], v[76:77], v[66:67] op_sel:[0,0,0] op_sel_hi:[0,1,1]\n"
      "v_pk_fma_f32 v[212:213], v[210:211], v[78:79], v[68:69] op_sel:[0,0,0] op_sel_hi:[0,1,1]\n"
      "v_pk_mul_f32 v[66:67], v[186:187], v[76:77] op_sel:[1,1] op_sel_hi:[1,0] neg_lo:[1,0]\n"
      "v_pk_mul_f32 v[68:69], v[224:225], v[76:77] op_sel:[1,1] op_sel_hi:[1,0] neg_lo:[1,0]\n"
      "v_pk_fma_f32 v[188:189], v[186:187], v[76:77], v[66:67] op_sel:[0,0,0] op_sel_hi:[0,1,1]\n"
      "v_pk_fma_f32 v[226:227], v[224:225], v[76:77], v[68:69] op_sel:[0,0,0] op_sel_hi:[0,1,1]\n"
      "v_pk_mul_f32 v[68:69], v[188:189], v[76:77] op_sel:[1,1] op_sel_hi:[1,0] neg_lo:[1,0]\n"
      "v_pk_mul_f32 v[70:71], v[226:227], v[76:77] op_sel:[1,1] op_sel_hi:[1,0] neg_lo:[1,0]\n"
      "v_pk_fma_f32 v[190:191], v[188:189], v[76:77], v[68:69] op_sel:[0,0,0] op_sel_hi:[0,1,1]\n"
      "v_pk_fma_f32 v[228:229], v[226:227], v[76:77], v[70:71] op_sel:[0,0,0] op_sel_hi:[0,1,1]\n"
      "v_pk_mul_f32 v[70:71], v[190:191], v[76:77] op_sel:[1,1] op_sel_hi:[1,0] neg_lo:[1,0]\n"
      "v_pk_mul_f32 v[68:69], v[214:215], v[80:81] op_sel:[1,1] op_sel_hi:[1,0] neg_lo:[1,0]\n"
      "v_pk_fma_f32 v[192:193], v[190:191], v[76:77], v[70:71] op_sel:[0,0,0] op_sel_hi:[0,1,1]\n"
      "v_cvt_pk_f16_f32 v122, v199, v201\n"
      "v_pk_mul_f32 v[64:65], v[76:77], v[76:77] op_sel:[1,1] op_sel_hi:[1,0] neg_lo:[1,0]\n"
      "v_pk_mul_f32 v[72:73], v[192:193], v[76:77] op_sel:[1,1] op_sel_hi:[1,0] neg_lo:[1,0]\n"
      "v_pk_fma_f32 v[198:199], v[214:215], v[80:81], v[68:69] op_sel:[0,0,0] op_sel_hi:[0,1,1]\n"
      "v_pk_fma_f32 v[82:83], v[76:77], v[76:77], v[64:65] op_sel:[0,0,0] op_sel_hi:[0,1,1]\n"
      "v_pk_fma_f32 v[194:195], v[192:193], v[76:77], v[72:73] op_sel:[0,0,0] op_sel_hi:[0,1,1]\n"
      "v_pk_mul_f32 v[70:71], v[198:199], v[80:81] op_sel:[1,1] op_sel_hi:[1,0] neg_lo:[1,0]\n"
      "v_cvt_pk_f16_f32 v120, v206, v208\n"
      "v_cvt_pk_f16_f32 v124, v207, v209\n"
      "v_pk_mul_f32 v[72:73], v[198:199], v[82:83] op_sel:[1,1] op_sel_hi:[1,0] neg_lo:[1,0]\n"
      "v_pk_mul_f32 v[74:75], v[194:195], v[76:77] op_sel:[1,1] op_sel_hi:[1,0] neg_lo:[1,0]\n"
      "v_pk_fma_f32 v[206:207], v[198:199], v[80:81], v[70:71] op_sel:[0,0,0] op_sel_hi:[0,1,1]\n"
      "v_pk_fma_f32 v[200:201], v[198:199], v[82:83], v[72:73] op_sel:[0,0,0] op_sel_hi:[0,1,1]\n"
      "v_pk_fma_f32 v[196:197], v[194:195], v[76:77], v[74:75] op_sel:[0,0,0] op_sel_hi:[0,1,1]\n"
      "v_pk_mul_f32 v[72:73], v[206:207], v[80:81] op_sel:[1,1] op_sel_hi:[1,0] neg_lo:[1,0]\n"
      "v_cvt_pk_f16_f32 v126, v214, v184\n"
      "v_cvt_pk_f16_f32 v130, v215, v185\n"
      "s_mov_b32 m0, s51\n"
      "s_nop 0\n"
      "buffer_load_dwordx4 v13, s[40:43], s50 offen offset:0 lds\n"
      "buffer_load_dwordx4 v14, s[40:43], s50 offen offset:1024 lds\n"
      "buffer_load_dwordx4 v15, s[40:43], s50 offen offset:2048 lds\n"
      "s_add_u32 s50, s50, 196608\n"
      "s_add_u32 s51, s51, 12288\n"
      "v_pk_mul_f32 v[74:75], v[206:207], v[82:83] op_sel:[1,1] op_sel_hi:[1,0] neg_lo:[1,0]\n"
      "v_pk_fma_f32 v[214:215], v[206:207], v[80:81], v[72:73] op_sel:[0,0,0] op_sel_hi:[0,1,1]\n"
      "v_pk_fma_f32 v[208:209], v[206:207], v[82:83], v[74:75] op_sel:[0,0,0] op_sel_hi:[0,1,1]\n"
      "v_pk_mul_f32 v[74:75], v[214:215], v[80:81] op_sel:[1,1] op_sel_hi:[1,0] neg_lo:[1,0]\n"
      "v_cvt_pk_f16_f32 v111, v218, v220\n"
      "v_cvt_pk_f16_f32 v115, v219, v221\n"
      "v_pk_mul_f32 v[64:65], v[200:201], v[82:83] op_sel:[1,1] op_sel_hi:[1,0] neg_lo:[1,0]\n"
      "v_cvt_pk_f16_f32 v119, v202, v204\n"
      "v_cvt_pk_f16_f32 v123, v203, v205\n"
      "v_pk_mul_f32 v[66:67], v[82:83], v[82:83] op_sel:[1,1] op_sel_hi:[1,0] neg_lo:[1,0]\n"
      "v_pk_fma_f32 v[218:219], v[214:215], v[80:81], v[74:75] op_sel:[0,0,0] op_sel_hi:[0,1,1]\n"
      "v_pk_fma_f32 v[202:203], v[200:201], v[82:83], v[64:65] op_sel:[0,0,0] op_sel_hi:[0,1,1]\n"
      "v_pk_fma_f32 v[84:85], v[82:83], v[82:83], v[66:67] op_sel:[0,0,0] op_sel_hi:[0,1,1]\n"
      "v_pk_mul_f32 v[64:65], v[218:219], v[80:81] op_sel:[1,1] op_sel_hi:[1,0] neg_lo:[1,0]\n"
      "v_cvt_pk_f16_f32 v112, v222, v224\n"
      "v_cvt_pk_f16_f32 v116, v223, v225\n"
      "v_pk_mul_f32 v[66:67], v[208:209], v[82:83] op_sel:[1,1] op_sel_hi:[1,0] neg_lo:[1,0]\n"
      "v_cvt_pk_f16_f32 v121, v210, v212\n"
      "v_cvt_pk_f16_f32 v125, v211, v213\n"
      "v_pk_fma_f32 v[222:223], v[218:219], v[80:81], v[64:65] op_sel:[0,0,0] op_sel_hi:[0,1,1]\n"
      "v_pk_fma_f32 v[210:211], v[208:209], v[82:83], v[66:67] op_sel:[0,0,0] op_sel_hi:[0,1,1]\n"
      "v_pk_mul_f32 v[66:67], v[222:223], v[80:81] op_sel:[1,1] op_sel_hi:[1,0] neg_lo:[1,0]\n"
      "v_cvt_pk_f16_f32 v113, v226, v228\n"
      "v_cvt_pk_f16_f32 v117, v227, v229\n"
      "v_pk_fma_f32 v[226:227], v[222:223], v[80:81], v[66:67] op_sel:[0,0,0] op_sel_hi:[0,1,1]\n"
      "v_pk_mul_f32 v[68:69], v[202:203], v[82:83] op_sel:[1,1] op_sel_hi:[1,0] neg_lo:[1,0]\n"
      "v_pk_mul_f32 v[64:65], v[226:227], v[80:81] op_sel:[1,1] op_sel_hi:[1,0] neg_lo:[1,0]\n"
      "v_pk_fma_f32 v[204:205], v[202:203], v[82:83], v[68:69] op_sel:[0,0,0] op_sel_hi:[0,1,1]\n"
      "v_pk_fma_f32 v[182:183], v[226:227], v[80:81], v[64:65] op_sel:[0,0,0] op_sel_hi:[0,1,1]\n"
      "v_pk_mul_f32 v[68:69], v[214:215], v[84:85] op_sel:[1,1] op_sel_hi:[1,0] neg_lo:[1,0]\n"
      "v_pk_mul_f32 v[66:67], v[182:183], v[80:81] op_sel:[1,1] op_sel_hi:[1,0] neg_lo:[1,0]\n"
      "v_cvt_pk_f16_f32 v110, v36, v216\n"
      "v_cvt_pk_f16_f32 v114, v37, v217\n"
      "v_pk_mul_f32 v[70:71], v[210:211], v[82:83] op_sel:[1,1] op_sel_hi:[1,0] neg_lo:[1,0]\n"
      "v_pk_fma_f32 v[184:185], v[182:183], v[80:81], v[66:67] op_sel:[0,0,0] op_sel_hi:[0,1,1]\n"
      "v_pk_fma_f32 v[216:217], v[214:215], v[84:85], v[68:69] op_sel:[0,0,0] op_sel_hi:[0,1,1]\n"
      "v_fract_f32_e64 v97, v97\n"
      "v_cvt_pk_f16_f32 v102, 1.0, v78\n"
      "v_pk_fma_f32 v[212:213], v[210:211], v[82:83], v[70:71] op_sel:[0,0,0] op_sel_hi:[0,1,1]\n"
      "v_pk_mul_f32 v[68:69], v[184:185], v[80:81] op_sel:[1,1] op_sel_hi:[1,0] neg_lo:[1,0]\n"
      "v_cvt_pk_f16_f32 v106, 0, v79\n"
      "v_cvt_pk_f16_f32 v127, v186, v188\n"
      "v_cvt_pk_f16_f32 v131, v187, v189\n"
      "v_pk_mul_f32 v[70:71], v[218:219], v[84:85] op_sel:[1,1] op_sel_hi:[1,0] neg_lo:[1,0]\n"
      "v_exp_f32 v96, v96\n"
      "v_cos_f32 v88, v97\n"
      "v_sin_f32_e64 v89, v97\n"
      "ds_write_b128 v46, v[102:105] offset:0\n"
      "v_pk_fma_f32 v[186:187], v[184:185], v[80:81], v[68:69] op_sel:[0,0,0] op_sel_hi:[0,1,1]\n"
      "v_pk_fma_f32 v[220:221], v[218:219], v[84:85], v[70:71] op_sel:[0,0,0] op_sel_hi:[0,1,1]\n"
      "v_pk_mul_f32 v[70:71], v[186:187], v[80:81] op_sel:[1,1] op_sel_hi:[1,0] neg_lo:[1,0]\n"
      "v_pk_mul_f32 v[72:73], v[222:223], v[84:85] op_sel:[1,1] op_sel_hi:[1,0] neg_lo:[1,0]\n"
      "v_pk_mul_f32 v[88:89], v[88:89], v[96:97] op_sel_hi:[1,0] neg_hi:[1,0]\n"
      "v_pk_fma_f32 v[188:189], v[186:187], v[80:81], v[70:71] op_sel:[0,0,0] op_sel_hi:[0,1,1]\n"
      "v_pk_fma_f32 v[224:225], v[222:223], v[84:85], v[72:73] op_sel:[0,0,0] op_sel_hi:[0,1,1]\n"
      "v_pk_mul_f32 v[70:71], v[88:89], v[88:89] op_sel:[1,1] op_sel_hi:[1,0] neg_lo:[1,0]\n"
      "v_pk_mul_f32 v[72:73], v[188:189], v[80:81] op_sel:[1,1] op_sel_hi:[1,0] neg_lo:[1,0]\n"
      "v_cvt_pk_f16_f32 v128, v190, v192\n"
      "v_cvt_pk_f16_f32 v132, v191, v193\n"
      "v_pk_mul_f32 v[74:75], v[226:227], v[84:85] op_sel:[1,1] op_sel_hi:[1,0] neg_lo:[1,0]\n"
      "ds_write_b128 v48, v[106:109] offset:0\n"
      "v_pk_fma_f32 v[190:191], v[188:189], v[80:81], v[72:73] op_sel:[0,0,0] op_sel_hi:[0,1,1]\n"
      "v_pk_fma_f32 v[228:229], v[226:227], v[84:85], v[74:75] op_sel:[0,0,0] op_sel_hi:[0,1,1]\n"
      "v_pk_mul_f32 v[74:75], v[190:191], v[80:81] op_sel:[1,1] op_sel_hi:[1,0] neg_lo:[1,0]\n"
      "v_cvt_pk_f16_f32 v129, v194, v196\n"
      "v_pk_fma_f32 v[192:193], v[190:191], v[80:81], v[74:75] op_sel:[0,0,0] op_sel_hi:[0,1,1]\n"
      "v_cvt_pk_f16_f32 v133, v195, v197\n"
      "v_pk_mul_f32 v[64:65], v[192:193], v[80:81] op_sel:[1,1] op_sel_hi:[1,0] neg_lo:[1,0]\n"
      "v_cvt_pk_f16_f32 v102, v198, v200\n"
      "v_pk_fma_f32 v[194:195], v[192:193], v[80:81], v[64:65] op_sel:[0,0,0] op_sel_hi:[0,1,1]\n"
      "v_cvt_pk_f16_f32 v106, v199, v201\n"
      "v_pk_mul_f32 v[66:67], v[194:195], v[80:81] op_sel:[1,1] op_sel_hi:[1,0] neg_lo:[1,0]\n"
      "v_fract_f32 v99, v34\n"
      "ds_write_b128 v46, v[110:113] offset:4096\n"
      "v_pk_fma_f32 v[196:197], v[194:195], v[80:81], v[66:67] op_sel:[0,0,0] op_sel_hi:[0,1,1]\n"
      "v_exp_f32 v98, v31\n"
      "v_pk_mul_f32 v[68:69], v[196:197], v[80:81] op_sel:[1,1] op_sel_hi:[1,0] neg_lo:[1,0]\n"
      "v_cos_f32 v86, v99\n"
      "v_pk_fma_f32 v[198:199], v[196:197], v[80:81], v[68:69] op_sel:[0,0,0] op_sel_hi:[0,1,1]\n"
      "v_sin_f32 v87, v99\n"
      "s_mov_b32 m0, s51\n"
      "s_nop 0\n"
      "buffer_load_dwordx4 v13, s[40:43], s50 offen offset:0 lds\n"
      "buffer_load_dwordx4 v14, s[40:43], s50 offen offset:1024 lds\n"
      "buffer_load_dwordx4 v15, s[40:43], s50 offen offset:2048 lds\n"
      "s_add_u32 s50, s50, 196608\n"
      "s_add_u32 s51, s51, 12288\n"
      "v_mov_b32_e64 v45, v198\n"
      "v_mul_f32 v101, 0x43000000, v34\n"
      "v_cvt_pk_f16_f32 v103, v202, v204\n"
      "v_cvt_pk_f16_f32 v107, v203, v205\n"
      "v_pk_mul_f32 v[86:87], v[86:87], v[98:99] op_sel_hi:[1,0]\n"
      "v_mul_f32 v100, 0x43000000, v31\n"
      "ds_write_b128 v48, v[114:117] offset:4096\n"
      "v_fract_f32 v101, v101\n"
      "v_cvt_pk_f16_f32 v110, v214, v216\n"
      "v_cvt_pk_f16_f32 v114, v215, v217\n"
      "v_exp_f32 v100, v100\n"
      "v_cos_f32 v90, v101\n"
      "v_sin_f32 v91, v101\n"
      "v_cvt_pk_f16_f32 v111, v218, v220\n"
      "v_cvt_pk_f16_f32 v115, v219, v221\n"
      "v_cvt_pk_f16_f32 v104, v206, v208\n"
      "v_pk_mul_f32 v[90:91], v[90:91], v[100:101] op_sel_hi:[1,0]\n"
      "v_cvt_pk_f16_f32 v108, v207, v209\n"
      "v_cvt_pk_f16_f32 v112, v222, v224\n"
      "ds_write_b128 v47, v[118:121] offset:0\n"
      "v_cvt_pk_f16_f32 v119, v186, v188\n"
      "v_cvt_pk_f16_f32 v120, v190, v192\n"
      "v_cvt_pk_f16_f32 v121, v194, v196\n"
      "v_cvt_pk_f16_f32 v118, v182, v184\n"
      "v_cvt_pk_f16_f32 v116, v223, v225\n"
      "v_cvt_pk_f16_f32 v105, v210, v212\n"
      "v_cvt_pk_f16_f32 v109, v211, v213\n"
      "v_cvt_pk_f16_f32 v113, v226, v228\n"
      "v_cvt_pk_f16_f32 v117, v227, v229\n"
      "v_mul_f32 v97, 0x41000000, v35\n"
      "v_mul_f32 v96, 0x41000000, v32\n"
      "v_fract_f32_e64 v97, v97\n"
      "ds_write_b128 v49, v[122:125] offset:0\n"
      "v_cvt_pk_f16_f32 v123, v187, v189\n"
      "v_pk_fma_f32 v[186:187], v[88:89], v[88:89], v[70:71] op_sel:[0,0,0] op_sel_hi:[0,1,1]\n"
      "v_cvt_pk_f16_f32 v124, v191, v193\n"
      "v_pk_mul_f32 v[72:73], v[186:187], v[88:89] op_sel:[1,1] op_sel_hi:[1,0] neg_lo:[1,0]\n"
      "v_cvt_pk_f16_f32 v125, v195, v197\n"
      "v_pk_fma_f32 v[188:189], v[186:187], v[88:89], v[72:73] op_sel:[0,0,0] op_sel_hi:[0,1,1]\n"
      "v_cvt_pk_f16_f32 v122, v183, v185\n"
      "v_pk_mul_f32 v[74:75], v[188:189], v[88:89] op_sel:[1,1] op_sel_hi:[1,0] neg_lo:[1,0]\n"
      "v_exp_f32 v96, v96\n"
      "v_pk_fma_f32 v[190:191], v[188:189], v[88:89], v[74:75] op_sel:[0,0,0] op_sel_hi:[0,1,1]\n"
      "v_cos_f32 v78, v97\n"
      "v_pk_mul_f32 v[64:65], v[190:191], v[88:89] op_sel:[1,1] op_sel_hi:[1,0] neg_lo:[1,0]\n"
      "ds_write_b128 v47, v[126:129] offset:4096\n"
      "v_pk_fma_f32 v[192:193], v[190:191], v[88:89], v[64:65] op_sel:[0,0,0] op_sel_hi:[0,1,1]\n"
      "v_cvt_pk_f16_f32 v127, v186, v188\n"
      "v_pk_mul_f32 v[66:67], v[192:193], v[88:89] op_sel:[1,1] op_sel_hi:[1,0] neg_lo:[1,0]\n"
      "v_cvt_pk_f16_f32 v128, v190, v192\n"
      "v_pk_fma_f32 v[194:195], v[192:193], v[88:89], v[66:67] op_sel:[0,0,0] op_sel_hi:[0,1,1]\n"
      "v_cvt_pk_f16_f32 v126, 1.0, v88\n"
      "v_pk_mul_f32 v[68:69], v[194:195], v[88:89] op_sel:[1,1] op_sel_hi:[1,0] neg_lo:[1,0]\n"
      "v_sin_f32 v79, v97\n"
      "v_pk_fma_f32 v[196:197], v[194:195], v[88:89], v[68:69] op_sel:[0,0,0] op_sel_hi:[0,1,1]\n"
      "v_fract_f32 v99, v35\n"
      "v_pk_mul_f32 v[70:71], v[196:197], v[88:89] op_sel:[1,1] op_sel_hi:[1,0] neg_lo:[1,0]\n"
      "v_cvt_pk_f16_f32 v129, v194, v196\n"
      "v_pk_fma_f32 v[198:199], v[196:197], v[88:89], v[70:71] op_sel:[0,0,0] op_sel_hi:[0,1,1]\n"
      "ds_write_b128 v49, v[130:133] offset:4096\n"
      "v_pk_mul_f32 v[72:73], v[198:199], v[88:89] op_sel:[1,1] op_sel_hi:[1,0] neg_lo:[1,0]\n"
      "v_cvt_pk_f16_f32 v131, v187, v189\n"
      "v_pk_fma_f32 v[200:201], v[198:199], v[88:89], v[72:73] op_sel:[0,0,0] op_sel_hi:[0,1,1]\n"
      "v_cvt_pk_f16_f32 v132, v191, v193\n"
      "v_pk_mul_f32 v[74:75], v[200:201], v[88:89] op_sel:[1,1] op_sel_hi:[1,0] neg_lo:[1,0]\n"
      "v_cvt_pk_f16_f32 v133, v195, v197\n"
      "v_pk_fma_f32 v[202:203], v[200:201], v[88:89], v[74:75] op_sel:[0,0,0] op_sel_hi:[0,1,1]\n"
      "v_pk_mul_f32 v[74:75], v[38:39], v[86:87] op_sel:[1,1] op_sel_hi:[1,0] neg_lo:[1,0]\n"
      "v_pk_mul_f32 v[64:65], v[202:203], v[88:89] op_sel:[1,1] op_sel_hi:[1,0] neg_lo:[1,0]\n"
      "v_pk_fma_f32 v[216:217], v[38:39], v[86:87], v[74:75] op_sel:[0,0,0] op_sel_hi:[0,1,1]\n"
      "v_pk_fma_f32 v[204:205], v[202:203], v[88:89], v[64:65] op_sel:[0,0,0] op_sel_hi:[0,1,1]\n"
      "v_pk_mul_f32 v[64:65], v[216:217], v[86:87] op_sel:[1,1] op_sel_hi:[1,0] neg_lo:[1,0]\n"
      "v_pk_mul_f32 v[66:67], v[204:205], v[88:89] op_sel:[1,1] op_sel_hi:[1,0] neg_lo:[1,0]\n"
      "v_pk_fma_f32 v[218:219], v[216:217], v[86:87], v[64:65] op_sel:[0,0,0] op_sel_hi:[0,1,1]\n"
      "v_pk_fma_f32 v[206:207], v[204:205], v[88:89], v[66:67] op_sel:[0,0,0] op_sel_hi:[0,1,1]\n"
      "v_pk_mul_f32 v[64:65], v[38:39], v[90:91] op_sel:[1,1] op_sel_hi:[1,0] neg_lo:[1,0]\n"
      "ds_read_b64_tr_b16 v[134:135], v50 offset:0\n"
      "v_pk_mul_f32 v[66:67], v[218:219], v[86:87] op_sel:[1,1] op_sel_hi:[1,0] neg_lo:[1,0]\n"
      "ds_read_b64_tr_b16 v[136:137], v50 offset:512\n"
      "v_pk_mul_f32 v[68:69], v[206:207], v[88:89] op_sel:[1,1] op_sel_hi:[1,0] neg_lo:[1,0]\n"
      "s_mov_b32 m0, s51\n"
      "s_nop 0\n"
      "buffer_load_dwordx4 v13, s[40:43], s50 offen offset:0 lds\n"
      "buffer_load_dwordx4 v14, s[40:43], s50 offen offset:1024 lds\n"
      "buffer_load_dwordx4 v15, s[40:43], s50 offen offset:2048 lds\n"
      "s_add_u32 s50, s50, 196608\n"
      "s_add_u32 s51, s51, 12288\n"
      "v_pk_fma_f32 v[214:215], v[38:39], v[90:91], v[64:65] op_sel:[0,0,0] op_sel_hi:[0,1,1]\n"
      "ds_read_b64_tr_b16 v[138:139], v50 offset:1024\n"
      "v_pk_fma_f32 v[220:221], v[218:219], v[86:87], v[66:67] op_sel:[0,0,0] op_sel_hi:[0,1,1]\n"
      "ds_read_b64_tr_b16 v[140:141], v50 offset:1536\n"
      "v_pk_fma_f32 v[208:209], v[206:207], v[88:89], v[68:69] op_sel:[0,0,0] op_sel_hi:[0,1,1]\n"
      "v_pk_mul_f32 v[66:67], v[214:215], v[86:87] op_sel:[1,1] op_sel_hi:[1,0] neg_lo:[1,0]\n"
      "ds_read_b64_tr_b16 v[142:143], v50 offset:2048\n"
      "v_pk_mul_f32 v[68:69], v[220:221], v[86:87] op_sel:[1,1] op_sel_hi:[1,0] neg_lo:[1,0]\n"
      "ds_read_b64_tr_b16 v[144:145], v50 offset:2560\n"
      "v_pk_mul_f32 v[70:71], v[208:209], v[88:89] op_sel:[1,1] op_sel_hi:[1,0] neg_lo:[1,0]\n"
      "v_pk_fma_f32 v[184:185], v[214:215], v[86:87], v[66:67] op_sel:[0,0,0] op_sel_hi:[0,1,1]\n"
      "ds_read_b64_tr_b16 v[146:147], v50 offset:3072\n"
      "v_pk_fma_f32 v[222:223], v[220:221], v[86:87], v[68:69] op_sel:[0,0,0] op_sel_hi:[0,1,1]\n"
      "ds_read_b64_tr_b16 v[148:149], v50 offset:3584\n"
      "v_pk_fma_f32 v[210:211], v[208:209], v[88:89], v[70:71] op_sel:[0,0,0] op_sel_hi:[0,1,1]\n"
      "v_pk_mul_f32 v[68:69], v[184:185], v[86:87] op_sel:[1,1] op_sel_hi:[1,0] neg_lo:[1,0]\n"
      "ds_read_b64_tr_b16 v[150:151], v50 offset:4096\n"
      "v_pk_mul_f32 v[70:71], v[222:223], v[86:87] op_sel:[1,1] op_sel_hi:[1,0] neg_lo:[1,0]\n"
      "ds_read_b64_tr_b16 v[152:153], v50 offset:4608\n"
      "v_pk_mul_f32 v[72:73], v[210:211], v[88:89] op_sel:[1,1] op_sel_hi:[1,0] neg_lo:[1,0]\n"
      "v_pk_fma_f32 v[186:187], v[184:185], v[86:87], v[68:69] op_sel:[0,0,0] op_sel_hi:[0,1,1]\n"
      "ds_read_b64_tr_b16 v[154:155], v50 offset:5120\n"
      "v_pk_fma_f32 v[224:225], v[222:223], v[86:87], v[70:71] op_sel:[0,0,0] op_sel_hi:[0,1,1]\n"
      "ds_read_b64_tr_b16 v[156:157], v50 offset:5632\n"
      "v_pk_fma_f32 v[212:213], v[210:211], v[88:89], v[72:73] op_sel:[0,0,0] op_sel_hi:[0,1,1]\n"
      "v_pk_mul_f32 v[70:71], v[186:187], v[86:87] op_sel:[1,1] op_sel_hi:[1,0] neg_lo:[1,0]\n"
      "ds_read_b64_tr_b16 v[158:159], v50 offset:6144\n"
      "v_pk_mul_f32 v[72:73], v[224:225], v[86:87] op_sel:[1,1] op_sel_hi:[1,0] neg_lo:[1,0]\n"
      "ds_read_b64_tr_b16 v[160:161], v50 offset:6656\n"
      "v_pk_fma_f32 v[188:189], v[186:187], v[86:87], v[70:71] op_sel:[0,0,0] op_sel_hi:[0,1,1]\n"
      "ds_read_b64_tr_b16 v[162:163], v50 offset:7168\n"
      "v_pk_fma_f32 v[226:227], v[224:225], v[86:87], v[72:73] op_sel:[0,0,0] op_sel_hi:[0,1,1]\n"
      "ds_read_b64_tr_b16 v[164:165], v50 offset:7680\n"
      "v_pk_mul_f32 v[72:73], v[188:189], v[86:87] op_sel:[1,1] op_sel_hi:[1,0] neg_lo:[1,0]\n"
      "ds_write_b128 v46, v[102:105] offset:0\n"
      "v_pk_mul_f32 v[74:75], v[226:227], v[86:87] op_sel:[1,1] op_sel_hi:[1,0] neg_lo:[1,0]\n"
      "v_pk_fma_f32 v[190:191], v[188:189], v[86:87], v[72:73] op_sel:[0,0,0] op_sel_hi:[0,1,1]\n"
      "v_pk_fma_f32 v[228:229], v[226:227], v[86:87], v[74:75] op_sel:[0,0,0] op_sel_hi:[0,1,1]\n"
      "v_pk_mul_f32 v[74:75], v[190:191], v[86:87] op_sel:[1,1] op_sel_hi:[1,0] neg_lo:[1,0]\n"
      "v_pk_mul_f32 v[72:73], v[214:215], v[90:91] op_sel:[1,1] op_sel_hi:[1,0] neg_lo:[1,0]\n"
      "v_pk_fma_f32 v[192:193], v[190:191], v[86:87], v[74:75] op_sel:[0,0,0] op_sel_hi:[0,1,1]\n"
      "v_pk_mul_f32 v[68:69], v[86:87], v[86:87] op_sel:[1,1] op_sel_hi:[1,0] neg_lo:[1,0]\n"
      "v_pk_mul_f32 v[64:65], v[192:193], v[86:87] op_sel:[1,1] op_sel_hi:[1,0] neg_lo:[1,0]\n"
      "v_pk_fma_f32 v[92:93], v[86:87], v[86:87], v[68:69] op_sel:[0,0,0] op_sel_hi:[0,1,1]\n"
      "v_pk_fma_f32 v[194:195], v[192:193], v[86:87], v[64:65] op_sel:[0,0,0] op_sel_hi:[0,1,1]\n"
      "v_cvt_pk_f16_f32 v103, v218, v220\n"
      "v_pk_mul_f32 v[66:67], v[194:195], v[86:87] op_sel:[1,1] op_sel_hi:[1,0] neg_lo:[1,0]\n"
      "ds_write_b128 v48, v[106:109] offset:0\n"
      "v_pk_fma_f32 v[196:197], v[194:195], v[86:87], v[66:67] op_sel:[0,0,0] op_sel_hi:[0,1,1]\n"
      "v_cvt_pk_f16_f32 v107, v219, v221\n"
      "v_pk_mul_f32 v[70:71], v[92:93], v[92:93] op_sel:[1,1] op_sel_hi:[1,0] neg_lo:[1,0]\n"
      "v_cvt_pk_f16_f32 v104, v222, v224\n"
      "v_pk_fma_f32 v[94:95], v[92:93], v[92:93], v[70:71] op_sel:[0,0,0] op_sel_hi:[0,1,1]\n"
      "v_cvt_pk_f16_f32 v108, v223, v225\n"
      "s_waitcnt lgkmcnt(8)\n"
      "v_mfma_f32_16x16x32_f16 v[52:55], v[150:153], v[134:137], 0\n"
      "s_waitcnt lgkmcnt(6)\n"
      "v_mfma_f32_16x16x32_f16 v[52:55], v[154:157], v[138:141], v[52:55]\n"
      "v_cvt_pk_f16_f32 v105, v226, v228\n"
      "v_cvt_pk_f16_f32 v109, v227, v229\n"
      "s_waitcnt lgkmcnt(4)\n"
      "v_mfma_f32_16x16x32_f16 v[52:55], v[158:161], v[142:145], v[52:55]\n"
      "s_nop 0\n"
      "v_cvt_pk_f16_f32 v102, v38, v216\n"
      "ds_write_b128 v47, v[110:113] offset:0\n"
      "v_cvt_pk_f16_f32 v110, v198, v200\n"
      "v_cvt_pk_f16_f32 v112, v206, v208\n"
      "v_cvt_pk_f16_f32 v111, v202, v204\n"
      "v_cvt_pk_f16_f32 v113, v210, v212\n"
      "s_waitcnt lgkmcnt(3)\n"
      "v_mfma_f32_16x16x32_f16 v[52:55], v[162:165], v[146:149], v[52:55]\n"
      "v_cvt_pk_f16_f32 v106, v39, v217\n"
      "v_cvt_pk_f16_f32 v130, 0, v89\n"
      "v_pk_mul_f32 v[78:79], v[78:79], v[96:97] op_sel_hi:[1,0] neg_hi:[1,0]\n"
      "v_exp_f32 v98, v32\n"
      "v_cos_f32 v76, v99\n"
      "v_sin_f32 v77, v99\n"
      "v_mul_f32 v101, 0x43000000, v35\n"
      "ds_write_b128 v49, v[114:117] offset:0\n"
      "v_cvt_pk_f16_f32 v114, v199, v201\n"
      "v_pk_fma_f32 v[198:199], v[214:215], v[90:91], v[72:73] op_sel:[0,0,0] op_sel_hi:[0,1,1]\n"
      "v_cvt_pk_f16_f32 v116, v207, v209\n"
      "v_pk_mul_f32 v[74:75], v[198:199], v[90:91] op_sel:[1,1] op_sel_hi:[1,0] neg_lo:[1,0]\n"
      "v_pk_mul_f32 v[64:65], v[198:199], v[92:93] op_sel:[1,1] op_sel_hi:[1,0] neg_lo:[1,0]\n"
      "v_pk_fma_f32 v[206:207], v[198:199], v[90:91], v[74:75] op_sel:[0,0,0] op_sel_hi:[0,1,1]\n"
      "s_mov_b32 m0, s51\n"
      "s_nop 0\n"
      "buffer_load_dwordx4 v13, s[40:43], s50 offen offset:0 lds\n"
      "buffer_load_dwordx4 v14, s[40:43], s50 offen offset:1024 lds\n"
      "buffer_load_dwordx4 v15, s[40:43], s50 offen offset:2048 lds\n"
      "s_add_u32 s50, s50, 196608\n"
      "s_add_u32 s51, s51, 12288\n"
      "v_pk_fma_f32 v[200:201], v[198:199], v[92:93], v[64:65] op_sel:[0,0,0] op_sel_hi:[0,1,1]\n"
      "v_pk_mul_f32 v[64:65], v[206:207], v[90:91] op_sel:[1,1] op_sel_hi:[1,0] neg_lo:[1,0]\n"
      "v_pk_mul_f32 v[66:67], v[206:207], v[92:93] op_sel:[1,1] op_sel_hi:[1,0] neg_lo:[1,0]\n"
      "v_pk_mul_f32 v[68:69], v[200:201], v[92:93] op_sel:[1,1] op_sel_hi:[1,0] neg_lo:[1,0]\n"
      "v_pk_fma_f32 v[208:209], v[206:207], v[92:93], v[66:67] op_sel:[0,0,0] op_sel_hi:[0,1,1]\n"
      "v_cvt_pk_f16_f32 v115, v203, v205\n"
      "ds_read_b64_tr_b16 v[166:167], v50 offset:0\n"
      "v_pk_fma_f32 v[202:203], v[200:201], v[92:93], v[68:69] op_sel:[0,0,0] op_sel_hi:[0,1,1]\n"
      "ds_read_b64_tr_b16 v[168:169], v50 offset:512\n"
      "v_pk_mul_f32 v[70:71], v[208:209], v[92:93] op_sel:[1,1] op_sel_hi:[1,0] neg_lo:[1,0]\n"
      "ds_read_b64_tr_b16 v[170:171], v50 offset:1024\n"
      "v_cvt_pk_f16_f32 v117, v211, v213\n"
      "ds_read_b64_tr_b16 v[172:173], v50 offset:1536\n"
      "v_pk_fma_f32 v[210:211], v[208:209], v[92:93], v[70:71] op_sel:[0,0,0] op_sel_hi:[0,1,1]\n"
      "ds_read_b64_tr_b16 v[174:175], v50 offset:2048\n"
      "v_pk_mul_f32 v[72:73], v[202:203], v[92:93] op_sel:[1,1] op_sel_hi:[1,0] neg_lo:[1,0]\n"
      "ds_read_b64_tr_b16 v[176:177], v50 offset:2560\n"
      "v_pk_fma_f32 v[204:205], v[202:203], v[92:93], v[72:73] op_sel:[0,0,0] op_sel_hi:[0,1,1]\n"
      "ds_read_b64_tr_b16 v[178:179], v50 offset:3072\n"
      "v_pk_mul_f32 v[74:75], v[210:211], v[92:93] op_sel:[1,1] op_sel_hi:[1,0] neg_lo:[1,0]\n"
      "ds_read_b64_tr_b16 v[180:181], v50 offset:3584\n"
      "v_pk_fma_f32 v[212:213], v[210:211], v[92:93], v[74:75] op_sel:[0,0,0] op_sel_hi:[0,1,1]\n"
      "ds_write_b128 v46, v[118:121] offset:4096\n"
      "v_cvt_pk_f16_f32 v118, v214, v184\n"
      "v_cvt_pk_f16_f32 v119, v186, v188\n"
      "v_cvt_pk_f16_f32 v120, v190, v192\n"
      "v_cvt_pk_f16_f32 v121, v194, v196\n"
      "v_pk_mul_f32 v[76:77], v[76:77], v[98:99] op_sel_hi:[1,0]\n"
      "v_mul_f32 v100, 0x43000000, v32\n"
      "v_fract_f32 v101, v101\n"
      "v_exp_f32 v100, v100\n"
      "v_cos_f32 v80, v101\n"
      "v_sin_f32 v81, v101\n"
      "ds_write_b128 v48, v[122:125] offset:4096\n"
      "v_cvt_pk_f16_f32 v122, v215, v185\n"
      "v_pk_fma_f32 v[214:215], v[206:207], v[90:91], v[64:65] op_sel:[0,0,0] op_sel_hi:[0,1,1]\n"
      "v_cvt_pk_f16_f32 v123, v187, v189\n"
      "v_pk_mul_f32 v[66:67], v[214:215], v[90:91] op_sel:[1,1] op_sel_hi:[1,0] neg_lo:[1,0]\n"
      "v_pk_mul_f32 v[72:73], v[214:215], v[94:95] op_sel:[1,1] op_sel_hi:[1,0] neg_lo:[1,0]\n"
      "v_pk_fma_f32 v[218:219], v[214:215], v[90:91], v[66:67] op_sel:[0,0,0] op_sel_hi:[0,1,1]\n"
      "v_pk_fma_f32 v[216:217], v[214:215], v[94:95], v[72:73] op_sel:[0,0,0] op_sel_hi:[0,1,1]\n"
      "v_pk_mul_f32 v[68:69], v[218:219], v[90:91] op_sel:[1,1] op_sel_hi:[1,0] neg_lo:[1,0]\n"
      "v_pk_mul_f32 v[74:75], v[218:219], v[94:95] op_sel:[1,1] op_sel_hi:[1,0] neg_lo:[1,0]\n"
      "v_pk_fma_f32 v[222:223], v[218:219], v[90:91], v[68:69] op_sel:[0,0,0] op_sel_hi:[0,1,1]\n"
      "v_pk_fma_f32 v[220:221], v[218:219], v[94:95], v[74:75] op_sel:[0,0,0] op_sel_hi:[0,1,1]\n"
      "v_pk_mul_f32 v[70:71], v[222:223], v[90:91] op_sel:[1,1] op_sel_hi:[1,0] neg_lo:[1,0]\n"
      "ds_read_b64_tr_b16 v[150:151], v50 offset:4096\n"
      "v_pk_fma_f32 v[226:227], v[222:223], v[90:91], v[70:71] op_sel:[0,0,0] op_sel_hi:[0,1,1]\n"
      "ds_read_b64_tr_b16 v[152:153], v50 offset:4608\n"
      "v_pk_mul_f32 v[68:69], v[226:227], v[90:91] op_sel:[1,1] op_sel_hi:[1,0] neg_lo:[1,0]\n"
      "ds_read_b64_tr_b16 v[154:155], v50 offset:5120\n"
      "v_pk_fma_f32 v[182:183], v[226:227], v[90:91], v[68:69] op_sel:[0,0,0] op_sel_hi:[0,1,1]\n"
      "ds_read_b64_tr_b16 v[156:157], v50 offset:5632\n"
      "v_pk_mul_f32 v[70:71], v[182:183], v[90:91] op_sel:[1,1] op_sel_hi:[1,0] neg_lo:[1,0]\n"
      "ds_read_b64_tr_b16 v[158:159], v50 offset:6144\n"
      "v_pk_fma_f32 v[184:185], v[182:183], v[90:91], v[70:71] op_sel:[0,0,0] op_sel_hi:[0,1,1]\n"
      "ds_read_b64_tr_b16 v[160:161], v50 offset:6656\n"
      "v_pk_mul_f32 v[72:73], v[184:185], v[90:91] op_sel:[1,1] op_sel_hi:[1,0] neg_lo:[1,0]\n"
      "ds_read_b64_tr_b16 v[162:163], v50 offset:7168\n"
      "v_pk_fma_f32 v[186:187], v[184:185], v[90:91], v[72:73] op_sel:[0,0,0] op_sel_hi:[0,1,1]\n"
      "ds_read_b64_tr_b16 v[164:165], v50 offset:7680\n"
      "v_pk_mul_f32 v[74:75], v[186:187], v[90:91] op_sel:[1,1] op_sel_hi:[1,0] neg_lo:[1,0]\n"
      "ds_write_b128 v46, v[126:129] offset:0\n"
      "v_pk_mul_f32 v[64:65], v[222:223], v[94:95] op_sel:[1,1] op_sel_hi:[1,0] neg_lo:[1,0]\n"
      "v_pk_fma_f32 v[188:189], v[186:187], v[90:91], v[74:75] op_sel:[0,0,0] op_sel_hi:[0,1,1]\n"
      "v_pk_fma_f32 v[224:225], v[222:223], v[94:95], v[64:65] op_sel:[0,0,0] op_sel_hi:[0,1,1]\n"
      "v_pk_mul_f32 v[74:75], v[78:79], v[78:79] op_sel:[1,1] op_sel_hi:[1,0] neg_lo:[1,0]\n"
      "v_pk_mul_f32 v[64:65], v[188:189], v[90:91] op_sel:[1,1] op_sel_hi:[1,0] neg_lo:[1,0]\n"
      "v_cvt_pk_f16_f32 v124, v191, v193\n"
      "v_pk_mul_f32 v[66:67], v[226:227], v[94:95] op_sel:[1,1] op_sel_hi:[1,0] neg_lo:[1,0]\n"
      "v_pk_fma_f32 v[190:191], v[188:189], v[90:91], v[64:65] op_sel:[0,0,0] op_sel_hi:[0,1,1]\n"
      "v_pk_fma_f32 v[228:229], v[226:227], v[94:95], v[66:67] op_sel:[0,0,0] op_sel_hi:[0,1,1]\n"
      "v_pk_mul_f32 v[66:67], v[190:191], v[90:91] op_sel:[1,1] op_sel_hi:[1,0] neg_lo:[1,0]\n"
      "v_cvt_pk_f16_f32 v125, v195, v197\n"
      "v_pk_fma_f32 v[192:193], v[190:191], v[90:91], v[66:67] op_sel:[0,0,0] op_sel_hi:[0,1,1]\n"
      "ds_write_b128 v48, v[130:133] offset:0\n"
      "v_pk_mul_f32 v[68:69], v[192:193], v[90:91] op_sel:[1,1] op_sel_hi:[1,0] neg_lo:[1,0]\n"
      "s_waitcnt lgkmcnt(15)\n"
      "v_mfma_f32_16x16x32_f16 v[56:59], v[166:169], v[134:137], 0\n"
      "s_waitcnt lgkmcnt(8)\n"
      "v_mfma_f32_16x16x32_f16 v[60:63], v[150:153], v[134:137], 0\n"
      "v_pk_fma_f32 v[194:195], v[192:193], v[90:91], v[68:69] op_sel:[0,0,0] op_sel_hi:[0,1,1]\n"
      "s_mov_b32 m0, s51\n"
      "s_nop 0\n"
      "buffer_load_dwordx4 v13, s[40:43], s50 offen offset:0 lds\n"
      "buffer_load_dwordx4 v14, s[40:43], s50 offen offset:1024 lds\n"
      "buffer_load_dwordx4 v15, s[40:43], s50 offen offset:2048 lds\n"
      "s_add_u32 s50, s50, 196608\n"
      "s_add_u32 s51, s51, 12288\n"
      "v_mfma_f32_16x16x32_f16 v[56:59], v[170:173], v[138:141], v[56:59]\n"
      "v_pk_mul_f32 v[70:71], v[194:195], v[90:91] op_sel:[1,1] op_sel_hi:[1,0] neg_lo:[1,0]\n"
      "s_waitcnt lgkmcnt(6)\n"
      "v_mfma_f32_16x16x32_f16 v[60:63], v[154:157], v[138:141], v[60:63]\n"
      "v_mfma_f32_16x16x32_f16 v[56:59], v[174:177], v[142:145], v[56:59]\n"
      "v_pk_fma_f32 v[196:197], v[194:195], v[90:91], v[70:71] op_sel:[0,0,0] op_sel_hi:[0,1,1]\n"
      "v_mfma_f32_16x16x32_f16 v[56:59], v[178:181], v[146:149], v[56:59]\n"
      "s_waitcnt lgkmcnt(4)\n"
      "v_mfma_f32_16x16x32_f16 v[60:63], v[158:161], v[142:145], v[60:63]\n"
      "v_pk_mul_f32 v[72:73], v[196:197], v[90:91] op_sel:[1,1] op_sel_hi:[1,0] neg_lo:[1,0]\n"
      "ds_write_b128 v46, v[102:105] offset:4096\n"
      "v_cvt_pk_f16_f32 v126, v198, v200\n"
      "s_waitcnt lgkmcnt(3)\n"
      "v_mfma_f32_16x16x32_f16 v[60:63], v[162:165], v[146:149], v[60:63]\n"
      "v_cvt_pk_f16_f32 v130, v199, v201\n"
      "v_pk_fma_f32 v[198:199], v[196:197], v[90:91], v[72:73] op_sel:[0,0,0] op_sel_hi:[0,1,1]\n"
      "v_cvt_pk_f16_f32 v127, v202, v204\n"
      "v_add_f32 v45, v45, v198\n"
      "v_cvt_pk_f16_f32 v131, v203, v205\n"
      "v_cvt_pk_f16_f32 v128, v206, v208\n"
      "v_cvt_pk_f16_f32 v129, v210, v212\n"
      "v_cvt_pk_f16_f32 v102, v214, v216\n"
      "v_cvt_pk_f16_f32 v132, v207, v209\n"
      "v_cvt_pk_f16_f32 v133, v211, v213\n"
      "ds_write_b128 v48, v[106:109] offset:4096\n"
      "v_cvt_pk_f16_f32 v106, v215, v217\n"
      "v_cvt_pk_f16_f32 v103, v218, v220\n"
      "v_cvt_pk_f16_f32 v104, v222, v224\n"
      "v_cvt_pk_f16_f32 v105, v226, v228\n"
      "v_cvt_pk_f16_f32 v107, v219, v221\n"
      "v_cvt_pk_f16_f32 v108, v223, v225\n"
      "v_cvt_pk_f16_f32 v109, v227, v229\n"
      "v_pk_mul_f32 v[80:81], v[80:81], v[100:101] op_sel_hi:[1,0]\n"
      "ds_write_b128 v47, v[110:113] offset:0\n"
      "v_cvt_pk_f16_f32 v111, v186, v188\n"
      "v_cvt_pk_f16_f32 v112, v190, v192\n"
      "v_cvt_pk_f16_f32 v113, v194, v196\n"
      "v_cvt_pk_f16_f32 v110, v182, v184\n"
      "ds_write_b128 v49, v[114:117] offset:0\n"
      "v_cvt_pk_f16_f32 v115, v187, v189\n"
      "v_pk_fma_f32 v[186:187], v[78:79], v[78:79], v[74:75] op_sel:[0,0,0] op_sel_hi:[0,1,1]\n"
      "v_cvt_pk_f16_f32 v116, v191, v193\n"
      "v_pk_mul_f32 v[64:65], v[186:187], v[78:79] op_sel:[1,1] op_sel_hi:[1,0] neg_lo:[1,0]\n"
      "v_cvt_pk_f16_f32 v117, v195, v197\n"
      "v_pk_fma_f32 v[188:189], v[186:187], v[78:79], v[64:65] op_sel:[0,0,0] op_sel_hi:[0,1,1]\n"
      "v_cvt_pk_f16_f32 v114, v183, v185\n"
      "v_pk_mul_f32 v[66:67], v[188:189], v[78:79] op_sel:[1,1] op_sel_hi:[1,0] neg_lo:[1,0]\n"
      "ds_write_b128 v47, v[118:121] offset:4096\n"
      "v_pk_fma_f32 v[190:191], v[188:189], v[78:79], v[66:67] op_sel:[0,0,0] op_sel_hi:[0,1,1]\n"
      "v_cvt_pk_f16_f32 v119, v186, v188\n"
      "v_pk_mul_f32 v[68:69], v[190:191], v[78:79] op_sel:[1,1] op_sel_hi:[1,0] neg_lo:[1,0]\n"
      "v_cvt_pk_f16_f32 v118, 1.0, v78\n"
      "v_pk_fma_f32 v[192:193], v[190:191], v[78:79], v[68:69] op_sel:[0,0,0] op_sel_hi:[0,1,1]\n"
      "ds_write_b128 v49, v[122:125] offset:4096\n"
      "v_pk_mul_f32 v[70:71], v[192:193], v[78:79] op_sel:[1,1] op_sel_hi:[1,0] neg_lo:[1,0]\n"
      "v_cvt_pk_f16_f32 v123, v187, v189\n"
      "v_pk_fma_f32 v[194:195], v[192:193], v[78:79], v[70:71] op_sel:[0,0,0] op_sel_hi:[0,1,1]\n"
      "v_cvt_pk_f16_f32 v120, v190, v192\n"
      "v_pk_mul_f32 v[72:73], v[194:195], v[78:79] op_sel:[1,1] op_sel_hi:[1,0] neg_lo:[1,0]\n"
      "v_cvt_pk_f16_f32 v124, v191, v193\n"
      "v_pk_fma_f32 v[196:197], v[194:195], v[78:79], v[72:73] op_sel:[0,0,0] op_sel_hi:[0,1,1]\n"
      "v_cvt_pk_f16_f32 v122, 0, v79\n"
      "v_pk_mul_f32 v[74:75], v[196:197], v[78:79] op_sel:[1,1] op_sel_hi:[1,0] neg_lo:[1,0]\n"
      "v_cvt_pk_f16_f32 v121, v194, v196\n"
      "v_pk_fma_f32 v[198:199], v[196:197], v[78:79], v[74:75] op_sel:[0,0,0] op_sel_hi:[0,1,1]\n"
      "v_cvt_pk_f16_f32 v125, v195, v197\n"
      "ds_read_b64_tr_b16 v[134:135], v50 offset:0\n"
      "v_pk_mul_f32 v[64:65], v[198:199], v[78:79] op_sel:[1,1] op_sel_hi:[1,0] neg_lo:[1,0]\n"
      "ds_read_b64_tr_b16 v[136:137], v50 offset:512\n"
      "v_pk_fma_f32 v[200:201], v[198:199], v[78:79], v[64:65] op_sel:[0,0,0] op_sel_hi:[0,1,1]\n"
      "ds_read_b64_tr_b16 v[138:139], v50 offset:1024\n"
      "v_pk_mul_f32 v[66:67], v[200:201], v[78:79] op_sel:[1,1] op_sel_hi:[1,0] neg_lo:[1,0]\n"
      "ds_read_b64_tr_b16 v[140:141], v50 offset:1536\n"
      "v_pk_fma_f32 v[202:203], v[200:201], v[78:79], v[66:67] op_sel:[0,0,0] op_sel_hi:[0,1,1]\n"
      "ds_read_b64_tr_b16 v[142:143], v50 offset:2048\n"
      "v_pk_mul_f32 v[66:67], v[40:41], v[76:77] op_sel:[1,1] op_sel_hi:[1,0] neg_lo:[1,0]\n"
      "ds_read_b64_tr_b16 v[144:145], v50 offset:2560\n"
      "v_pk_mul_f32 v[68:69], v[202:203], v[78:79] op_sel:[1,1] op_sel_hi:[1,0] neg_lo:[1,0]\n"
      "ds_read_b64_tr_b16 v[146:147], v50 offset:3072\n"
      "v_pk_fma_f32 v[216:217], v[40:41], v[76:77], v[66:67] op_sel:[0,0,0] op_sel_hi:[0,1,1]\n"
      "ds_read_b64_tr_b16 v[148:149], v50 offset:3584\n"
      "v_pk_fma_f32 v[204:205], v[202:203], v[78:79], v[68:69] op_sel:[0,0,0] op_sel_hi:[0,1,1]\n"
      "ds_read_b64_tr_b16 v[150:151], v50 offset:4096\n"
      "s_mov_b32 m0, s51\n"
      "s_nop 0\n"
      "buffer_load_dwordx4 v13, s[40:43], s50 offen offset:0 lds\n"
      "buffer_load_dwordx4 v14, s[40:43], s50 offen offset:1024 lds\n"
      "buffer_load_dwordx4 v15, s[40:43], s50 offen offset:2048 lds\n"
      "s_add_u32 s50, s50, 196608\n"
      "s_add_u32 s51, s51, 12288\n"
      "v_pk_mul_f32 v[68:69], v[216:217], v[76:77] op_sel:[1,1] op_sel_hi:[1,0] neg_lo:[1,0]\n"
      "ds_read_b64_tr_b16 v[152:153], v50 offset:4608\n"
      "v_pk_mul_f32 v[70:71], v[204:205], v[78:79] op_sel:[1,1] op_sel_hi:[1,0] neg_lo:[1,0]\n"
      "ds_read_b64_tr_b16 v[154:155], v50 offset:5120\n"
      "v_pk_fma_f32 v[218:219], v[216:217], v[76:77], v[68:69] op_sel:[0,0,0] op_sel_hi:[0,1,1]\n"
      "ds_read_b64_tr_b16 v[156:157], v50 offset:5632\n"
      "v_pk_fma_f32 v[206:207], v[204:205], v[78:79], v[70:71] op_sel:[0,0,0] op_sel_hi:[0,1,1]\n"
      "ds_read_b64_tr_b16 v[158:159], v50 offset:6144\n"
      "v_pk_mul_f32 v[68:69], v[40:41], v[80:81] op_sel:[1,1] op_sel_hi:[1,0] neg_lo:[1,0]\n"
      "ds_read_b64_tr_b16 v[160:161], v50 offset:6656\n"
      "v_pk_mul_f32 v[70:71], v[218:219], v[76:77] op_sel:[1,1] op_sel_hi:[1,0] neg_lo:[1,0]\n"
      "ds_read_b64_tr_b16 v[162:163], v50 offset:7168\n"
      "v_pk_mul_f32 v[72:73], v[206:207], v[78:79] op_sel:[1,1] op_sel_hi:[1,0] neg_lo:[1,0]\n"
      "ds_read_b64_tr_b16 v[164:165], v50 offset:7680\n"
      "v_pk_fma_f32 v[214:215], v[40:41], v[80:81], v[68:69] op_sel:[0,0,0] op_sel_hi:[0,1,1]\n"
      "ds_write_b128 v46, v[126:129] offset:0\n"
      "v_pk_fma_f32 v[220:221], v[218:219], v[76:77], v[70:71] op_sel:[0,0,0] op_sel_hi:[0,1,1]\n"
      "v_pk_fma_f32 v[208:209], v[206:207], v[78:79], v[72:73] op_sel:[0,0,0] op_sel_hi:[0,1,1]\n"
      "v_pk_mul_f32 v[70:71], v[214:215], v[76:77] op_sel:[1,1] op_sel_hi:[1,0] neg_lo:[1,0]\n"
      "v_pk_mul_f32 v[72:73], v[220:221], v[76:77] op_sel:[1,1] op_sel_hi:[1,0] neg_lo:[1,0]\n"
      "v_pk_mul_f32 v[74:75], v[208:209], v[78:79] op_sel:[1,1] op_sel_hi:[1,0] neg_lo:[1,0]\n"
      "v_pk_fma_f32 v[184:185], v[214:215], v[76:77], v[70:71] op_sel:[0,0,0] op_sel_hi:[0,1,1]\n"
      "v_pk_fma_f32 v[222:223], v[220:221], v[76:77], v[72:73] op_sel:[0,0,0] op_sel_hi:[0,1,1]\n"
      "v_pk_fma_f32 v[210:211], v[208:209], v[78:79], v[74:75] op_sel:[0,0,0] op_sel_hi:[0,1,1]\n"
      "v_pk_mul_f32 v[72:73], v[184:185], v[76:77] op_sel:[1,1] op_sel_hi:[1,0] neg_lo:[1,0]\n"
      "v_pk_mul_f32 v[74:75], v[222:223], v[76:77] op_sel:[1,1] op_sel_hi:[1,0] neg_lo:[1,0]\n"
      "v_pk_mul_f32 v[64:65], v[210:211], v[78:79] op_sel:[1,1] op_sel_hi:[1,0] neg_lo:[1,0]\n"
      "v_pk_fma_f32 v[186:187], v[184:185], v[76:77], v[72:73] op_sel:[0,0,0] op_sel_hi:[0,1,1]\n"
      "ds_write_b128 v48, v[130:133] offset:0\n"
      "v_pk_fma_f32 v[224:225], v[222:223], v[76:77], v[74:75] op_sel:[0,0,0] op_sel_hi:[0,1,1]\n"
      "v_pk_fma_f32 v[212:213], v[210:211], v[78:79], v[64:65] op_sel:[0,0,0] op_sel_hi:[0,1,1]\n"
      "s_waitcnt lgkmcnt(8)\n"
      "v_mfma_f32_16x16x32_f16 v[52:55], v[150:153], v[134:137], v[52:55]\n"
      "v_pk_mul_f32 v[74:75], v[186:187], v[76:77] op_sel:[1,1] op_sel_hi:[1,0] neg_lo:[1,0]\n"
      "v_pk_mul_f32 v[64:65], v[224:225], v[76:77] op_sel:[1,1] op_sel_hi:[1,0] neg_lo:[1,0]\n"
      "v_pk_fma_f32 v[188:189], v[186:187], v[76:77], v[74:75] op_sel:[0,0,0] op_sel_hi:[0,1,1]\n"
      "s_waitcnt lgkmcnt(6)\n"
      "v_mfma_f32_16x16x32_f16 v[52:55], v[154:157], v[138:141], v[52:55]\n"
      "v_pk_fma_f32 v[226:227], v[224:225], v[76:77], v[64:65] op_sel:[0,0,0] op_sel_hi:[0,1,1]\n"
      "v_pk_mul_f32 v[64:65], v[188:189], v[76:77] op_sel:[1,1] op_sel_hi:[1,0] neg_lo:[1,0]\n"
      "v_pk_mul_f32 v[66:67], v[226:227], v[76:77] op_sel:[1,1] op_sel_hi:[1,0] neg_lo:[1,0]\n"
      "s_waitcnt lgkmcnt(4)\n"
      "v_mfma_f32_16x16x32_f16 v[52:55], v[158:161], v[142:145], v[52:55]\n"
      "v_pk_fma_f32 v[190:191], v[188:189], v[76:77], v[64:65] op_sel:[0,0,0] op_sel_hi:[0,1,1]\n"
      "ds_write_b128 v47, v[102:105] offset:0\n"
      "v_pk_fma_f32 v[228:229], v[226:227], v[76:77], v[66:67] op_sel:[0,0,0] op_sel_hi:[0,1,1]\n"
      "s_waitcnt lgkmcnt(3)\n"
      "v_mfma_f32_16x16x32_f16 v[52:55], v[162:165], v[146:149], v[52:55]\n"
      "v_pk_mul_f32 v[66:67], v[190:191], v[76:77] op_sel:[1,1] op_sel_hi:[1,0] neg_lo:[1,0]\n"
      "v_pk_mul_f32 v[64:65], v[214:215], v[80:81] op_sel:[1,1] op_sel_hi:[1,0] neg_lo:[1,0]\n"
      "v_pk_fma_f32 v[192:193], v[190:191], v[76:77], v[66:67] op_sel:[0,0,0] op_sel_hi:[0,1,1]\n"
      "v_cvt_pk_f16_f32 v102, v198, v200\n"
      "v_pk_mul_f32 v[72:73], v[76:77], v[76:77] op_sel:[1,1] op_sel_hi:[1,0] neg_lo:[1,0]\n"
      "v_pk_mul_f32 v[68:69], v[192:193], v[76:77] op_sel:[1,1] op_sel_hi:[1,0] neg_lo:[1,0]\n"
      "v_pk_fma_f32 v[82:83], v[76:77], v[76:77], v[72:73] op_sel:[0,0,0] op_sel_hi:[0,1,1]\n"
      "v_pk_fma_f32 v[194:195], v[192:193], v[76:77], v[68:69] op_sel:[0,0,0] op_sel_hi:[0,1,1]\n"
      "v_cvt_pk_f16_f32 v104, v206, v208\n"
      "v_pk_mul_f32 v[70:71], v[194:195], v[76:77] op_sel:[1,1] op_sel_hi:[1,0] neg_lo:[1,0]\n"
      "ds_write_b128 v49, v[106:109] offset:0\n"
      "v_cvt_pk_f16_f32 v106, v199, v201\n"
      "v_pk_fma_f32 v[198:199], v[214:215], v[80:81], v[64:65] op_sel:[0,0,0] op_sel_hi:[0,1,1]\n"
      "v_cvt_pk_f16_f32 v108, v207, v209\n"
      "v_pk_mul_f32 v[66:67], v[198:199], v[80:81] op_sel:[1,1] op_sel_hi:[1,0] neg_lo:[1,0]\n"
      "v_pk_mul_f32 v[68:69], v[198:199], v[82:83] op_sel:[1,1] op_sel_hi:[1,0] neg_lo:[1,0]\n"
      "v_pk_fma_f32 v[206:207], v[198:199], v[80:81], v[66:67] op_sel:[0,0,0] op_sel_hi:[0,1,1]\n"
      "v_pk_fma_f32 v[200:201], v[198:199], v[82:83], v[68:69] op_sel:[0,0,0] op_sel_hi:[0,1,1]\n"
      "v_pk_fma_f32 v[196:197], v[194:195], v[76:77], v[70:71] op_sel:[0,0,0] op_sel_hi:[0,1,1]\n"
      "v_pk_mul_f32 v[68:69], v[206:207], v[80:81] op_sel:[1,1] op_sel_hi:[1,0] neg_lo:[1,0]\n"
      "v_pk_mul_f32 v[70:71], v[206:207], v[82:83] op_sel:[1,1] op_sel_hi:[1,0] neg_lo:[1,0]\n"
      "v_cvt_pk_f16_f32 v126, v40, v216\n"
      "v_pk_fma_f32 v[208:209], v[206:207], v[82:83], v[70:71] op_sel:[0,0,0] op_sel_hi:[0,1,1]\n"
      "ds_read_b64_tr_b16 v[166:167], v50 offset:0\n"
      "v_cvt_pk_f16_f32 v127, v218, v220\n"
      "ds_read_b64_tr_b16 v[168:169], v50 offset:512\n"
      "v_cvt_pk_f16_f32 v128, v222, v224\n"
      "ds_read_b64_tr_b16 v[170:171], v50 offset:1024\n"
      "v_cvt_pk_f16_f32 v129, v226, v228\n"
      "ds_read_b64_tr_b16 v[172:173], v50 offset:1536\n"
      "v_cvt_pk_f16_f32 v130, v41, v217\n"
      "ds_read_b64_tr_b16 v[174:175], v50 offset:2048\n"
      "v_cvt_pk_f16_f32 v131, v219, v221\n"
      "ds_read_b64_tr_b16 v[176:177], v50 offset:2560\n"
      "v_cvt_pk_f16_f32 v132, v223, v225\n"
      "ds_read_b64_tr_b16 v[178:179], v50 offset:3072\n"
      "s_mov_b32 m0, s51\n"
      "s_nop 0\n"
      "buffer_load_dwordx4 v13, s[40:43], s50 offen offset:0 lds\n"
      "buffer_load_dwordx4 v14, s[40:43], s50 offen offset:1024 lds\n"
      "buffer_load_dwordx4 v15, s[40:43], s50 offen offset:2048 lds\n"
      "v_cvt_pk_f16_f32 v133, v227, v229\n"
      "ds_read_b64_tr_b16 v[180:181], v50 offset:3584\n"
      "v_pk_mul_f32 v[72:73], v[200:201], v[82:83] op_sel:[1,1] op_sel_hi:[1,0] neg_lo:[1,0]\n"
      "ds_write_b128 v46, v[110:113] offset:4096\n"
      "v_cvt_pk_f16_f32 v110, v214, v184\n"
      "v_cvt_pk_f16_f32 v103, v202, v204\n"
      "v_cvt_pk_f16_f32 v107, v203, v205\n"
      "v_cvt_pk_f16_f32 v105, v210, v212\n"
      "s_mov_b64 exec, 3\n"
      "buffer_load_dword v240, v11, s[32:35], s93 offen sc1\n"
      "s_mov_b64 exec, -1\n"
      "v_pk_mul_f32 v[74:75], v[82:83], v[82:83] op_sel:[1,1] op_sel_hi:[1,0] neg_lo:[1,0]\n"
      "v_cvt_pk_f16_f32 v109, v211, v213\n"
      "v_pk_fma_f32 v[202:203], v[200:201], v[82:83], v[72:73] op_sel:[0,0,0] op_sel_hi:[0,1,1]\n"
      "v_cvt_pk_f16_f32 v111, v186, v188\n"
      "v_cvt_pk_f16_f32 v112, v190, v192\n"
      "v_cvt_pk_f16_f32 v113, v194, v196\n"
      "v_pk_fma_f32 v[84:85], v[82:83], v[82:83], v[74:75] op_sel:[0,0,0] op_sel_hi:[0,1,1]\n"
      "v_pk_mul_f32 v[74:75], v[208:209], v[82:83] op_sel:[1,1] op_sel_hi:[1,0] neg_lo:[1,0]\n"
      "ds_write_b128 v48, v[114:117] offset:4096\n"
      "v_cvt_pk_f16_f32 v114, v215, v185\n"
      "v_pk_fma_f32 v[214:215], v[206:207], v[80:81], v[68:69] op_sel:[0,0,0] op_sel_hi:[0,1,1]\n"
      "v_cvt_pk_f16_f32 v115, v187, v189\n"
      "v_pk_mul_f32 v[70:71], v[214:215], v[80:81] op_sel:[1,1] op_sel_hi:[1,0] neg_lo:[1,0]\n"
      "v_cvt_pk_f16_f32 v116, v191, v193\n"
      "v_pk_fma_f32 v[218:219], v[214:215], v[80:81], v[70:71] op_sel:[0,0,0] op_sel_hi:[0,1,1]\n"
      "v_cvt_pk_f16_f32 v117, v195, v197\n"
      "v_pk_mul_f32 v[72:73], v[218:219], v[80:81] op_sel:[1,1] op_sel_hi:[1,0] neg_lo:[1,0]\n"
      "s_waitcnt lgkmcnt(8)\n"
      "v_mfma_f32_16x16x32_f16 v[56:59], v[166:169], v[134:137], v[56:59]\n"
      "v_pk_fma_f32 v[222:223], v[218:219], v[80:81], v[72:73] op_sel:[0,0,0] op_sel_hi:[0,1,1]\n"
      "v_pk_fma_f32 v[210:211], v[208:209], v[82:83], v[74:75] op_sel:[0,0,0] op_sel_hi:[0,1,1]\n"
      "v_pk_mul_f32 v[74:75], v[222:223], v[80:81] op_sel:[1,1] op_sel_hi:[1,0] neg_lo:[1,0]\n"
      "ds_read_b64_tr_b16 v[150:151], v50 offset:4096\n"
      "s_waitcnt lgkmcnt(7)\n"
      "v_mfma_f32_16x16x32_f16 v[56:59], v[170:173], v[138:141], v[56:59]\n"
      "ds_read_b64_tr_b16 v[152:153], v50 offset:4608\n"
      "v_pk_fma_f32 v[226:227], v[222:223], v[80:81], v[74:75] op_sel:[0,0,0] op_sel_hi:[0,1,1]\n"
      "ds_read_b64_tr_b16 v[154:155], v50 offset:5120\n"
      "s_waitcnt lgkmcnt(7)\n"
      "v_mfma_f32_16x16x32_f16 v[56:59], v[174:177], v[142:145], v[56:59]\n"
      "ds_read_b64_tr_b16 v[156:157], v50 offset:5632\n"
      "v_pk_mul_f32 v[72:73], v[226:227], v[80:81] op_sel:[1,1] op_sel_hi:[1,0] neg_lo:[1,0]\n"
      "ds_read_b64_tr_b16 v[158:159], v50 offset:6144\n"
      "s_waitcnt lgkmcnt(7)\n"
      "v_mfma_f32_16x16x32_f16 v[56:59], v[178:181], v[146:149], v[56:59]\n"
      "ds_read_b64_tr_b16 v[160:161], v50 offset:6656\n"
      "v_pk_mul_f32 v[64:65], v[202:203], v[82:83] op_sel:[1,1] op_sel_hi:[1,0] neg_lo:[1,0]\n"
      "ds_read_b64_tr_b16 v[162:163], v50 offset:7168\n"
      "v_pk_fma_f32 v[182:183], v[226:227], v[80:81], v[72:73] op_sel:[0,0,0] op_sel_hi:[0,1,1]\n"
      "ds_read_b64_tr_b16 v[164:165], v50 offset:7680\n"
      "v_pk_fma_f32 v[204:205], v[202:203], v[82:83], v[64:65] op_sel:[0,0,0] op_sel_hi:[0,1,1]\n"
      "ds_write_b128 v46, v[118:121] offset:0\n"
      "v_pk_mul_f32 v[74:75], v[182:183], v[80:81] op_sel:[1,1] op_sel_hi:[1,0] neg_lo:[1,0]\n"
      "v_pk_mul_f32 v[64:65], v[214:215], v[84:85] op_sel:[1,1] op_sel_hi:[1,0] neg_lo:[1,0]\n"
      "v_pk_mul_f32 v[66:67], v[210:211], v[82:83] op_sel:[1,1] op_sel_hi:[1,0] neg_lo:[1,0]\n"
      "v_pk_fma_f32 v[184:185], v[182:183], v[80:81], v[74:75] op_sel:[0,0,0] op_sel_hi:[0,1,1]\n"
      "v_pk_fma_f32 v[216:217], v[214:215], v[84:85], v[64:65] op_sel:[0,0,0] op_sel_hi:[0,1,1]\n"
      "v_pk_fma_f32 v[212:213], v[210:211], v[82:83], v[66:67] op_sel:[0,0,0] op_sel_hi:[0,1,1]\n"
      "v_pk_mul_f32 v[64:65], v[184:185], v[80:81] op_sel:[1,1] op_sel_hi:[1,0] neg_lo:[1,0]\n"
      "v_pk_mul_f32 v[66:67], v[218:219], v[84:85] op_sel:[1,1] op_sel_hi:[1,0] neg_lo:[1,0]\n"
      "v_pk_fma_f32 v[186:187], v[184:185], v[80:81], v[64:65] op_sel:[0,0,0] op_sel_hi:[0,1,1]\n"
      "v_pk_fma_f32 v[220:221], v[218:219], v[84:85], v[66:67] op_sel:[0,0,0] op_sel_hi:[0,1,1]\n"
      "v_pk_mul_f32 v[66:67], v[186:187], v[80:81] op_sel:[1,1] op_sel_hi:[1,0] neg_lo:[1,0]\n"
      "v_pk_mul_f32 v[68:69], v[222:223], v[84:85] op_sel:[1,1] op_sel_hi:[1,0] neg_lo:[1,0]\n"
      "ds_write_b128 v48, v[122:125] offset:0\n"
      "v_pk_fma_f32 v[188:189], v[186:187], v[80:81], v[66:67] op_sel:[0,0,0] op_sel_hi:[0,1,1]\n"
      "v_pk_fma_f32 v[224:225], v[222:223], v[84:85], v[68:69] op_sel:[0,0,0] op_sel_hi:[0,1,1]\n"
      "s_waitcnt lgkmcnt(8)\n"
      "v_mfma_f32_16x16x32_f16 v[60:63], v[150:153], v[134:137], v[60:63]\n"
      "v_pk_mul_f32 v[68:69], v[188:189], v[80:81] op_sel:[1,1] op_sel_hi:[1,0] neg_lo:[1,0]\n"
      "v_pk_mul_f32 v[70:71], v[226:227], v[84:85] op_sel:[1,1] op_sel_hi:[1,0] neg_lo:[1,0]\n"
      "v_pk_fma_f32 v[190:191], v[188:189], v[80:81], v[68:69] op_sel:[0,0,0] op_sel_hi:[0,1,1]\n"
      "s_waitcnt lgkmcnt(6)\n"
      "v_mfma_f32_16x16x32_f16 v[60:63], v[154:157], v[138:141], v[60:63]\n"
      "v_pk_fma_f32 v[228:229], v[226:227], v[84:85], v[70:71] op_sel:[0,0,0] op_sel_hi:[0,1,1]\n"
      "v_cvt_pk_f16_f32 v118, v198, v200\n"
      "v_cvt_pk_f16_f32 v119, v202, v204\n"
      "s_waitcnt lgkmcnt(4)\n"
      "v_mfma_f32_16x16x32_f16 v[60:63], v[158:161], v[142:145], v[60:63]\n"
      "v_cvt_pk_f16_f32 v120, v206, v208\n"
      "ds_write_b128 v46, v[126:129] offset:4096\n"
      "v_cvt_pk_f16_f32 v121, v210, v212\n"
      "s_waitcnt lgkmcnt(3)\n"
      "v_mfma_f32_16x16x32_f16 v[60:63], v[162:165], v[146:149], v[60:63]\n"
      "v_pk_mul_f32 v[70:71], v[190:191], v[80:81] op_sel:[1,1] op_sel_hi:[1,0] neg_lo:[1,0]\n"
      "v_cvt_pk_f16_f32 v122, v199, v201\n"
      "v_cvt_pk_f16_f32 v123, v203, v205\n"
      "v_cvt_pk_f16_f32 v124, v207, v209\n"
      "v_cvt_pk_f16_f32 v125, v211, v213\n"
      "v_cvt_pk_f16_f32 v126, v214, v216\n"
      "v_cvt_pk_f16_f32 v127, v218, v220\n"
      "v_cvt_pk_f16_f32 v128, v222, v224\n"
      "v_cvt_pk_f16_f32 v129, v226, v228\n"
      "v_pk_fma_f32 v[192:193], v[190:191], v[80:81], v[70:71] op_sel:[0,0,0] op_sel_hi:[0,1,1]\n"
      "ds_write_b128 v48, v[130:133] offset:4096\n"
      "v_cvt_pk_f16_f32 v130, v215, v217\n"
      "v_cvt_pk_f16_f32 v131, v219, v221\n"
      "v_cvt_pk_f16_f32 v132, v223, v225\n"
      "v_cvt_pk_f16_f32 v133, v227, v229\n"
      "v_pk_mul_f32 v[72:73], v[192:193], v[80:81] op_sel:[1,1] op_sel_hi:[1,0] neg_lo:[1,0]\n"
      "ds_write_b128 v47, v[102:105] offset:0\n"
      "v_pk_fma_f32 v[194:195], v[192:193], v[80:81], v[72:73] op_sel:[0,0,0] op_sel_hi:[0,1,1]\n"
      "v_cvt_pk_f16_f32 v102, v182, v184\n"
      "v_pk_mul_f32 v[74:75], v[194:195], v[80:81] op_sel:[1,1] op_sel_hi:[1,0] neg_lo:[1,0]\n"
      "v_cvt_pk_f16_f32 v103, v186, v188\n"
      "v_pk_fma_f32 v[196:197], v[194:195], v[80:81], v[74:75] op_sel:[0,0,0] op_sel_hi:[0,1,1]\n"
      "v_cvt_pk_f16_f32 v104, v190, v192\n"
      "v_cvt_pk_f16_f32 v105, v194, v196\n"
      "v_pk_mul_f32 v[64:65], v[196:197], v[80:81] op_sel:[1,1] op_sel_hi:[1,0] neg_lo:[1,0]\n"
      "ds_write_b128 v49, v[106:109] offset:0\n"
      "v_cvt_pk_f16_f32 v106, v183, v185\n"
      "v_cvt_pk_f16_f32 v107, v187, v189\n"
      "v_cvt_pk_f16_f32 v108, v191, v193\n"
      "v_cvt_pk_f16_f32 v109, v195, v197\n"
      "v_pk_fma_f32 v[198:199], v[196:197], v[80:81], v[64:65] op_sel:[0,0,0] op_sel_hi:[0,1,1]\n"
      "ds_write_b128 v47, v[110:113] offset:4096\n"
      "v_add_f32_e64 v45, v45, v198\n"
      "ds_write_b128 v49, v[114:117] offset:4096\n"
      "ds_read_b64_tr_b16 v[134:135], v50 offset:0\n"
      "ds_read_b64_tr_b16 v[136:137], v50 offset:512\n"
      "ds_read_b64_tr_b16 v[138:139], v50 offset:1024\n"
      "ds_read_b64_tr_b16 v[140:141], v50 offset:1536\n"
      "ds_read_b64_tr_b16 v[142:143], v50 offset:2048\n"
      "ds_read_b64_tr_b16 v[144:145], v50 offset:2560\n"
      "ds_read_b64_tr_b16 v[146:147], v50 offset:3072\n"
      "ds_read_b64_tr_b16 v[148:149], v50 offset:3584\n"
      "ds_read_b64_tr_b16 v[150:151], v50 offset:4096\n"
      "ds_read_b64_tr_b16 v[152:153], v50 offset:4608\n"
      "ds_read_b64_tr_b16 v[154:155], v50 offset:5120\n"
      "ds_read_b64_tr_b16 v[156:157], v50 offset:5632\n"
      "ds_read_b64_tr_b16 v[158:159], v50 offset:6144\n"
      "ds_read_b64_tr_b16 v[160:161], v50 offset:6656\n"
      "ds_read_b64_tr_b16 v[162:163], v50 offset:7168\n"
      "ds_read_b64_tr_b16 v[164:165], v50 offset:7680\n"
      "ds_write_b128 v46, v[118:121] offset:0\n"
      "ds_write_b128 v48, v[122:125] offset:0\n"
      "ds_write_b128 v47, v[126:129] offset:0\n"
      "ds_write_b128 v49, v[130:133] offset:0\n"
      "ds_read_b64_tr_b16 v[166:167], v50 offset:0\n"
      "ds_read_b64_tr_b16 v[168:169], v50 offset:512\n"
      "ds_read_b64_tr_b16 v[170:171], v50 offset:1024\n"
      "ds_read_b64_tr_b16 v[172:173], v50 offset:1536\n"
      "ds_read_b64_tr_b16 v[174:175], v50 offset:2048\n"
      "ds_read_b64_tr_b16 v[176:177], v50 offset:2560\n"
      "ds_read_b64_tr_b16 v[178:179], v50 offset:3072\n"
      "ds_read_b64_tr_b16 v[180:181], v50 offset:3584\n"
      "ds_write_b128 v46, v[102:105] offset:4096\n"
      "ds_write_b128 v48, v[106:109] offset:4096\n"
      "s_waitcnt lgkmcnt(15)\n"
      "v_mfma_f32_16x16x32_f16 v[52:55], v[150:153], v[134:137], v[52:55]\n"
      "ds_read_b64_tr_b16 v[150:151], v50 offset:4096\n"
      "v_mfma_f32_16x16x32_f16 v[52:55], v[154:157], v[138:141], v[52:55]\n"
      "ds_read_b64_tr_b16 v[152:153], v50 offset:4608\n"
      "v_mfma_f32_16x16x32_f16 v[52:55], v[158:161], v[142:145], v[52:55]\n"
      "ds_read_b64_tr_b16 v[154:155], v50 offset:5120\n"
      "s_waitcnt lgkmcnt(15)\n"
      "v_mfma_f32_16x16x32_f16 v[52:55], v[162:165], v[146:149], v[52:55]\n"
      "ds_read_b64_tr_b16 v[156:157], v50 offset:5632\n"
      "ds_read_b64_tr_b16 v[158:159], v50 offset:6144\n"
      "ds_read_b64_tr_b16 v[160:161], v50 offset:6656\n"
      "ds_read_b64_tr_b16 v[162:163], v50 offset:7168\n"
      "ds_read_b64_tr_b16 v[164:165], v50 offset:7680\n"
      "s_waitcnt lgkmcnt(15)\n"
      "v_mfma_f32_16x16x32_f16 v[56:59], v[166:169], v[134:137], v[56:59]\n"
      "s_waitcnt lgkmcnt(14)\n"
      "v_mfma_f32_16x16x32_f16 v[56:59], v[170:173], v[138:141], v[56:59]\n"
      "s_waitcnt lgkmcnt(12)\n"
      "v_mfma_f32_16x16x32_f16 v[56:59], v[174:177], v[142:145], v[56:59]\n"
      "s_waitcnt lgkmcnt(10)\n"
      "v_mfma_f32_16x16x32_f16 v[56:59], v[178:181], v[146:149], v[56:59]\n"
      "s_waitcnt lgkmcnt(6)\n"
      "v_mfma_f32_16x16x32_f16 v[60:63], v[150:153], v[134:137], v[60:63]\n"
      "s_waitcnt lgkmcnt(4)\n"
      "v_mfma_f32_16x16x32_f16 v[60:63], v[154:157], v[138:141], v[60:63]\n"
      "s_waitcnt lgkmcnt(2)\n"
      "v_mfma_f32_16x16x32_f16 v[60:63], v[158:161], v[142:145], v[60:63]\n"
      "s_waitcnt lgkmcnt(0)\n"
      "v_mfma_f32_16x16x32_f16 v[60:63], v[162:165], v[146:149], v[60:63]\n"
      "s_add_u32 s52, s81, 8608\n"
      "v_cvt_pk_f16_f32 v70, v52, v53\n"
      "v_cvt_pk_f16_f32 v71, v54, v55\n"
      "v_cvt_pk_f16_f32 v72, v56, v57\n"
      "v_cvt_pk_f16_f32 v73, v58, v59\n"
      "s_nop 2\n"
      "v_cvt_pk_f16_f32 v74, v60, v61\n"
      "v_cvt_pk_f16_f32 v75, v62, v63\n"
      "v_and_b32 v65, 1, v3\n"
      "v_lshlrev_b32 v65, 3, v65\n"
      "v_lshl_add_u32 v65, v4, 4, v65\n"
      "v_lshrrev_b32 v66, 1, v3\n"
      "v_lshl_add_u32 v65, v66, 8, v65\n"
      "v_add_u32 v65, s52, v65\n"
      "ds_write_b64 v65, v[70:71]\n"
      "v_cmp_gt_u32 vcc, 2, v3\n"
      "v_lshlrev_b32 v66, 3, v4\n"
      "v_lshlrev_b32 v67, 2, v4\n"
      "v_cndmask_b32 v66, v67, v66, vcc\n"
      "v_lshl_add_u32 v66, v3, 7, v66\n"
      "v_add_u32 v66, s52, v66\n"
      "v_add_u32 v66, 512, v66\n"
      "v_mov_b32 v67, 64\n"
      "v_cndmask_b32 v67, v67, 4, vcc\n"
      "v_add_u32 v67, v66, v67\n"
      "ds_write_b32 v66, v72\n"
      "ds_write_b32 v67, v73\n"
      "v_lshlrev_b32 v68, 1, v4\n"
      "v_lshl_add_u32 v68, v3, 7, v68\n"
      "v_add_u32 v68, s52, v68\n"
      "ds_write_b16 v68, v74 offset:1024\n"
      "ds_write_b16_d16_hi v68, v74 offset:1056\n"
      "ds_write_b16 v68, v75 offset:1088\n"
      "ds_write_b16_d16_hi v68, v75 offset:1120\n"
      "v_mov_b32 v76, 0\n"
      "v_and_b32 v69, 7, v1\n"
      "v_lshlrev_b32 v69, 2, v69\n"
      "v_add_u32 v69, s52, v69\n"
      "v_subrev_u32 v69, 32, v69\n"
      "ds_write_b32 v69, v76\n"
      "v_add_f32_dpp v45, v45, v45 quad_perm:[1,0,3,2] row_mask:0xf bank_mask:0xf bound_ctrl:1\n"
      "s_nop 1\n"
      "v_add_f32_dpp v45, v45, v45 quad_perm:[2,3,0,1] row_mask:0xf bank_mask:0xf bound_ctrl:1\n"
      "s_nop 1\n"
      "v_add_f32_dpp v45, v45, v45 row_half_mirror row_mask:0xf bank_mask:0xf bound_ctrl:1\n"
      "s_nop 1\n"
      "v_add_f32_dpp v45, v45, v45 row_mirror row_mask:0xf bank_mask:0xf bound_ctrl:1\n"
      "v_mov_b32 v77, v45\n"
      "s_nop 1\n"
      "v_permlane16_swap_b32 v77, v45\n"
      "v_add_f32 v45, v77, v45\n"
      "v_mov_b32 v77, v45\n"
      "s_nop 1\n"
      "v_permlane32_swap_b32 v77, v45\n"
      "v_add_f32 v45, v77, v45\n"
      "v_cvt_pk_f16_f32 v77, v45, v45\n"
      "v_mov_b32 v78, s52\n"
      "ds_write_b32 v78, v77 offset:1280\n"
      "v_readfirstlane_b32 s56, v5\n"
      "s_add_u32 s56, s56, 1\n"
      "s_waitcnt vmcnt(0)\n"
      "s_mov_b64 exec, 3\n"
      "v_cmp_ne_u32 vcc, s56, v240\n"
      "s_mov_b64 exec, -1\n"
      "s_cbranch_vccnz K2_spin_1\n"
      "K2_cont_2:\n"
      "s_mov_b32 s82, 0x03020504\n"
      "s_mov_b32 s83, 0x01000302\n"
      "s_mov_b32 s84, 0x01000100\n"
      "s_mov_b32 s85, 0x03020302\n"
      "s_mov_b32 s86, 0x05040100\n"
      "s_mov_b32 s88, 0xaaaaaaaa\n"
      "s_mov_b32 s89, 0xaaaaaaaa\n"
      "s_mov_b32 s90, 0\n"
      "s_mov_b32 s91, 2\n"
      "buffer_load_dwordx4 a[0:3], v241, s[32:35], 0 offen offset:0 sc1\n"
      "v_lshl_add_u32 v64, v1, 4, s52\n"
      "v_lshl_add_u32 v66, v1, 3, s52\n"
      "v_subrev_u32_e64 v69, 32, v64\n"
      "v_lshl_add_u32 v67, v1, 2, s52\n"
      "buffer_load_dwordx4 a[4:7], v241, s[32:35], 0 offen offset:1024 sc1\n"
      "ds_read_b128 v[72:75], v69\n"
      "ds_read_b32 v76, v69 offset:16\n"
      "ds_read_b64 v[78:79], v66 offset:512\n"
      "ds_read_b32 v80, v66 offset:520\n"
      "buffer_load_dwordx4 a[8:11], v241, s[32:35], 0 offen offset:2048 sc1\n"
      "v_lshl_add_u32 v68, v1, 1, s52\n"
      "ds_read_b32 v82, v67 offset:768\n"
      "ds_read_b32 v83, v67 offset:772\n"
      "ds_read_u16 v84, v68 offset:1024\n"
      "buffer_load_dwordx4 a[12:15], v241, s[32:35], 0 offen offset:3072 sc1\n"
      "ds_read_u16 v85, v68 offset:1026\n"
      "ds_read_u16 v86, v68 offset:1152\n"
      "ds_read_u16 v87, v68 offset:1154\n"
      "v_sub_u32 v65, s81, v2\n"
      "buffer_load_dwordx4 a[16:19], v241, s[32:35], s65 offen offset:0 sc1\n"
      "s_waitcnt lgkmcnt(9)\n"
      "v_perm_b32 v97, v75, v74, s82\n"
      "s_waitcnt lgkmcnt(8)\n"
      "s_nop 0\n"
      "v_perm_b32 v77, v76, v76, s84\n"
      "v_perm_b32 v98, v74, v73, s82\n"
      "v_cndmask_b32 v76, v76, v77, s[90:91]\n"
      "buffer_load_dwordx4 a[20:23], v241, s[32:35], s65 offen offset:1024 sc1\n"
      "v_perm_b32 v99, v73, v72, s82\n"
      "v_perm_b32 v96, v76, v75, s82\n"
      "v_perm_b32 v100, v76, v76, s83\n"
      "s_waitcnt lgkmcnt(3)\n"
      "v_perm_b32 v123, v84, v84, s84\n"
      "buffer_load_dwordx4 a[24:27], v241, s[32:35], s65 offen offset:2048 sc1\n"
      "s_waitcnt lgkmcnt(2)\n"
      "v_perm_b32 v88, v84, v85, s86\n"
      "v_perm_b32 v124, v85, v85, s84\n"
      "s_waitcnt lgkmcnt(1)\n"
      "v_perm_b32 v131, v86, v86, s84\n"
      "s_waitcnt lgkmcnt(0)\n"
      "v_perm_b32 v89, v86, v87, s86\n"
      "buffer_load_dwordx4 a[28:31], v241, s[32:35], s65 offen offset:3072 sc1\n"
      "v_perm_b32 v132, v87, v87, s84\n"
      "v_perm_b32 v101, v75, v75, s83\n"
      "v_perm_b32 v102, v74, v74, s83\n"
      "v_perm_b32 v103, v73, v73, s83\n"
      "buffer_load_dwordx4 a[32:35], v241, s[32:35], s66 offen offset:0 sc1\n"
      "v_perm_b32 v104, v80, v80, s84\n"
      "v_perm_b32 v105, v79, v79, s83\n"
      "v_perm_b32 v106, v79, v79, s84\n"
      "v_perm_b32 v107, v78, v78, s83\n"
      "buffer_load_dwordx4 a[36:39], v241, s[32:35], s66 offen offset:1024 sc1\n"
      "v_perm_b32 v108, v80, v80, s84\n"
      "v_perm_b32 v109, v80, v79, s82\n"
      "v_perm_b32 v110, v79, v79, s84\n"
      "v_perm_b32 v111, v79, v78, s82\n"
      "buffer_load_dwordx4 a[40:43], v241, s[32:35], s66 offen offset:2048 sc1\n"
      "v_perm_b32 v112, v83, v83, s84\n"
      "v_perm_b32 v113, v83, v82, s82\n"
      "v_perm_b32 v114, v82, v82, s85\n"
      "v_perm_b32 v115, v82, v82, s84\n"
      "buffer_load_dwordx4 a[44:47], v241, s[32:35], s66 offen offset:3072 sc1\n"
      "v_perm_b32 v116, v83, v83, s84\n"
      "v_perm_b32 v117, v83, v83, s84\n"
      "v_perm_b32 v118, v82, v82, s85\n"
      "v_perm_b32 v119, v82, v82, s83\n"
      "buffer_load_dwordx4 a[48:51], v241, s[32:35], s67 offen offset:0 sc1\n"
      "v_perm_b32 v120, v85, v85, s84\n"
      "v_perm_b32 v121, v85, v85, s84\n"
      "v_cndmask_b32 v122, v123, v88, s[88:89]\n"
      "v_perm_b32 v125, v85, v85, s84\n"
      "buffer_load_dwordx4 a[52:55], v241, s[32:35], s67 offen offset:1024 sc1\n"
      "v_perm_b32 v127, v84, v84, s84\n"
      "v_cndmask_b32 v126, v88, v124, s[88:89]\n"
      "v_perm_b32 v128, v87, v87, s84\n"
      "v_perm_b32 v129, v87, v87, s84\n"
      "buffer_load_dwordx4 a[56:59], v241, s[32:35], s67 offen offset:2048 sc1\n"
      "v_cndmask_b32 v130, v131, v89, s[88:89]\n"
      "v_perm_b32 v133, v87, v87, s84\n"
      "v_perm_b32 v135, v86, v86, s84\n"
      "v_cndmask_b32 v134, v89, v132, s[88:89]\n"
      "buffer_load_dwordx4 a[60:63], v241, s[32:35], s67 offen offset:3072 sc1\n"
      "buffer_load_dwordx4 a[64:67], v241, s[32:35], s68 offen offset:0 sc1\n"
      "buffer_load_dwordx4 a[68:71], v241, s[32:35], s68 offen offset:1024 sc1\n"
      "buffer_load_dwordx4 a[72:75], v241, s[32:35], s68 offen offset:2048 sc1\n"
      "buffer_load_dwordx4 a[76:79], v241, s[32:35], s68 offen offset:3072 sc1\n"
      "ds_write_b128 v65, v[112:115] offset:3088\n"
      "ds_write_b128 v65, v[116:119] offset:7376\n"
      "ds_write_b128 v65, v[120:123] offset:2064\n"
      "ds_write_b128 v65, v[124:127] offset:6352\n"
      "ds_write_b128 v65, v[128:131] offset:1040\n"
      "ds_write_b128 v65, v[132:135] offset:5328\n"
      "s_mov_b32 exec_hi, 3\n"
      "ds_write_b128 v65, v[96:99] offset:4144\n"
      "ds_write_b128 v65, v[100:103] offset:8432\n"
      "s_mov_b32 exec_hi, 0\n"
      "ds_write_b128 v65, v[104:107] offset:3600\n"
      "ds_write_b128 v65, v[108:111] offset:7888\n"
      "s_mov_b64 exec, -1\n"
      "s_mov_b32 s44, s32\n"
      "s_mov_b32 s45, s33\n"
      "s_mov_b32 s47, s35\n"
      "s_cmp_lt_u32 s58, 16\n"
      "s_cselect_b32 s46, s34, 0\n"
      "s_bfe_u32 s85, s30, 0x20002\n"
      "s_lshl_b32 s85, s85, 13\n"
      "s_add_u32 s85, s85, 229376\n"
      "v_lshl_add_u32 v56, v3, 3, v7\n"
      "v_mul_u32_u24 v56, 192, v56\n"
      "v_lshl_add_u32 v56, v8, 3, v56\n"
      "v_and_b32 v57, 1, v3\n"
      "v_lshlrev_b32 v57, 5, v57\n"
      "v_add_u32 v96, v56, v57\n"
      "v_xor_b32 v57, 32, v57\n"
      "v_add_u32 v97, v56, v57\n"
      "v_add_u32 v98, 61440, v96\n"
      "v_add_u32 v99, 61440, v97\n"
      "s_mul_i32 s53, s94, 48\n"
      "s_mul_i32 s54, s60, 3\n"
      "s_add_u32 s53, s53, s54\n"
      "s_lshl_b32 s53, s53, 10\n"
      "s_add_u32 s83, s53, 12582912\n"
      "s_lshl_b32 s53, s60, 7\n"
      "s_add_u32 s53, s53, s94\n"
      "s_lshl_b32 s53, s53, 7\n"
      "s_add_u32 s84, s53, 262144\n"
      "s_lshl_b32 s53, s30, 14\n"
      "s_lshl_b32 s54, s31, 10\n"
      "s_add_u32 s53, s53, s54\n"
      "s_add_u32 s53, s53, 4194304\n"
      "v_add_u32 v125, s53, v2\n"
      "v_mul_u32_u24 v124, 4640, v8\n"
      "v_lshrrev_b32 v56, 2, v1\n"
      "s_lshl_b32 s54, s31, 8\n"
      "s_add_u32 s54, s54, 512\n"
      "v_lshl_add_u32 v56, v56, 4, s54\n"
      "v_add_u32 v124, v124, v56\n"
      "buffer_load_dword v104, v11, s[32:35], s85 offen sc1\n"
      "v_lshlrev_b32 v16, 16, v242\n"
      "v_and_b32 v17, s63, v242\n"
      "v_lshlrev_b32 v18, 16, v243\n"
      "v_and_b32 v19, s63, v243\n"
      "v_lshlrev_b32 v20, 16, v244\n"
      "v_and_b32 v21, s63, v244\n"
      "v_lshlrev_b32 v22, 16, v245\n"
      "v_and_b32 v23, s63, v245\n"
      "v_lshlrev_b32 v24, 16, v246\n"
      "v_and_b32 v25, s63, v246\n"
      "v_lshlrev_b32 v26, 16, v247\n"
      "v_and_b32 v27, s63, v247\n"
      "v_lshlrev_b32 v28, 16, v248\n"
      "v_and_b32 v29, s63, v248\n"
      "v_lshlrev_b32 v30, 16, v249\n"
      "v_and_b32 v31, s63, v249\n"
      "v_lshlrev_b32 v32, 16, v250\n"
      "v_and_b32 v33, s63, v250\n"
      "v_lshlrev_b32 v34, 16, v251\n"
      "v_and_b32 v35, s63, v251\n"
      "v_lshlrev_b32 v36, 16, v252\n"
      "v_and_b32 v37, s63, v252\n"
      "v_lshlrev_b32 v38, 16, v253\n"
      "v_and_b32 v39, s63, v253\n"
      "s_barrier\n"
      "s_cmp_eq_u32 s31, 0\n"
      "s_cbranch_scc0 K2_bf_3\n"
      "s_lshl_b32 s52, s30, 7\n"
      "s_add_u32 s52, s52, 65536\n"
      "v_mov_b32 v6, s56\n"
      "s_mov_b64 exec, 1\n"
      "buffer_store_dword v6, off, s[32:35], s52 sc1\n"
      "s_mov_b64 exec, -1\n"
      "K2_bf_3:\n"
      "s_nop 0\n"
      "ds_read_b64_tr_b16 v[64:65], v96 offset:0\n"
      "ds_read_b64_tr_b16 v[66:67], v96 offset:768\n"
      "ds_read_b64_tr_b16 v[68:69], v97 offset:0\n"
      "ds_read_b64_tr_b16 v[70:71], v97 offset:768\n"
      "ds_read_b64_tr_b16 v[72:73], v96 offset:64\n"
      "ds_read_b64_tr_b16 v[74:75], v96 offset:832\n"
      "ds_read_b64_tr_b16 v[76:77], v97 offset:64\n"
      "ds_read_b64_tr_b16 v[78:79], v97 offset:832\n"
      "ds_read_b64_tr_b16 v[80:81], v96 offset:128\n"
      "ds_read_b64_tr_b16 v[82:83], v96 offset:896\n"
      "ds_read_b64_tr_b16 v[84:85], v97 offset:128\n"
      "ds_read_b64_tr_b16 v[86:87], v97 offset:896\n"
      "ds_read_b64_tr_b16 v[88:89], v96 offset:6144\n"
      "ds_read_b64_tr_b16 v[90:91], v96 offset:6912\n"
      "ds_read_b64_tr_b16 v[92:93], v97 offset:6144\n"
      "ds_read_b64_tr_b16 v[94:95], v97 offset:6912\n"
      "s_mov_b32 s92, 94208\n"
      "s_waitcnt vmcnt(20) lgkmcnt(14)\n"
      "v_mfma_f32_16x16x32_bf16 v[16:19], v[64:67], a[0:3], v[16:19]\n"
      "ds_read_b64_tr_b16 v[64:65], v96 offset:6208\n"
      "ds_read_b64_tr_b16 v[66:67], v96 offset:6976\n"
      "s_waitcnt lgkmcnt(14)\n"
      "v_mfma_f32_16x16x32_bf16 v[20:23], v[68:71], a[0:3], v[20:23]\n"
      "ds_read_b64_tr_b16 v[68:69], v97 offset:6208\n"
      "ds_read_b64_tr_b16 v[70:71], v97 offset:6976\n"
      "s_waitcnt lgkmcnt(12)\n"
      "v_mfma_f32_16x16x32_bf16 v[24:27], v[72:75], a[0:3], v[24:27]\n"
      "ds_read_b64_tr_b16 v[72:73], v96 offset:6272\n"
      "ds_read_b64_tr_b16 v[74:75], v96 offset:7040\n"
      "v_mfma_f32_16x16x32_bf16 v[28:31], v[76:79], a[0:3], v[28:31]\n"
      "ds_read_b64_tr_b16 v[76:77], v97 offset:6272\n"
      "ds_read_b64_tr_b16 v[78:79], v97 offset:7040\n"
      "s_waitcnt lgkmcnt(12)\n"
      "v_mfma_f32_16x16x32_bf16 v[32:35], v[80:83], a[0:3], v[32:35]\n"
      "ds_read_b64_tr_b16 v[80:81], v96 offset:12288\n"
      "ds_read_b64_tr_b16 v[82:83], v96 offset:13056\n"
      "v_and_b32 v60, 1, v4\n"
      "v_mul_u32_u24 v60, 4288, v60\n"
      "v_mfma_f32_16x16x32_bf16 v[36:39], v[84:87], a[0:3], v[36:39]\n"
      "ds_read_b64_tr_b16 v[84:85], v97 offset:12288\n"
      "ds_read_b64_tr_b16 v[86:87], v97 offset:13056\n"
      "s_waitcnt lgkmcnt(12)\n"
      "v_lshrrev_b32 v61, 1, v4\n"
      "s_waitcnt vmcnt(19)\n"
      "v_mfma_f32_16x16x32_bf16 v[16:19], v[88:91], a[4:7], v[16:19]\n"
      "ds_read_b64_tr_b16 v[88:89], v96 offset:12352\n"
      "ds_read_b64_tr_b16 v[90:91], v96 offset:13120\n"
      "v_lshlrev_b32 v62, 2, v3\n"
      "v_mfma_f32_16x16x32_bf16 v[20:23], v[92:95], a[4:7], v[20:23]\n"
      "ds_read_b64_tr_b16 v[92:93], v97 offset:12352\n"
      "ds_read_b64_tr_b16 v[94:95], v97 offset:13120\n"
      "s_waitcnt lgkmcnt(12)\n"
      "v_sub_u32 v61, v62, v61\n"
      "v_mfma_f32_16x16x32_bf16 v[24:27], v[64:67], a[4:7], v[24:27]\n"
      "ds_read_b64_tr_b16 v[64:65], v96 offset:12416\n"
      "ds_read_b64_tr_b16 v[66:67], v96 offset:13184\n"
      "v_lshl_add_u32 v60, v61, 2, v60\n"
      "v_mfma_f32_16x16x32_bf16 v[28:31], v[68:71], a[4:7], v[28:31]\n"
      "ds_read_b64_tr_b16 v[68:69], v97 offset:12416\n"
      "ds_read_b64_tr_b16 v[70:71], v97 offset:13184\n"
      "s_waitcnt lgkmcnt(12)\n"
      "s_add_u32 s52, s81, 3136\n"
      "v_mfma_f32_16x16x32_bf16 v[32:35], v[72:75], a[4:7], v[32:35]\n"
      "ds_read_b64_tr_b16 v[72:73], v96 offset:18432\n"
      "ds_read_b64_tr_b16 v[74:75], v96 offset:19200\n"
      "v_add_u32 v224, s52, v60\n"
      "v_mfma_f32_16x16x32_bf16 v[36:39], v[76:79], a[4:7], v[36:39]\n"
      "ds_read_b64_tr_b16 v[76:77], v97 offset:18432\n"
      "ds_read_b64_tr_b16 v[78:79], v97 offset:19200\n"
      "s_waitcnt lgkmcnt(12)\n"
      "v_subrev_u32 v225, 1024, v224\n"
      "s_waitcnt vmcnt(18)\n"
      "v_mfma_f32_16x16x32_bf16 v[16:19], v[80:83], a[8:11], v[16:19]\n"
      "ds_read_b64_tr_b16 v[80:81], v96 offset:18496\n"
      "ds_read_b64_tr_b16 v[82:83], v96 offset:19264\n"
      "v_subrev_u32 v226, 2048, v224\n"
      "v_mfma_f32_16x16x32_bf16 v[20:23], v[84:87], a[8:11], v[20:23]\n"
      "ds_read_b64_tr_b16 v[84:85], v97 offset:18496\n"
      "ds_read_b64_tr_b16 v[86:87], v97 offset:19264\n"
      "s_waitcnt lgkmcnt(12)\n"
      "v_subrev_u32 v227, 3072, v224\n"
      "v_mfma_f32_16x16x32_bf16 v[24:27], v[88:91], a[8:11], v[24:27]\n"
      "ds_read_b64_tr_b16 v[88:89], v96 offset:18560\n"
      "ds_read_b64_tr_b16 v[90:91], v96 offset:19328\n"
      "s_mul_i32 s52, s31, 4640\n"
      "v_lshlrev_b32 v60, 5, v4\n"
      "v_mfma_f32_16x16x32_bf16 v[28:31], v[92:95], a[8:11], v[28:31]\n"
      "ds_read_b64_tr_b16 v[92:93], v97 offset:18560\n"
      "ds_read_b64_tr_b16 v[94:95], v97 offset:19328\n"
      "s_waitcnt lgkmcnt(12)\n"
      "v_lshl_add_u32 v61, v3, 4, v60\n"
      "v_mfma_f32_16x16x32_bf16 v[32:35], v[64:67], a[8:11], v[32:35]\n"
      "ds_read_b64_tr_b16 v[64:65], v96 offset:24576\n"
      "ds_read_b64_tr_b16 v[66:67], v96 offset:25344\n"
      "v_add_u32 v61, s52, v61\n"
      "v_mfma_f32_16x16x32_bf16 v[36:39], v[68:71], a[8:11], v[36:39]\n"
      "ds_read_b64_tr_b16 v[68:69], v97 offset:24576\n"
      "ds_read_b64_tr_b16 v[70:71], v97 offset:25344\n"
      "s_waitcnt lgkmcnt(12)\n"
      "v_add_u32 v228, 32, v61\n"
      "s_waitcnt vmcnt(17)\n"
      "v_mfma_f32_16x16x32_bf16 v[16:19], v[72:75], a[12:15], v[16:19]\n"
      "ds_read_b64_tr_b16 v[72:73], v96 offset:24640\n"
      "ds_read_b64_tr_b16 v[74:75], v96 offset:25408\n"
      "v_lshl_add_u32 v61, v3, 3, v60\n"
      "v_mfma_f32_16x16x32_bf16 v[20:23], v[76:79], a[12:15], v[20:23]\n"
      "ds_read_b64_tr_b16 v[76:77], v97 offset:24640\n"
      "ds_read_b64_tr_b16 v[78:79], v97 offset:25408\n"
      "s_waitcnt lgkmcnt(12)\n"
      "v_add_u32 v61, s52, v61\n"
      "v_mfma_f32_16x16x32_bf16 v[24:27], v[80:83], a[12:15], v[24:27]\n"
      "ds_read_b64_tr_b16 v[80:81], v96 offset:24704\n"
      "ds_read_b64_tr_b16 v[82:83], v96 offset:25472\n"
      "v_add_u32 v229, 512, v61\n"
      "v_mfma_f32_16x16x32_bf16 v[28:31], v[84:87], a[12:15], v[28:31]\n"
      "ds_read_b64_tr_b16 v[84:85], v97 offset:24704\n"
      "ds_read_b64_tr_b16 v[86:87], v97 offset:25472\n"
      "s_waitcnt lgkmcnt(12)\n"
      "s_lshl_b32 s52, s31, 2\n"
      "v_mfma_f32_16x16x32_bf16 v[32:35], v[88:91], a[12:15], v[32:35]\n"
      "ds_read_b64_tr_b16 v[88:89], v96 offset:30720\n"
      "ds_read_b64_tr_b16 v[90:91], v96 offset:31488\n"
      "v_add_u32 v60, s52, v3\n"
      "v_mfma_f32_16x16x32_bf16 v[36:39], v[92:95], a[12:15], v[36:39]\n"
      "ds_read_b64_tr_b16 v[92:93], v97 offset:30720\n"
      "ds_read_b64_tr_b16 v[94:95], v97 offset:31488\n"
      "s_waitcnt lgkmcnt(12)\n"
      "v_xor_b32 v60, v60, v4\n"
      "s_waitcnt vmcnt(16)\n"
      "v_mfma_f32_16x16x32_bf16 v[16:19], v[64:67], a[16:19], v[16:19]\n"
      "ds_read_b64_tr_b16 v[64:65], v96 offset:30784\n"
      "ds_read_b64_tr_b16 v[66:67], v96 offset:31552\n"
      "v_lshlrev_b32 v60, 3, v60\n"
      "v_bfe_u32 v61, v4, 1, 1\n"
      "v_mfma_f32_16x16x32_bf16 v[20:23], v[68:71], a[16:19], v[20:23]\n"
      "ds_read_b64_tr_b16 v[68:69], v97 offset:30784\n"
      "ds_read_b64_tr_b16 v[70:71], v97 offset:31552\n"
      "s_waitcnt lgkmcnt(12)\n"
      "v_lshl_add_u32 v60, v61, 7, v60\n"
      "v_mfma_f32_16x16x32_bf16 v[24:27], v[72:75], a[16:19], v[24:27]\n"
      "ds_read_b64_tr_b16 v[72:73], v96 offset:30848\n"
      "ds_read_b64_tr_b16 v[74:75], v96 offset:31616\n"
      "v_and_b32 v61, 1, v3\n"
      "v_mfma_f32_16x16x32_bf16 v[28:31], v[76:79], a[16:19], v[28:31]\n"
      "ds_read_b64_tr_b16 v[76:77], v97 offset:30848\n"
      "ds_read_b64_tr_b16 v[78:79], v97 offset:31616\n"
      "s_waitcnt lgkmcnt(12)\n"
      "v_lshl_add_u32 v60, v61, 8, v60\n"
      "v_mfma_f32_16x16x32_bf16 v[32:35], v[80:83], a[16:19], v[32:35]\n"
      "ds_read_b64_tr_b16 v[80:81], v96 offset:36864\n"
      "ds_read_b64_tr_b16 v[82:83], v96 offset:37632\n"
      "s_lshl_b32 s52, s31, 9\n"
      "v_mfma_f32_16x16x32_bf16 v[36:39], v[84:87], a[16:19], v[36:39]\n"
      "ds_read_b64_tr_b16 v[84:85], v97 offset:36864\n"
      "ds_read_b64_tr_b16 v[86:87], v97 offset:37632\n"
      "s_waitcnt lgkmcnt(12)\n"
      "s_add_u32 s52, s52, 20480\n"
      "s_waitcnt vmcnt(15)\n"
      "v_mfma_f32_16x16x32_bf16 v[16:19], v[88:91], a[20:23], v[16:19]\n"
      "ds_read_b64_tr_b16 v[88:89], v96 offset:36928\n"
      "ds_read_b64_tr_b16 v[90:91], v96 offset:37696\n"
      "v_add_u32 v230, s52, v60\n"
      "v_mfma_f32_16x16x32_bf16 v[20:23], v[92:95], a[20:23], v[20:23]\n"
      "ds_read_b64_tr_b16 v[92:93], v97 offset:36928\n"
      "ds_read_b64_tr_b16 v[94:95], v97 offset:37696\n"
      "s_waitcnt lgkmcnt(12)\n"
      "v_lshlrev_b32 v60, 1, v3\n"
      "v_mfma_f32_16x16x32_bf16 v[24:27], v[64:67], a[20:23], v[24:27]\n"
      "ds_read_b64_tr_b16 v[64:65], v96 offset:36992\n"
      "ds_read_b64_tr_b16 v[66:67], v96 offset:37760\n"
      "v_lshl_add_u32 v61, v7, 2, v8\n"
      "v_mfma_f32_16x16x32_bf16 v[28:31], v[68:71], a[20:23], v[28:31]\n"
      "ds_read_b64_tr_b16 v[68:69], v97 offset:36992\n"
      "ds_read_b64_tr_b16 v[70:71], v97 offset:37760\n"
      "s_waitcnt lgkmcnt(12)\n"
      "v_xor_b32 v60, v60, v61\n"
      "v_mfma_f32_16x16x32_bf16 v[32:35], v[72:75], a[20:23], v[32:35]\n"
      "ds_read_b64_tr_b16 v[72:73], v96 offset:43008\n"
      "ds_read_b64_tr_b16 v[74:75], v96 offset:43776\n"
      "v_lshlrev_b32 v60, 3, v60\n"
      "v_mfma_f32_16x16x32_bf16 v[36:39], v[76:79], a[20:23], v[36:39]\n"
      "ds_read_b64_tr_b16 v[76:77], v97 offset:43008\n"
      "ds_read_b64_tr_b16 v[78:79], v97 offset:43776\n"
      "s_waitcnt lgkmcnt(12)\n"
      "v_and_b32 v61, 1, v3\n"
      "v_lshl_add_u32 v60, v61, 7, v60\n"
      "s_waitcnt vmcnt(14)\n"
      "v_mfma_f32_16x16x32_bf16 v[16:19], v[80:83], a[24:27], v[16:19]\n"
      "ds_read_b64_tr_b16 v[80:81], v96 offset:43072\n"
      "ds_read_b64_tr_b16 v[82:83], v96 offset:43840\n"
      "v_and_b32 v61, 1, v8\n"
      "v_mfma_f32_16x16x32_bf16 v[20:23], v[84:87], a[24:27], v[20:23]\n"
      "ds_read_b64_tr_b16 v[84:85], v97 offset:43072\n"
      "ds_read_b64_tr_b16 v[86:87], v97 offset:43840\n"
      "s_waitcnt lgkmcnt(12)\n"
      "v_lshl_add_u32 v61, v7, 1, v61\n"
      "v_mfma_f32_16x16x32_bf16 v[24:27], v[88:91], a[24:27], v[24:27]\n"
      "ds_read_b64_tr_b16 v[88:89], v96 offset:43136\n"
      "ds_read_b64_tr_b16 v[90:91], v96 offset:43904\n"
      "v_lshl_add_u32 v60, v61, 8, v60\n"
      "v_mfma_f32_16x16x32_bf16 v[28:31], v[92:95], a[24:27], v[28:31]\n"
      "ds_read_b64_tr_b16 v[92:93], v97 offset:43136\n"
      "ds_read_b64_tr_b16 v[94:95], v97 offset:43904\n"
      "s_waitcnt lgkmcnt(12)\n"
      "s_lshl_b32 s52, s31, 12\n"
      "v_mfma_f32_16x16x32_bf16 v[32:35], v[64:67], a[24:27], v[32:35]\n"
      "ds_read_b64_tr_b16 v[64:65], v96 offset:49152\n"
      "ds_read_b64_tr_b16 v[66:67], v96 offset:49920\n"
      "s_add_u32 s52, s52, 20480\n"
      "v_mfma_f32_16x16x32_bf16 v[36:39], v[68:71], a[24:27], v[36:39]\n"
      "ds_read_b64_tr_b16 v[68:69], v97 offset:49152\n"
      "ds_read_b64_tr_b16 v[70:71], v97 offset:49920\n"
      "s_waitcnt lgkmcnt(12)\n"
      "v_add_u32 v231, s52, v60\n"
      "s_waitcnt vmcnt(13)\n"
      "v_mfma_f32_16x16x32_bf16 v[16:19], v[72:75], a[28:31], v[16:19]\n"
      "ds_read_b64_tr_b16 v[72:73], v96 offset:49216\n"
      "ds_read_b64_tr_b16 v[74:75], v96 offset:49984\n"
      "v_xor_b32 v232, 8, v231\n"
      "v_mfma_f32_16x16x32_bf16 v[20:23], v[76:79], a[28:31], v[20:23]\n"
      "ds_read_b64_tr_b16 v[76:77], v97 offset:49216\n"
      "ds_read_b64_tr_b16 v[78:79], v97 offset:49984\n"
      "s_waitcnt lgkmcnt(12)\n"
      "v_xor_b32 v233, 64, v231\n"
      "v_mfma_f32_16x16x32_bf16 v[24:27], v[80:83], a[28:31], v[24:27]\n"
      "ds_read_b64_tr_b16 v[80:81], v96 offset:49280\n"
      "ds_read_b64_tr_b16 v[82:83], v96 offset:50048\n"
      "v_xor_b32 v234, 72, v231\n"
      "v_mfma_f32_16x16x32_bf16 v[28:31], v[84:87], a[28:31], v[28:31]\n"
      "ds_read_b64_tr_b16 v[84:85], v97 offset:49280\n"
      "ds_read_b64_tr_b16 v[86:87], v97 offset:50048\n"
      "s_waitcnt lgkmcnt(12)\n"
      "v_mul_u32_u24 v60, 5120, v3\n"
      "v_lshl_add_u32 v235, v4, 4, v60\n"
      "v_mfma_f32_16x16x32_bf16 v[32:35], v[88:91], a[28:31], v[32:35]\n"
      "ds_read_b64_tr_b16 v[88:89], v96 offset:55296\n"
      "ds_read_b64_tr_b16 v[90:91], v96 offset:56064\n"
      "s_mul_i32 s52, s49, 240\n"
      "v_mfma_f32_16x16x32_bf16 v[36:39], v[92:95], a[28:31], v[36:39]\n"
      "ds_read_b64_tr_b16 v[92:93], v97 offset:55296\n"
      "ds_read_b64_tr_b16 v[94:95], v97 offset:56064\n"
      "s_waitcnt lgkmcnt(12)\n"
      "s_bfe_u32 s53, s48, 0x10001\n"
      "s_waitcnt vmcnt(12)\n"
      "v_mfma_f32_16x16x32_bf16 v[16:19], v[64:67], a[32:35], v[16:19]\n"
      "ds_read_b64_tr_b16 v[64:65], v96 offset:55360\n"
      "ds_read_b64_tr_b16 v[66:67], v96 offset:56128\n"
      "s_barrier\n"
      "s_mul_i32 s55, s31, 4640\n"
      "v_lshl_add_u32 v56, v1, 3, s55\n"
      "v_mov_b32 v58, 0\n"
      "v_mov_b32 v59, 0\n"
      "ds_write_b64 v56, v[58:59]\n"
      "s_lshl1_add_u32 s53, s31, s53\n"
      "v_mfma_f32_16x16x32_bf16 v[20:23], v[68:71], a[32:35], v[20:23]\n"
      "ds_read_b64_tr_b16 v[68:69], v97 offset:55360\n"
      "ds_read_b64_tr_b16 v[70:71], v97 offset:56128\n"
      "s_waitcnt lgkmcnt(13)\n"
      "s_mul_i32 s53, s53, 15\n"
      "v_mfma_f32_16x16x32_bf16 v[24:27], v[72:75], a[32:35], v[24:27]\n"
      "ds_read_b64_tr_b16 v[72:73], v96 offset:55424\n"
      "ds_read_b64_tr_b16 v[74:75], v96 offset:56192\n"
      "s_add_u32 s53, s52, s53\n"
      "v_mfma_f32_16x16x32_bf16 v[28:31], v[76:79], a[32:35], v[28:31]\n"
      "ds_read_b64_tr_b16 v[76:77], v97 offset:55424\n"
      "ds_read_b64_tr_b16 v[78:79], v97 offset:56192\n"
      "s_waitcnt lgkmcnt(13)\n"
      "s_lshl_b32 s53, s53, 10\n"
      "v_mfma_f32_16x16x32_bf16 v[32:35], v[80:83], a[32:35], v[32:35]\n"
      "ds_read_b64_tr_b16 v[80:81], v98 offset:0\n"
      "ds_read_b64_tr_b16 v[82:83], v98 offset:768\n"
      "s_add_u32 s53, s53, 8388608\n"
      "v_mfma_f32_16x16x32_bf16 v[36:39], v[84:87], a[32:35], v[36:39]\n"
      "ds_read_b64_tr_b16 v[84:85], v99 offset:0\n"
      "ds_read_b64_tr_b16 v[86:87], v99 offset:768\n"
      "s_waitcnt lgkmcnt(13)\n"
      "v_add_u32 v12, s53, v2\n"
      "s_waitcnt vmcnt(11)\n"
      "v_mfma_f32_16x16x32_bf16 v[16:19], v[88:91], a[36:39], v[16:19]\n"
      "ds_read_b64_tr_b16 v[88:89], v98 offset:64\n"
      "ds_read_b64_tr_b16 v[90:91], v98 offset:832\n"
      "s_bfe_u32 s53, s48, 0x10001\n"
      "v_mfma_f32_16x16x32_bf16 v[20:23], v[92:95], a[36:39], v[20:23]\n"
      "ds_read_b64_tr_b16 v[92:93], v99 offset:64\n"
      "ds_read_b64_tr_b16 v[94:95], v99 offset:832\n"
      "s_waitcnt lgkmcnt(12)\n"
      "s_lshl1_add_u32 s53, s31, s53\n"
      "v_mfma_f32_16x16x32_bf16 v[24:27], v[64:67], a[36:39], v[24:27]\n"
      "ds_read_b64_tr_b16 v[64:65], v98 offset:128\n"
      "ds_read_b64_tr_b16 v[66:67], v98 offset:896\n"
      "s_add_u32 s53, s53, 8\n"
      "s_mul_i32 s53, s53, 15\n"
      "v_mfma_f32_16x16x32_bf16 v[28:31], v[68:71], a[36:39], v[28:31]\n"
      "ds_read_b64_tr_b16 v[68:69], v99 offset:128\n"
      "ds_read_b64_tr_b16 v[70:71], v99 offset:896\n"
      "s_waitcnt lgkmcnt(12)\n"
      "s_add_u32 s53, s52, s53\n"
      "v_mfma_f32_16x16x32_bf16 v[32:35], v[72:75], a[36:39], v[32:35]\n"
      "ds_read_b64_tr_b16 v[72:73], v98 offset:6144\n"
      "ds_read_b64_tr_b16 v[74:75], v98 offset:6912\n"
      "s_lshl_b32 s53, s53, 10\n"
      "v_mfma_f32_16x16x32_bf16 v[36:39], v[76:79], a[36:39], v[36:39]\n"
      "ds_read_b64_tr_b16 v[76:77], v99 offset:6144\n"
      "ds_read_b64_tr_b16 v[78:79], v99 offset:6912\n"
      "s_waitcnt lgkmcnt(12)\n"
      "s_add_u32 s53, s53, 8388608\n"
      "s_waitcnt vmcnt(10)\n"
      "v_mfma_f32_16x16x32_bf16 v[16:19], v[80:83], a[40:43], v[16:19]\n"
      "ds_read_b64_tr_b16 v[80:81], v98 offset:6208\n"
      "ds_read_b64_tr_b16 v[82:83], v98 offset:6976\n"
      "v_add_u32 v242, s53, v2\n"
      "v_mfma_f32_16x16x32_bf16 v[20:23], v[84:87], a[40:43], v[20:23]\n"
      "ds_read_b64_tr_b16 v[84:85], v99 offset:6208\n"
      "ds_read_b64_tr_b16 v[86:87], v99 offset:6976\n"
      "s_waitcnt lgkmcnt(12)\n"
      "s_bfe_u32 s53, s48, 0x10001\n"
      "v_mfma_f32_16x16x32_bf16 v[24:27], v[88:91], a[40:43], v[24:27]\n"
      "ds_read_b64_tr_b16 v[88:89], v98 offset:6272\n"
      "ds_read_b64_tr_b16 v[90:91], v98 offset:7040\n"
      "s_xor_b32 s53, s53, 1\n"
      "v_mfma_f32_16x16x32_bf16 v[28:31], v[92:95], a[40:43], v[28:31]\n"
      "ds_read_b64_tr_b16 v[92:93], v99 offset:6272\n"
      "ds_read_b64_tr_b16 v[94:95], v99 offset:7040\n"
      "s_waitcnt lgkmcnt(12)\n"
      "s_lshl1_add_u32 s53, s31, s53\n"
      "v_mfma_f32_16x16x32_bf16 v[32:35], v[64:67], a[40:43], v[32:35]\n"
      "ds_read_b64_tr_b16 v[64:65], v98 offset:12288\n"
      "ds_read_b64_tr_b16 v[66:67], v98 offset:13056\n"
      "s_mul_i32 s53, s53, 15\n"
      "v_mfma_f32_16x16x32_bf16 v[36:39], v[68:71], a[40:43], v[36:39]\n"
      "ds_read_b64_tr_b16 v[68:69], v99 offset:12288\n"
      "ds_read_b64_tr_b16 v[70:71], v99 offset:13056\n"
      "s_waitcnt lgkmcnt(12)\n"
      "s_add_u32 s53, s52, s53\n"
      "s_waitcnt vmcnt(9)\n"
      "v_mfma_f32_16x16x32_bf16 v[16:19], v[72:75], a[44:47], v[16:19]\n"
      "ds_read_b64_tr_b16 v[72:73], v98 offset:12352\n"
      "ds_read_b64_tr_b16 v[74:75], v98 offset:13120\n"
      "s_lshl_b32 s53, s53, 10\n"
      "s_add_u32 s53, s53, 8388608\n"
      "v_mfma_f32_16x16x32_bf16 v[20:23], v[76:79], a[44:47], v[20:23]\n"
      "ds_read_b64_tr_b16 v[76:77], v99 offset:12352\n"
      "ds_read_b64_tr_b16 v[78:79], v99 offset:13120\n"
      "s_waitcnt lgkmcnt(12)\n"
      "v_add_u32 v243, s53, v2\n"
      "v_mfma_f32_16x16x32_bf16 v[24:27], v[80:83], a[44:47], v[24:27]\n"
      "ds_read_b64_tr_b16 v[80:81], v98 offset:12416\n"
      "ds_read_b64_tr_b16 v[82:83], v98 offset:13184\n"
      "s_bfe_u32 s53, s48, 0x10001\n"
      "v_mfma_f32_16x16x32_bf16 v[28:31], v[84:87], a[44:47], v[28:31]\n"
      "ds_read_b64_tr_b16 v[84:85], v99 offset:12416\n"
      "ds_read_b64_tr_b16 v[86:87], v99 offset:13184\n"
      "s_waitcnt lgkmcnt(12)\n"
      "s_xor_b32 s53, s53, 1\n"
      "v_mfma_f32_16x16x32_bf16 v[32:35], v[88:91], a[44:47], v[32:35]\n"
      "ds_read_b64_tr_b16 v[88:89], v98 offset:18432\n"
      "ds_read_b64_tr_b16 v[90:91], v98 offset:19200\n"
      "s_lshl1_add_u32 s53, s31, s53\n"
      "v_mfma_f32_16x16x32_bf16 v[36:39], v[92:95], a[44:47], v[36:39]\n"
      "ds_read_b64_tr_b16 v[92:93], v99 offset:18432\n"
      "ds_read_b64_tr_b16 v[94:95], v99 offset:19200\n"
      "s_waitcnt lgkmcnt(12)\n"
      "s_add_u32 s53, s53, 8\n"
      "s_waitcnt vmcnt(8)\n"
      "v_mfma_f32_16x16x32_bf16 v[16:19], v[64:67], a[48:51], v[16:19]\n"
      "ds_read_b64_tr_b16 v[64:65], v98 offset:18496\n"
      "ds_read_b64_tr_b16 v[66:67], v98 offset:19264\n"
      "s_mul_i32 s53, s53, 15\n"
      "v_mfma_f32_16x16x32_bf16 v[20:23], v[68:71], a[48:51], v[20:23]\n"
      "ds_read_b64_tr_b16 v[68:69], v99 offset:18496\n"
      "ds_read_b64_tr_b16 v[70:71], v99 offset:19264\n"
      "s_waitcnt lgkmcnt(12)\n"
      "s_add_u32 s53, s52, s53\n"
      "v_mfma_f32_16x16x32_bf16 v[24:27], v[72:75], a[48:51], v[24:27]\n"
      "ds_read_b64_tr_b16 v[72:73], v98 offset:18560\n"
      "ds_read_b64_tr_b16 v[74:75], v98 offset:19328\n"
      "s_lshl_b32 s53, s53, 10\n"
      "v_mfma_f32_16x16x32_bf16 v[28:31], v[76:79], a[48:51], v[28:31]\n"
      "ds_read_b64_tr_b16 v[76:77], v99 offset:18560\n"
      "ds_read_b64_tr_b16 v[78:79], v99 offset:19328\n"
      "s_waitcnt lgkmcnt(12)\n"
      "s_add_u32 s53, s53, 8388608\n"
      "v_mfma_f32_16x16x32_bf16 v[32:35], v[80:83], a[48:51], v[32:35]\n"
      "ds_read_b64_tr_b16 v[80:81], v98 offset:24576\n"
      "ds_read_b64_tr_b16 v[82:83], v98 offset:25344\n"
      "v_add_u32 v244, s53, v2\n"
      "v_mfma_f32_16x16x32_bf16 v[36:39], v[84:87], a[48:51], v[36:39]\n"
      "ds_read_b64_tr_b16 v[84:85], v99 offset:24576\n"
      "ds_read_b64_tr_b16 v[86:87], v99 offset:25344\n"
      "s_waitcnt lgkmcnt(12)\n"
      "s_waitcnt vmcnt(7)\n"
      "v_mfma_f32_16x16x32_bf16 v[16:19], v[88:91], a[52:55], v[16:19]\n"
      "ds_read_b64_tr_b16 v[88:89], v98 offset:24640\n"
      "ds_read_b64_tr_b16 v[90:91], v98 offset:25408\n"
      "v_mfma_f32_16x16x32_bf16 v[20:23], v[92:95], a[52:55], v[20:23]\n"
      "ds_read_b64_tr_b16 v[92:93], v99 offset:24640\n"
      "ds_read_b64_tr_b16 v[94:95], v99 offset:25408\n"
      "s_waitcnt lgkmcnt(12)\n"
      "v_mfma_f32_16x16x32_bf16 v[24:27], v[64:67], a[52:55], v[24:27]\n"
      "ds_read_b64_tr_b16 v[64:65], v98 offset:24704\n"
      "ds_read_b64_tr_b16 v[66:67], v98 offset:25472\n"
      "s_waitcnt vmcnt(0)\n"
      "v_cmp_ne_u32_e64 vcc, s56, v104\n"
      "s_and_b64 vcc, exec, vcc\n"
      "s_cbranch_vccnz K2_spin_4\n"
      "K2_cont_5:\n"
      "buffer_load_dwordx4 v[108:111], v125, s[32:35], 0 offen sc1\n"
      "buffer_load_dwordx4 v[112:115], v125, s[32:35], s65 offen sc1\n"
      "buffer_load_dwordx4 v[116:119], v125, s[32:35], s66 offen sc1\n"
      "buffer_load_dwordx4 v[120:123], v125, s[32:35], s67 offen sc1\n"
      "ds_read2_b32 v[128:129], v224 offset0:240 offset1:241\n"
      "ds_read2_b32 v[130:131], v224 offset0:242 offset1:243\n"
      "v_mfma_f32_16x16x32_bf16 v[28:31], v[68:71], a[52:55], v[28:31]\n"
      "ds_read_b64_tr_b16 v[68:69], v99 offset:24704\n"
      "ds_read_b64_tr_b16 v[70:71], v99 offset:25472\n"
      "s_waitcnt lgkmcnt(14)\n"
      "v_mfma_f32_16x16x32_bf16 v[32:35], v[72:75], a[52:55], v[32:35]\n"
      "ds_read_b64_tr_b16 v[72:73], v98 offset:30720\n"
      "ds_read_b64_tr_b16 v[74:75], v98 offset:31488\n"
      "v_mfma_f32_16x16x32_bf16 v[36:39], v[76:79], a[52:55], v[36:39]\n"
      "ds_read_b64_tr_b16 v[76:77], v99 offset:30720\n"
      "ds_read_b64_tr_b16 v[78:79], v99 offset:31488\n"
      "s_waitcnt lgkmcnt(14)\n"
      "v_mfma_f32_16x16x32_bf16 v[16:19], v[80:83], a[56:59], v[16:19]\n"
      "ds_read_b64_tr_b16 v[80:81], v98 offset:30784\n"
      "ds_read_b64_tr_b16 v[82:83], v98 offset:31552\n"
      "ds_read2_b32 v[132:133], v224 offset0:112 offset1:113\n"
      "ds_read2_b32 v[134:135], v224 offset0:114 offset1:115\n"
      "v_mfma_f32_16x16x32_bf16 v[20:23], v[84:87], a[56:59], v[20:23]\n"
      "ds_read_b64_tr_b16 v[84:85], v99 offset:30784\n"
      "ds_read_b64_tr_b16 v[86:87], v99 offset:31552\n"
      "s_waitcnt lgkmcnt(15)\n"
      "v_mfma_f32_16x16x32_bf16 v[24:27], v[88:91], a[56:59], v[24:27]\n"
      "ds_read_b64_tr_b16 v[88:89], v98 offset:30848\n"
      "ds_read_b64_tr_b16 v[90:91], v98 offset:31616\n"
      "v_mfma_f32_16x16x32_bf16 v[28:31], v[92:95], a[56:59], v[28:31]\n"
      "ds_read_b64_tr_b16 v[92:93], v99 offset:30848\n"
      "ds_read_b64_tr_b16 v[94:95], v99 offset:31616\n"
      "s_waitcnt lgkmcnt(14)\n"
      "v_mfma_f32_16x16x32_bf16 v[32:35], v[64:67], a[56:59], v[32:35]\n"
      "ds_read_b64_tr_b16 v[64:65], v98 offset:36864\n"
      "ds_read_b64_tr_b16 v[66:67], v98 offset:37632\n"
      "ds_read2_b32 v[136:137], v225 offset0:240 offset1:241\n"
      "ds_read2_b32 v[138:139], v225 offset0:242 offset1:243\n"
      "v_mfma_f32_16x16x32_bf16 v[36:39], v[68:71], a[56:59], v[36:39]\n"
      "ds_read_b64_tr_b16 v[68:69], v99 offset:36864\n"
      "ds_read_b64_tr_b16 v[70:71], v99 offset:37632\n"
      "s_waitcnt lgkmcnt(15)\n"
      "v_mfma_f32_16x16x32_bf16 v[16:19], v[72:75], a[60:63], v[16:19]\n"
      "ds_read_b64_tr_b16 v[72:73], v98 offset:36928\n"
      "ds_read_b64_tr_b16 v[74:75], v98 offset:37696\n"
      "v_mfma_f32_16x16x32_bf16 v[20:23], v[76:79], a[60:63], v[20:23]\n"
      "ds_read_b64_tr_b16 v[76:77], v99 offset:36928\n"
      "ds_read_b64_tr_b16 v[78:79], v99 offset:37696\n"
      "s_waitcnt lgkmcnt(14)\n"
      "v_mfma_f32_16x16x32_bf16 v[24:27], v[80:83], a[60:63], v[24:27]\n"
      "ds_read_b64_tr_b16 v[80:81], v98 offset:36992\n"
      "ds_read_b64_tr_b16 v[82:83], v98 offset:37760\n"
      "ds_read2_b32 v[140:141], v225 offset0:112 offset1:113\n"
      "ds_read2_b32 v[142:143], v225 offset0:114 offset1:115\n"
      "v_mfma_f32_16x16x32_bf16 v[28:31], v[84:87], a[60:63], v[28:31]\n"
      "ds_read_b64_tr_b16 v[84:85], v99 offset:36992\n"
      "ds_read_b64_tr_b16 v[86:87], v99 offset:37760\n"
      "s_waitcnt lgkmcnt(15)\n"
      "v_mfma_f32_16x16x32_bf16 v[32:35], v[88:91], a[60:63], v[32:35]\n"
      "ds_read_b64_tr_b16 v[88:89], v98 offset:43008\n"
      "ds_read_b64_tr_b16 v[90:91], v98 offset:43776\n"
      "v_mfma_f32_16x16x32_bf16 v[36:39], v[92:95], a[60:63], v[36:39]\n"
      "ds_read_b64_tr_b16 v[92:93], v99 offset:43008\n"
      "ds_read_b64_tr_b16 v[94:95], v99 offset:43776\n"
      "s_waitcnt lgkmcnt(14)\n"
      "v_mfma_f32_16x16x32_bf16 v[16:19], v[64:67], a[64:67], v[16:19]\n"
      "ds_read_b64_tr_b16 v[64:65], v98 offset:43072\n"
      "ds_read_b64_tr_b16 v[66:67], v98 offset:43840\n"
      "ds_read2_b32 v[144:145], v226 offset0:240 offset1:241\n"
      "ds_read2_b32 v[146:147], v226 offset0:242 offset1:243\n"
      "v_mfma_f32_16x16x32_bf16 v[20:23], v[68:71], a[64:67], v[20:23]\n"
      "ds_read_b64_tr_b16 v[68:69], v99 offset:43072\n"
      "ds_read_b64_tr_b16 v[70:71], v99 offset:43840\n"
      "s_waitcnt lgkmcnt(15)\n"
      "v_mfma_f32_16x16x32_bf16 v[24:27], v[72:75], a[64:67], v[24:27]\n"
      "ds_read_b64_tr_b16 v[72:73], v98 offset:43136\n"
      "ds_read_b64_tr_b16 v[74:75], v98 offset:43904\n"
      "v_mfma_f32_16x16x32_bf16 v[28:31], v[76:79], a[64:67], v[28:31]\n"
      "ds_read_b64_tr_b16 v[76:77], v99 offset:43136\n"
      "ds_read_b64_tr_b16 v[78:79], v99 offset:43904\n"
      "s_waitcnt lgkmcnt(14)\n"
      "v_mfma_f32_16x16x32_bf16 v[32:35], v[80:83], a[64:67], v[32:35]\n"
      "ds_read_b64_tr_b16 v[80:81], v98 offset:49152\n"
      "ds_read_b64_tr_b16 v[82:83], v98 offset:49920\n"
      "s_mov_b32 exec_hi, 0\n"
      "buffer_load_dword v254, v11, s[32:35], s92 offen sc1\n"
      "s_mov_b32 exec_hi, -1\n"
      "ds_read2_b32 v[148:149], v226 offset0:112 offset1:113\n"
      "ds_read2_b32 v[150:151], v226 offset0:114 offset1:115\n"
      "v_mfma_f32_16x16x32_bf16 v[36:39], v[84:87], a[64:67], v[36:39]\n"
      "ds_read_b64_tr_b16 v[84:85], v99 offset:49152\n"
      "ds_read_b64_tr_b16 v[86:87], v99 offset:49920\n"
      "s_waitcnt lgkmcnt(15)\n"
      "v_mfma_f32_16x16x32_bf16 v[16:19], v[88:91], a[68:71], v[16:19]\n"
      "ds_read_b64_tr_b16 v[88:89], v98 offset:49216\n"
      "ds_read_b64_tr_b16 v[90:91], v98 offset:49984\n"
      "v_mfma_f32_16x16x32_bf16 v[20:23], v[92:95], a[68:71], v[20:23]\n"
      "ds_read_b64_tr_b16 v[92:93], v99 offset:49216\n"
      "ds_read_b64_tr_b16 v[94:95], v99 offset:49984\n"
      "s_waitcnt lgkmcnt(14)\n"
      "v_mfma_f32_16x16x32_bf16 v[24:27], v[64:67], a[68:71], v[24:27]\n"
      "ds_read_b64_tr_b16 v[64:65], v98 offset:49280\n"
      "ds_read_b64_tr_b16 v[66:67], v98 offset:50048\n"
      "ds_read2_b32 v[152:153], v227 offset0:240 offset1:241\n"
      "ds_read2_b32 v[154:155], v227 offset0:242 offset1:243\n"
      "v_mfma_f32_16x16x32_bf16 v[28:31], v[68:71], a[68:71], v[28:31]\n"
      "ds_read_b64_tr_b16 v[68:69], v99 offset:49280\n"
      "ds_read_b64_tr_b16 v[70:71], v99 offset:50048\n"
      "s_waitcnt lgkmcnt(15)\n"
      "v_mfma_f32_16x16x32_bf16 v[32:35], v[72:75], a[68:71], v[32:35]\n"
      "ds_read_b64_tr_b16 v[72:73], v98 offset:55296\n"
      "ds_read_b64_tr_b16 v[74:75], v98 offset:56064\n"
      "v_mfma_f32_16x16x32_bf16 v[36:39], v[76:79], a[68:71], v[36:39]\n"
      "ds_read_b64_tr_b16 v[76:77], v99 offset:55296\n"
      "ds_read_b64_tr_b16 v[78:79], v99 offset:56064\n"
      "s_waitcnt lgkmcnt(14)\n"
      "v_mfma_f32_16x16x32_bf16 v[16:19], v[80:83], a[72:75], v[16:19]\n"
      "ds_read_b64_tr_b16 v[80:81], v98 offset:55360\n"
      "ds_read_b64_tr_b16 v[82:83], v98 offset:56128\n"
      "ds_read2_b32 v[156:157], v227 offset0:112 offset1:113\n"
      "ds_read2_b32 v[158:159], v227 offset0:114 offset1:115\n"
      "v_mfma_f32_16x16x32_bf16 v[20:23], v[84:87], a[72:75], v[20:23]\n"
      "ds_read_b64_tr_b16 v[84:85], v99 offset:55360\n"
      "ds_read_b64_tr_b16 v[86:87], v99 offset:56128\n"
      "s_waitcnt lgkmcnt(15)\n"
      "v_mfma_f32_16x16x32_bf16 v[24:27], v[88:91], a[72:75], v[24:27]\n"
      "ds_read_b64_tr_b16 v[88:89], v98 offset:55424\n"
      "ds_read_b64_tr_b16 v[90:91], v98 offset:56192\n"
      "v_mfma_f32_16x16x32_bf16 v[28:31], v[92:95], a[72:75], v[28:31]\n"
      "ds_read_b64_tr_b16 v[92:93], v99 offset:55424\n"
      "ds_read_b64_tr_b16 v[94:95], v99 offset:56192\n"
      "s_waitcnt lgkmcnt(14)\n"
      "v_mfma_f32_16x16x32_bf16 v[32:35], v[64:67], a[72:75], v[32:35]\n"
      "v_mfma_f32_16x16x32_bf16 v[36:39], v[68:71], a[72:75], v[36:39]\n"
      "s_waitcnt lgkmcnt(10)\n"
      "v_mfma_f32_16x16x32_bf16 v[16:19], v[72:75], a[76:79], v[16:19]\n"
      "v_mfma_f32_16x16x32_bf16 v[20:23], v[76:79], a[76:79], v[20:23]\n"
      "s_waitcnt lgkmcnt(4)\n"
      "v_mfma_f32_16x16x32_bf16 v[24:27], v[80:83], a[76:79], v[24:27]\n"
      "v_mfma_f32_16x16x32_bf16 v[28:31], v[84:87], a[76:79], v[28:31]\n"
      "s_waitcnt lgkmcnt(0)\n"
      "v_mfma_f32_16x16x32_bf16 v[32:35], v[88:91], a[76:79], v[32:35]\n"
      "v_mfma_f32_16x16x32_bf16 v[36:39], v[92:95], a[76:79], v[36:39]\n"
      "s_nop 0\n"
      "v_mul_f32 v40, v16, |v16|\n"
      "v_mul_f32 v41, v17, |v17|\n"
      "v_mul_f32 v42, v18, |v18|\n"
      "v_mul_f32 v43, v19, |v19|\n"
      "v_mul_f32 v44, v20, |v20|\n"
      "v_mul_f32 v45, v21, |v21|\n"
      "v_mul_f32 v46, v22, |v22|\n"
      "v_mul_f32 v47, v23, |v23|\n"
      "v_cvt_pk_bf16_f32 v48, v40, v41\n"
      "v_cvt_pk_bf16_f32 v49, v42, v43\n"
      "v_cvt_pk_bf16_f32 v50, v44, v45\n"
      "v_cvt_pk_bf16_f32 v51, v46, v47\n"
      "v_pk_max_i16 v48, v48, 0\n"
      "v_pk_max_i16 v49, v49, 0\n"
      "v_pk_max_i16 v50, v50, 0\n"
      "v_pk_max_i16 v51, v51, 0\n"
      "buffer_store_dwordx4 v[48:51], v2, s[44:47], s83 offen offset:0 sc1\n"
      "v_mul_f32 v52, v24, |v24|\n"
      "v_mul_f32 v53, v25, |v25|\n"
      "v_mul_f32 v54, v26, |v26|\n"
      "v_mul_f32 v55, v27, |v27|\n"
      "v_mul_f32 v56, v28, |v28|\n"
      "v_mul_f32 v57, v29, |v29|\n"
      "v_mul_f32 v58, v30, |v30|\n"
      "v_mul_f32 v59, v31, |v31|\n"
      "v_cvt_pk_bf16_f32 v60, v52, v53\n"
      "v_cvt_pk_bf16_f32 v61, v54, v55\n"
      "v_cvt_pk_bf16_f32 v62, v56, v57\n"
      "v_cvt_pk_bf16_f32 v63, v58, v59\n"
      "v_pk_max_i16 v60, v60, 0\n"
      "v_pk_max_i16 v61, v61, 0\n"
      "v_pk_max_i16 v62, v62, 0\n"
      "v_pk_max_i16 v63, v63, 0\n"
      "buffer_store_dwordx4 v[60:63], v2, s[44:47], s83 offen offset:1024 sc1\n"
      "v_mul_f32 v40, v32, |v32|\n"
      "v_mul_f32 v41, v33, |v33|\n"
      "v_mul_f32 v42, v34, |v34|\n"
      "v_mul_f32 v43, v35, |v35|\n"
      "v_mul_f32 v44, v36, |v36|\n"
      "v_mul_f32 v45, v37, |v37|\n"
      "v_mul_f32 v46, v38, |v38|\n"
      "v_mul_f32 v47, v39, |v39|\n"
      "v_cvt_pk_bf16_f32 v48, v40, v41\n"
      "v_cvt_pk_bf16_f32 v49, v42, v43\n"
      "v_cvt_pk_bf16_f32 v50, v44, v45\n"
      "v_cvt_pk_bf16_f32 v51, v46, v47\n"
      "v_pk_max_i16 v48, v48, 0\n"
      "v_pk_max_i16 v49, v49, 0\n"
      "v_pk_max_i16 v50, v50, 0\n"
      "v_pk_max_i16 v51, v51, 0\n"
      "buffer_store_dwordx4 v[48:51], v2, s[44:47], s83 offen offset:2048 sc1\n"
      "s_waitcnt vmcnt(7)\n"
      "ds_write_b128 v124, v[108:111] offset:0\n"
      "s_waitcnt vmcnt(6)\n"
      "ds_write_b128 v124, v[112:115] offset:1024\n"
      "s_waitcnt vmcnt(5)\n"
      "ds_write_b128 v124, v[116:119] offset:2048\n"
      "s_waitcnt vmcnt(4)\n"
      "ds_write_b128 v124, v[120:123] offset:3072\n"
      "s_mul_i32 s52, s30, 52429\n"
      "s_lshr_b32 s52, s52, 20\n"
      "s_mul_i32 s53, s52, 20\n"
      "s_sub_u32 s53, s30, s53\n"
      "s_mul_i32 s52, s52, 320\n"
      "s_lshr_b32 s54, s53, 2\n"
      "s_add_u32 s52, s52, s54\n"
      "s_lshl_b32 s52, s52, 10\n"
      "s_and_b32 s53, s53, 3\n"
      "s_lshl_b32 s53, s53, 8\n"
      "s_add_u32 s52, s52, s53\n"
      "s_mul_i32 s53, s31, 81920\n"
      "s_add_u32 s52, s52, s53\n"
      "s_add_u32 s86, s52, 20971520\n"
      "s_mul_i32 s52, s31, 160\n"
      "s_add_u32 s52, s52, s30\n"
      "s_lshl_b32 s52, s52, 7\n"
      "s_add_u32 s88, s52, 131072\n"
      "s_waitcnt lgkmcnt(0)\n"
      "s_barrier\n"
      "ds_read_b128 v[160:163], v228 offset:448\n"
      "ds_read_b128 v[164:167], v228 offset:960\n"
      "ds_read_b128 v[168:171], v228 offset:1472\n"
      "ds_read_b128 v[172:175], v228 offset:1984\n"
      "ds_read_b128 v[176:179], v228 offset:2496\n"
      "ds_read_b128 v[180:183], v228 offset:3008\n"
      "ds_read_b128 v[184:187], v228 offset:3520\n"
      "ds_read_b128 v[188:191], v228 offset:4032\n"
      "s_waitcnt lgkmcnt(7)\n"
      "v_mfma_f32_16x16x32_f16 v[192:195], v[128:131], v[160:163], 0\n"
      "ds_read2_b32 v[64:65], v224 offset0:224 offset1:225\n"
      "v_mfma_f32_16x16x32_f16 v[196:199], v[132:135], v[160:163], 0\n"
      "ds_read2_b32 v[66:67], v224 offset0:226 offset1:227\n"
      "v_mfma_f32_16x16x32_f16 v[200:203], v[136:139], v[160:163], 0\n"
      "ds_read_b128 v[96:99], v228 offset:384\n"
      "v_mfma_f32_16x16x32_f16 v[204:207], v[140:143], v[160:163], 0\n"
      "v_mfma_f32_16x16x32_f16 v[208:211], v[144:147], v[160:163], 0\n"
      "ds_read2_b32 v[68:69], v224 offset0:96 offset1:97\n"
      "s_waitcnt vmcnt(3)\n"
      "s_mov_b32 exec_hi, 0\n"
      "v_cmp_ne_u32 vcc, s56, v254\n"
      "s_mov_b32 exec_hi, -1\n"
      "s_cbranch_vccnz K2_spin_6\n"
      "K2_cont_7:\n"
      "v_mfma_f32_16x16x32_f16 v[212:215], v[148:151], v[160:163], 0\n"
      "ds_read2_b32 v[70:71], v224 offset0:98 offset1:99\n"
      "v_mfma_f32_16x16x32_f16 v[216:219], v[152:155], v[160:163], 0\n"
      "buffer_load_dwordx4 a[160:163], v12, s[32:35], 0 offen offset:0 sc1\n"
      "v_mfma_f32_16x16x32_f16 v[220:223], v[156:159], v[160:163], 0\n"
      "ds_read_b128 v[100:103], v228 offset:896\n"
      "s_waitcnt lgkmcnt(12)\n"
      "v_mfma_f32_16x16x32_f16 v[196:199], v[128:131], v[164:167], v[196:199]\n"
      "ds_read2_b32 v[72:73], v225 offset0:224 offset1:225\n"
      "v_mfma_f32_16x16x32_f16 v[200:203], v[132:135], v[164:167], v[200:203]\n"
      "ds_read2_b32 v[74:75], v225 offset0:226 offset1:227\n"
      "v_mfma_f32_16x16x32_f16 v[204:207], v[136:139], v[164:167], v[204:207]\n"
      "v_mfma_f32_16x16x32_f16 v[208:211], v[140:143], v[164:167], v[208:211]\n"
      "ds_read_b128 v[104:107], v228 offset:1408\n"
      "v_mfma_f32_16x16x32_f16 v[212:215], v[144:147], v[164:167], v[212:215]\n"
      "ds_read2_b32 v[76:77], v225 offset0:96 offset1:97\n"
      "buffer_load_dwordx4 a[164:167], v12, s[32:35], 0 offen offset:1024 sc1\n"
      "v_mfma_f32_16x16x32_f16 v[216:219], v[148:151], v[164:167], v[216:219]\n"
      "v_mfma_f32_16x16x32_f16 v[220:223], v[152:155], v[164:167], v[220:223]\n"
      "ds_read2_b32 v[78:79], v225 offset0:98 offset1:99\n"
      "s_waitcnt lgkmcnt(15)\n"
      "v_mfma_f32_16x16x32_f16 v[200:203], v[128:131], v[168:171], v[200:203]\n"
      "ds_read_b128 v[108:111], v228 offset:1920\n"
      "v_mfma_f32_16x16x32_f16 v[204:207], v[132:135], v[168:171], v[204:207]\n"
      "v_mfma_f32_16x16x32_f16 v[208:211], v[136:139], v[168:171], v[208:211]\n"
      "ds_read2_b32 v[80:81], v226 offset0:224 offset1:225\n"
      "v_mfma_f32_16x16x32_f16 v[212:215], v[140:143], v[168:171], v[212:215]\n"
      "ds_read2_b32 v[82:83], v226 offset0:226 offset1:227\n"
      "buffer_load_dwordx4 a[168:171], v12, s[32:35], 0 offen offset:2048 sc1\n"
      "v_mfma_f32_16x16x32_f16 v[216:219], v[144:147], v[168:171], v[216:219]\n"
      "ds_read_b128 v[112:115], v228 offset:2432\n"
      "v_mfma_f32_16x16x32_f16 v[220:223], v[148:151], v[168:171], v[220:223]\n"
      "v_mfma_f32_16x16x32_f16 v[204:207], v[128:131], v[172:175], v[204:207]\n"
      "ds_read2_b32 v[84:85], v226 offset0:96 offset1:97\n"
      "v_mfma_f32_16x16x32_f16 v[208:211], v[132:135], v[172:175], v[208:211]\n"
      "ds_read2_b32 v[86:87], v226 offset0:98 offset1:99\n"
      "v_mfma_f32_16x16x32_f16 v[212:215], v[136:139], v[172:175], v[212:215]\n"
      "v_mfma_f32_16x16x32_f16 v[216:219], v[140:143], v[172:175], v[216:219]\n"
      "ds_read_b128 v[116:119], v228 offset:2944\n"
      "buffer_load_dwordx4 a[172:175], v12, s[32:35], 0 offen offset:3072 sc1\n"
      "v_mfma_f32_16x16x32_f16 v[220:223], v[144:147], v[172:175], v[220:223]\n"
      "ds_read2_b32 v[88:89], v227 offset0:224 offset1:225\n"
      "s_waitcnt lgkmcnt(15)\n"
      "v_mfma_f32_16x16x32_f16 v[208:211], v[128:131], v[176:179], v[208:211]\n"
      "ds_read2_b32 v[90:91], v227 offset0:226 offset1:227\n"
      "v_mfma_f32_16x16x32_f16 v[212:215], v[132:135], v[176:179], v[212:215]\n"
      "v_mfma_f32_16x16x32_f16 v[216:219], v[136:139], v[176:179], v[216:219]\n"
      "ds_read_b128 v[120:123], v228 offset:3456\n"
      "v_mfma_f32_16x16x32_f16 v[220:223], v[140:143], v[176:179], v[220:223]\n"
      "ds_read2_b32 v[92:93], v227 offset0:96 offset1:97\n"
      "v_mfma_f32_16x16x32_f16 v[212:215], v[128:131], v[180:183], v[212:215]\n"
      "buffer_load_dwordx4 a[176:179], v12, s[32:35], s65 offen offset:0 sc1\n"
      "v_mfma_f32_16x16x32_f16 v[216:219], v[132:135], v[180:183], v[216:219]\n"
      "ds_read2_b32 v[94:95], v227 offset0:98 offset1:99\n"
      "v_mfma_f32_16x16x32_f16 v[220:223], v[136:139], v[180:183], v[220:223]\n"
      "ds_read_b128 v[124:127], v228 offset:3968\n"
      "v_mfma_f32_16x16x32_f16 v[216:219], v[128:131], v[184:187], v[216:219]\n"
      "v_mfma_f32_16x16x32_f16 v[220:223], v[132:135], v[184:187], v[220:223]\n"
      "v_mfma_f32_16x16x32_f16 v[220:223], v[128:131], v[188:191], v[220:223]\n"
      "v_mfma_f32_16x16x32_f16 v[192:195], v[64:67], v[96:99], v[192:195]\n"
      "ds_read2_b32 v[128:129], v224 offset0:208 offset1:209\n"
      "buffer_load_dwordx4 a[180:183], v12, s[32:35], s65 offen offset:1024 sc1\n"
      "s_waitcnt lgkmcnt(15)\n"
      "v_mfma_f32_16x16x32_f16 v[196:199], v[68:71], v[96:99], v[196:199]\n"
      "ds_read2_b32 v[130:131], v224 offset0:210 offset1:211\n"
      "v_mfma_f32_16x16x32_f16 v[200:203], v[68:71], v[100:103], v[200:203]\n"
      "ds_read_b128 v[160:163], v228 offset:320\n"
      "v_mfma_f32_16x16x32_f16 v[196:199], v[64:67], v[100:103], v[196:199]\n"
      "v_mfma_f32_16x16x32_f16 v[200:203], v[72:75], v[96:99], v[200:203]\n"
      "ds_read2_b32 v[132:133], v224 offset0:80 offset1:81\n"
      "v_mfma_f32_16x16x32_f16 v[204:207], v[72:75], v[100:103], v[204:207]\n"
      "ds_read2_b32 v[134:135], v224 offset0:82 offset1:83\n"
      "v_mfma_f32_16x16x32_f16 v[208:211], v[72:75], v[104:107], v[208:211]\n"
      "buffer_load_dwordx4 a[184:187], v12, s[32:35], s65 offen offset:2048 sc1\n"
      "v_mfma_f32_16x16x32_f16 v[200:203], v[64:67], v[104:107], v[200:203]\n"
      "ds_read_b128 v[164:167], v228 offset:832\n"
      "v_mfma_f32_16x16x32_f16 v[204:207], v[68:71], v[104:107], v[204:207]\n"
      "ds_read2_b32 v[136:137], v225 offset0:208 offset1:209\n"
      "s_waitcnt lgkmcnt(15)\n"
      "v_mfma_f32_16x16x32_f16 v[204:207], v[76:79], v[96:99], v[204:207]\n"
      "ds_read2_b32 v[138:139], v225 offset0:210 offset1:211\n"
      "v_mfma_f32_16x16x32_f16 v[208:211], v[76:79], v[100:103], v[208:211]\n"
      "v_mfma_f32_16x16x32_f16 v[212:215], v[76:79], v[104:107], v[212:215]\n"
      "ds_read_b128 v[168:171], v228 offset:1344\n"
      "v_mfma_f32_16x16x32_f16 v[216:219], v[76:79], v[108:111], v[216:219]\n"
      "ds_read2_b32 v[140:141], v225 offset0:80 offset1:81\n"
      "buffer_load_dwordx4 a[188:191], v12, s[32:35], s65 offen offset:3072 sc1\n"
      "v_mfma_f32_16x16x32_f16 v[204:207], v[64:67], v[108:111], v[204:207]\n"
      "v_mfma_f32_16x16x32_f16 v[208:211], v[68:71], v[108:111], v[208:211]\n"
      "ds_read2_b32 v[142:143], v225 offset0:82 offset1:83\n"
      "v_mfma_f32_16x16x32_f16 v[212:215], v[72:75], v[108:111], v[212:215]\n"
      "ds_read_b128 v[172:175], v228 offset:1856\n"
      "v_mfma_f32_16x16x32_f16 v[208:211], v[80:83], v[96:99], v[208:211]\n"
      "v_mfma_f32_16x16x32_f16 v[212:215], v[80:83], v[100:103], v[212:215]\n"
      "ds_read2_b32 v[144:145], v226 offset0:208 offset1:209\n"
      "v_mfma_f32_16x16x32_f16 v[216:219], v[80:83], v[104:107], v[216:219]\n"
      "ds_read2_b32 v[146:147], v226 offset0:210 offset1:211\n"
      "buffer_load_dwordx4 a[192:195], v12, s[32:35], s66 offen offset:0 sc1\n"
      "v_mfma_f32_16x16x32_f16 v[220:223], v[80:83], v[108:111], v[220:223]\n"
      "ds_read_b128 v[176:179], v228 offset:2368\n"
      "v_mfma_f32_16x16x32_f16 v[208:211], v[64:67], v[112:115], v[208:211]\n"
      "v_mfma_f32_16x16x32_f16 v[212:215], v[68:71], v[112:115], v[212:215]\n"
      "ds_read2_b32 v[148:149], v226 offset0:80 offset1:81\n"
      "v_mfma_f32_16x16x32_f16 v[216:219], v[72:75], v[112:115], v[216:219]\n"
      "ds_read2_b32 v[150:151], v226 offset0:82 offset1:83\n"
      "v_mfma_f32_16x16x32_f16 v[220:223], v[76:79], v[112:115], v[220:223]\n"
      "s_waitcnt lgkmcnt(15)\n"
      "v_mfma_f32_16x16x32_f16 v[212:215], v[84:87], v[96:99], v[212:215]\n"
      "ds_read_b128 v[180:183], v228 offset:2880\n"
      "buffer_load_dwordx4 a[196:199], v12, s[32:35], s66 offen offset:1024 sc1\n"
      "v_mfma_f32_16x16x32_f16 v[216:219], v[84:87], v[100:103], v[216:219]\n"
      "ds_read2_b32 v[152:153], v227 offset0:208 offset1:209\n"
      "v_mfma_f32_16x16x32_f16 v[220:223], v[84:87], v[104:107], v[220:223]\n"
      "ds_read2_b32 v[154:155], v227 offset0:210 offset1:211\n"
      "v_mfma_f32_16x16x32_f16 v[212:215], v[64:67], v[116:119], v[212:215]\n"
      "v_mfma_f32_16x16x32_f16 v[216:219], v[68:71], v[116:119], v[216:219]\n"
      "ds_read_b128 v[184:187], v228 offset:3392\n"
      "v_mfma_f32_16x16x32_f16 v[220:223], v[72:75], v[116:119], v[220:223]\n"
      "ds_read2_b32 v[156:157], v227 offset0:80 offset1:81\n"
      "v_mfma_f32_16x16x32_f16 v[216:219], v[88:91], v[96:99], v[216:219]\n"
      "buffer_load_dwordx4 a[200:203], v12, s[32:35], s66 offen offset:2048 sc1\n"
      "v_mfma_f32_16x16x32_f16 v[220:223], v[88:91], v[100:103], v[220:223]\n"
      "ds_read2_b32 v[158:159], v227 offset0:82 offset1:83\n"
      "v_mfma_f32_16x16x32_f16 v[216:219], v[64:67], v[120:123], v[216:219]\n"
      "ds_read_b128 v[188:191], v228 offset:3904\n"
      "v_mfma_f32_16x16x32_f16 v[220:223], v[68:71], v[120:123], v[220:223]\n"
      "v_mfma_f32_16x16x32_f16 v[220:223], v[92:95], v[96:99], v[220:223]\n"
      "v_mfma_f32_16x16x32_f16 v[220:223], v[64:67], v[124:127], v[220:223]\n"
      "s_waitcnt lgkmcnt(15)\n"
      "v_mfma_f32_16x16x32_f16 v[192:195], v[128:131], v[160:163], v[192:195]\n"
      "ds_read2_b32 v[64:65], v224 offset0:192 offset1:193\n"
      "buffer_load_dwordx4 a[204:207], v12, s[32:35], s66 offen offset:3072 sc1\n"
      "v_mfma_f32_16x16x32_f16 v[196:199], v[132:135], v[160:163], v[196:199]\n"
      "ds_read2_b32 v[66:67], v224 offset0:194 offset1:195\n"
      "v_mfma_f32_16x16x32_f16 v[200:203], v[132:135], v[164:167], v[200:203]\n"
      "ds_read_b128 v[96:99], v228 offset:256\n"
      "v_mfma_f32_16x16x32_f16 v[196:199], v[128:131], v[164:167], v[196:199]\n"
      "v_mfma_f32_16x16x32_f16 v[200:203], v[136:139], v[160:163], v[200:203]\n"
      "ds_read2_b32 v[68:69], v224 offset0:64 offset1:65\n"
      "v_mfma_f32_16x16x32_f16 v[204:207], v[136:139], v[164:167], v[204:207]\n"
      "ds_read2_b32 v[70:71], v224 offset0:66 offset1:67\n"
      "v_mfma_f32_16x16x32_f16 v[208:211], v[136:139], v[168:171], v[208:211]\n"
      "buffer_load_dwordx4 a[208:211], v12, s[32:35], s67 offen offset:0 sc1\n"
      "v_mfma_f32_16x16x32_f16 v[200:203], v[128:131], v[168:171], v[200:203]\n"
      "ds_read_b128 v[100:103], v228 offset:768\n"
      "v_mfma_f32_16x16x32_f16 v[204:207], v[132:135], v[168:171], v[204:207]\n"
      "ds_read2_b32 v[72:73], v225 offset0:192 offset1:193\n"
      "s_waitcnt lgkmcnt(15)\n"
      "v_mfma_f32_16x16x32_f16 v[204:207], v[140:143], v[160:163], v[204:207]\n"
      "ds_read2_b32 v[74:75], v225 offset0:194 offset1:195\n"
      "v_mfma_f32_16x16x32_f16 v[208:211], v[140:143], v[164:167], v[208:211]\n"
      "v_mfma_f32_16x16x32_f16 v[212:215], v[140:143], v[168:171], v[212:215]\n"
      "ds_read_b128 v[104:107], v228 offset:1280\n"
      "v_mfma_f32_16x16x32_f16 v[216:219], v[140:143], v[172:175], v[216:219]\n"
      "ds_read2_b32 v[76:77], v225 offset0:64 offset1:65\n"
      "buffer_load_dwordx4 a[212:215], v12, s[32:35], s67 offen offset:1024 sc1\n"
      "v_mfma_f32_16x16x32_f16 v[204:207], v[128:131], v[172:175], v[204:207]\n"
      "v_mfma_f32_16x16x32_f16 v[208:211], v[132:135], v[172:175], v[208:211]\n"
      "ds_read2_b32 v[78:79], v225 offset0:66 offset1:67\n"
      "v_mfma_f32_16x16x32_f16 v[212:215], v[136:139], v[172:175], v[212:215]\n"
      "ds_read_b128 v[108:111], v228 offset:1792\n"
      "v_mfma_f32_16x16x32_f16 v[208:211], v[144:147], v[160:163], v[208:211]\n"
      "v_mfma_f32_16x16x32_f16 v[212:215], v[144:147], v[164:167], v[212:215]\n"
      "ds_read2_b32 v[80:81], v226 offset0:192 offset1:193\n"
      "v_mfma_f32_16x16x32_f16 v[216:219], v[144:147], v[168:171], v[216:219]\n"
      "ds_read2_b32 v[82:83], v226 offset0:194 offset1:195\n"
      "buffer_load_dwordx4 a[216:219], v12, s[32:35], s67 offen offset:2048 sc1\n"
      "v_mfma_f32_16x16x32_f16 v[220:223], v[144:147], v[172:175], v[220:223]\n"
      "ds_read_b128 v[112:115], v228 offset:2304\n"
      "v_mfma_f32_16x16x32_f16 v[208:211], v[128:131], v[176:179], v[208:211]\n"
      "v_mfma_f32_16x16x32_f16 v[212:215], v[132:135], v[176:179], v[212:215]\n"
      "ds_read2_b32 v[84:85], v226 offset0:64 offset1:65\n"
      "v_mfma_f32_16x16x32_f16 v[216:219], v[136:139], v[176:179], v[216:219]\n"
      "ds_read2_b32 v[86:87], v226 offset0:66 offset1:67\n"
      "v_mfma_f32_16x16x32_f16 v[220:223], v[140:143], v[176:179], v[220:223]\n"
      "s_waitcnt lgkmcnt(15)\n"
      "v_mfma_f32_16x16x32_f16 v[212:215], v[148:151], v[160:163], v[212:215]\n"
      "ds_read_b128 v[116:119], v228 offset:2816\n"
      "buffer_load_dwordx4 a[220:223], v242, s[32:35], 0 offen offset:0 sc1\n"
      "v_mfma_f32_16x16x32_f16 v[216:219], v[148:151], v[164:167], v[216:219]\n"
      "ds_read2_b32 v[88:89], v227 offset0:192 offset1:193\n"
      "v_mfma_f32_16x16x32_f16 v[220:223], v[148:151], v[168:171], v[220:223]\n"
      "ds_read2_b32 v[90:91], v227 offset0:194 offset1:195\n"
      "v_mfma_f32_16x16x32_f16 v[212:215], v[128:131], v[180:183], v[212:215]\n"
      "v_mfma_f32_16x16x32_f16 v[216:219], v[132:135], v[180:183], v[216:219]\n"
      "ds_read_b128 v[120:123], v228 offset:3328\n"
      "v_mfma_f32_16x16x32_f16 v[220:223], v[136:139], v[180:183], v[220:223]\n"
      "ds_read2_b32 v[92:93], v227 offset0:64 offset1:65\n"
      "v_mfma_f32_16x16x32_f16 v[216:219], v[152:155], v[160:163], v[216:219]\n"
      "buffer_load_dwordx4 a[224:227], v242, s[32:35], 0 offen offset:1024 sc1\n"
      "v_mfma_f32_16x16x32_f16 v[220:223], v[152:155], v[164:167], v[220:223]\n"
      "ds_read2_b32 v[94:95], v227 offset0:66 offset1:67\n"
      "v_mfma_f32_16x16x32_f16 v[216:219], v[128:131], v[184:187], v[216:219]\n"
      "ds_read_b128 v[124:127], v228 offset:3840\n"
      "v_mfma_f32_16x16x32_f16 v[220:223], v[132:135], v[184:187], v[220:223]\n"
      "v_mfma_f32_16x16x32_f16 v[220:223], v[156:159], v[160:163], v[220:223]\n"
      "v_mfma_f32_16x16x32_f16 v[220:223], v[128:131], v[188:191], v[220:223]\n"
      "s_waitcnt lgkmcnt(15)\n"
      "v_mfma_f32_16x16x32_f16 v[192:195], v[64:67], v[96:99], v[192:195]\n"
      "ds_read2_b32 v[128:129], v224 offset0:176 offset1:177\n"
      "buffer_load_dwordx4 a[228:231], v242, s[32:35], 0 offen offset:2048 sc1\n"
      "s_waitcnt vmcnt(18)\n"
      "v_mov_b32 v6, s56\n"
      "s_mov_b64 exec, 1\n"
      "buffer_store_dword v6, off, s[44:47], s84 sc1\n"
      "s_mov_b64 exec, -1\n"
      "v_mfma_f32_16x16x32_f16 v[196:199], v[68:71], v[96:99], v[196:199]\n"
      "ds_read2_b32 v[130:131], v224 offset0:178 offset1:179\n"
      "v_mfma_f32_16x16x32_f16 v[200:203], v[68:71], v[100:103], v[200:203]\n"
      "ds_read_b128 v[160:163], v228 offset:192\n"
      "ds_read_b64 v[48:49], v229 offset:0\n"
      "v_mfma_f32_16x16x32_f16 v[196:199], v[64:67], v[100:103], v[196:199]\n"
      "v_mfma_f32_16x16x32_f16 v[200:203], v[72:75], v[96:99], v[200:203]\n"
      "ds_read2_b32 v[132:133], v224 offset0:48 offset1:49\n"
      "v_mfma_f32_16x16x32_f16 v[204:207], v[72:75], v[100:103], v[204:207]\n"
      "ds_read2_b32 v[134:135], v224 offset0:50 offset1:51\n"
      "v_mfma_f32_16x16x32_f16 v[208:211], v[72:75], v[104:107], v[208:211]\n"
      "buffer_load_dwordx4 a[232:235], v242, s[32:35], 0 offen offset:3072 sc1\n"
      "ds_read_b64 v[50:51], v229 offset:512\n"
      "v_mfma_f32_16x16x32_f16 v[200:203], v[64:67], v[104:107], v[200:203]\n"
      "ds_read_b128 v[164:167], v228 offset:704\n"
      "v_mfma_f32_16x16x32_f16 v[204:207], v[68:71], v[104:107], v[204:207]\n"
      "ds_read2_b32 v[136:137], v225 offset0:176 offset1:177\n"
      "s_waitcnt lgkmcnt(15)\n"
      "v_mfma_f32_16x16x32_f16 v[204:207], v[76:79], v[96:99], v[204:207]\n"
      "ds_read2_b32 v[138:139], v225 offset0:178 offset1:179\n"
      "v_mfma_f32_16x16x32_f16 v[208:211], v[76:79], v[100:103], v[208:211]\n"
      "ds_read_b64 v[52:53], v229 offset:1024\n"
      "v_mfma_f32_16x16x32_f16 v[212:215], v[76:79], v[104:107], v[212:215]\n"
      "ds_read_b128 v[168:171], v228 offset:1216\n"
      "v_mfma_f32_16x16x32_f16 v[216:219], v[76:79], v[108:111], v[216:219]\n"
      "ds_read2_b32 v[140:141], v225 offset0:48 offset1:49\n"
      "buffer_load_dwordx4 a[236:239], v242, s[32:35], s65 offen offset:0 sc1\n"
      "v_mfma_f32_16x16x32_f16 v[204:207], v[64:67], v[108:111], v[204:207]\n"
      "v_mfma_f32_16x16x32_f16 v[208:211], v[68:71], v[108:111], v[208:211]\n"
      "ds_read2_b32 v[142:143], v225 offset0:50 offset1:51\n"
      "ds_read_b64 v[54:55], v229 offset:1536\n"
      "v_mfma_f32_16x16x32_f16 v[212:215], v[72:75], v[108:111], v[212:215]\n"
      "ds_read_b128 v[172:175], v228 offset:1728\n"
      "v_mfma_f32_16x16x32_f16 v[208:211], v[80:83], v[96:99], v[208:211]\n"
      "v_mfma_f32_16x16x32_f16 v[212:215], v[80:83], v[100:103], v[212:215]\n"
      "ds_read2_b32 v[144:145], v226 offset0:176 offset1:177\n"
      "v_mfma_f32_16x16x32_f16 v[216:219], v[80:83], v[104:107], v[216:219]\n"
      "ds_read2_b32 v[146:147], v226 offset0:178 offset1:179\n"
      "buffer_load_dwordx4 a[0:3], v242, s[32:35], s65 offen offset:1024 sc1\n"
      "ds_read_b64 v[56:57], v229 offset:2048\n"
      "v_mfma_f32_16x16x32_f16 v[220:223], v[80:83], v[108:111], v[220:223]\n"
      "ds_read_b128 v[176:179], v228 offset:2240\n"
      "v_mfma_f32_16x16x32_f16 v[208:211], v[64:67], v[112:115], v[208:211]\n"
      "v_mfma_f32_16x16x32_f16 v[212:215], v[68:71], v[112:115], v[212:215]\n"
      "ds_read2_b32 v[148:149], v226 offset0:48 offset1:49\n"
      "v_mfma_f32_16x16x32_f16 v[216:219], v[72:75], v[112:115], v[216:219]\n"
      "ds_read2_b32 v[150:151], v226 offset0:50 offset1:51\n"
      "ds_read_b64 v[58:59], v229 offset:2560\n"
      "v_mfma_f32_16x16x32_f16 v[220:223], v[76:79], v[112:115], v[220:223]\n"
      "v_mfma_f32_16x16x32_f16 v[212:215], v[84:87], v[96:99], v[212:215]\n"
      "ds_read_b128 v[180:183], v228 offset:2752\n"
      "buffer_load_dwordx4 a[4:7], v242, s[32:35], s65 offen offset:2048 sc1\n"
      "v_mfma_f32_16x16x32_f16 v[216:219], v[84:87], v[100:103], v[216:219]\n"
      "ds_read2_b32 v[152:153], v227 offset0:176 offset1:177\n"
      "v_mfma_f32_16x16x32_f16 v[220:223], v[84:87], v[104:107], v[220:223]\n"
      "ds_read2_b32 v[154:155], v227 offset0:178 offset1:179\n"
      "ds_read_b64 v[60:61], v229 offset:3072\n"
      "v_mfma_f32_16x16x32_f16 v[212:215], v[64:67], v[116:119], v[212:215]\n"
      "v_mfma_f32_16x16x32_f16 v[216:219], v[68:71], v[116:119], v[216:219]\n"
      "ds_read_b128 v[184:187], v228 offset:3264\n"
      "v_mfma_f32_16x16x32_f16 v[220:223], v[72:75], v[116:119], v[220:223]\n"
      "ds_read2_b32 v[156:157], v227 offset0:48 offset1:49\n"
      "s_waitcnt lgkmcnt(15)\n"
      "v_mfma_f32_16x16x32_f16 v[216:219], v[88:91], v[96:99], v[216:219]\n"
      "buffer_load_dwordx4 a[8:11], v242, s[32:35], s65 offen offset:3072 sc1\n"
      "ds_read_b64 v[62:63], v229 offset:3584\n"
      "v_mfma_f32_16x16x32_f16 v[220:223], v[88:91], v[100:103], v[220:223]\n"
      "ds_read2_b32 v[158:159], v227 offset0:50 offset1:51\n"
      "v_mfma_f32_16x16x32_f16 v[216:219], v[64:67], v[120:123], v[216:219]\n"
      "ds_read_b128 v[188:191], v228 offset:3776\n"
      "v_mfma_f32_16x16x32_f16 v[220:223], v[68:71], v[120:123], v[220:223]\n"
      "v_mfma_f32_16x16x32_f16 v[220:223], v[92:95], v[96:99], v[220:223]\n"
      "v_mfma_f32_16x16x32_f16 v[220:223], v[64:67], v[124:127], v[220:223]\n"
      "v_mfma_f32_16x16x32_f16 v[192:195], v[128:131], v[160:163], v[192:195]\n"
      "ds_read2_b32 v[64:65], v224 offset0:160 offset1:161\n"
      "buffer_load_dwordx4 a[12:15], v242, s[32:35], s66 offen offset:0 sc1\n"
      "v_mfma_f32_16x16x32_f16 v[196:199], v[132:135], v[160:163], v[196:199]\n"
      "ds_read2_b32 v[66:67], v224 offset0:162 offset1:163\n"
      "v_fma_mix_f32 v16, v48, v9, 0 op_sel:[0,0,0] op_sel_hi:[1,0,0]\n"
      "v_mfma_f32_16x16x32_f16 v[200:203], v[132:135], v[164:167], v[200:203]\n"
      "ds_read_b128 v[96:99], v228 offset:128\n"
      "v_mfma_f32_16x16x32_f16 v[196:199], v[128:131], v[164:167], v[196:199]\n"
      "s_lshl_b32 s52, s48, 9\n"
      "v_mfma_f32_16x16x32_f16 v[200:203], v[136:139], v[160:163], v[200:203]\n"
      "ds_read2_b32 v[68:69], v224 offset0:32 offset1:33\n"
      "s_add_u32 s52, s52, 262144\n"
      "v_fma_mix_f32 v17, v48, v9, 0 op_sel:[1,0,0] op_sel_hi:[1,0,0]\n"
      "v_mfma_f32_16x16x32_f16 v[204:207], v[136:139], v[164:167], v[204:207]\n"
      "ds_read2_b32 v[70:71], v224 offset0:34 offset1:35\n"
      "v_mfma_f32_16x16x32_f16 v[208:211], v[136:139], v[168:171], v[208:211]\n"
      "buffer_load_dwordx4 a[16:19], v242, s[32:35], s66 offen offset:1024 sc1\n"
      "v_lshl_add_u32 v245, v8, 7, s52\n"
      "v_mfma_f32_16x16x32_f16 v[200:203], v[128:131], v[168:171], v[200:203]\n"
      "ds_read_b128 v[100:103], v228 offset:640\n"
      "v_fma_mix_f32 v18, v49, v9, 0 op_sel:[0,0,0] op_sel_hi:[1,0,0]\n"
      "v_mfma_f32_16x16x32_f16 v[204:207], v[132:135], v[168:171], v[204:207]\n"
      "ds_read2_b32 v[72:73], v225 offset0:160 offset1:161\n"
      "v_mov_b32 v253, v245\n"
      "v_mfma_f32_16x16x32_f16 v[204:207], v[140:143], v[160:163], v[204:207]\n"
      "ds_read2_b32 v[74:75], v225 offset0:162 offset1:163\n"
      "v_mfma_f32_16x16x32_f16 v[208:211], v[140:143], v[164:167], v[208:211]\n"
      "s_mul_i32 s54, s48, 196608\n"
      "v_fma_mix_f32 v19, v49, v9, 0 op_sel:[1,0,0] op_sel_hi:[1,0,0]\n"
      "v_mfma_f32_16x16x32_f16 v[212:215], v[140:143], v[168:171], v[212:215]\n"
      "ds_read_b128 v[104:107], v228 offset:1152\n"
      "s_waitcnt lgkmcnt(15)\n"
      "v_mfma_f32_16x16x32_f16 v[216:219], v[140:143], v[172:175], v[216:219]\n"
      "ds_read2_b32 v[76:77], v225 offset0:32 offset1:33\n"
      "buffer_load_dwordx4 a[20:23], v242, s[32:35], s66 offen offset:2048 sc1\n"
      "s_add_u32 s54, s54, 12582912\n"
      "v_mfma_f32_16x16x32_f16 v[204:207], v[128:131], v[172:175], v[204:207]\n"
      "s_bfe_u32 s52, s48, 0x10001\n"
      "v_mfma_f32_16x16x32_f16 v[208:211], v[132:135], v[172:175], v[208:211]\n"
      "ds_read2_b32 v[78:79], v225 offset0:34 offset1:35\n"
      "v_fma_mix_f32 v20, v50, v9, 0 op_sel:[0,0,0] op_sel_hi:[1,0,0]\n"
      "v_mfma_f32_16x16x32_f16 v[212:215], v[136:139], v[172:175], v[212:215]\n"
      "ds_read_b128 v[108:111], v228 offset:1664\n"
      "s_lshl1_add_u32 s52, s31, s52\n"
      "v_mfma_f32_16x16x32_f16 v[208:211], v[144:147], v[160:163], v[208:211]\n"
      "v_mfma_f32_16x16x32_f16 v[212:215], v[144:147], v[164:167], v[212:215]\n"
      "ds_read2_b32 v[80:81], v226 offset0:160 offset1:161\n"
      "s_mul_i32 s53, s52, 3072\n"
      "v_fma_mix_f32 v21, v50, v9, 0 op_sel:[1,0,0] op_sel_hi:[1,0,0]\n"
      "v_mfma_f32_16x16x32_f16 v[216:219], v[144:147], v[168:171], v[216:219]\n"
      "ds_read2_b32 v[82:83], v226 offset0:162 offset1:163\n"
      "buffer_load_dwordx4 a[24:27], v242, s[32:35], s66 offen offset:3072 sc1\n"
      "v_mfma_f32_16x16x32_f16 v[220:223], v[144:147], v[172:175], v[220:223]\n"
      "ds_read_b128 v[112:115], v228 offset:2176\n"
      "s_add_u32 s53, s53, s54\n"
      "v_mfma_f32_16x16x32_f16 v[208:211], v[128:131], v[176:179], v[208:211]\n"
      "v_fma_mix_f32 v22, v51, v9, 0 op_sel:[0,0,0] op_sel_hi:[1,0,0]\n"
      "v_mfma_f32_16x16x32_f16 v[212:215], v[132:135], v[176:179], v[212:215]\n"
      "ds_read2_b32 v[84:85], v226 offset0:32 offset1:33\n"
      "v_add_u32 v247, s53, v2\n"
      "v_mfma_f32_16x16x32_f16 v[216:219], v[136:139], v[176:179], v[216:219]\n"
      "ds_read2_b32 v[86:87], v226 offset0:34 offset1:35\n"
      "s_lshl_b32 s52, s52, 14\n"
      "v_mfma_f32_16x16x32_f16 v[220:223], v[140:143], v[176:179], v[220:223]\n"
      "v_fma_mix_f32 v23, v51, v9, 0 op_sel:[1,0,0] op_sel_hi:[1,0,0]\n"
      "v_mfma_f32_16x16x32_f16 v[212:215], v[148:151], v[160:163], v[212:215]\n"
      "ds_read_b128 v[116:119], v228 offset:2688\n"
      "buffer_load_dwordx4 a[28:31], v242, s[32:35], s67 offen offset:0 sc1\n"
      "s_mov_b64 exec, 0xf\n"
      "v_add_u32 v245, s52, v245\n"
      "s_mov_b64 exec, -1\n"
      "v_mfma_f32_16x16x32_f16 v[216:219], v[148:151], v[164:167], v[216:219]\n"
      "ds_read2_b32 v[88:89], v227 offset0:160 offset1:161\n"
      "v_mfma_f32_16x16x32_f16 v[220:223], v[148:151], v[168:171], v[220:223]\n"
      "ds_read2_b32 v[90:91], v227 offset0:162 offset1:163\n"
      "s_bfe_u32 s52, s48, 0x10001\n"
      "v_mfma_f32_16x16x32_f16 v[212:215], v[128:131], v[180:183], v[212:215]\n"
      "v_fma_mix_f32 v24, v52, v9, 0 op_sel:[0,0,0] op_sel_hi:[1,0,0]\n"
      "v_mfma_f32_16x16x32_f16 v[216:219], v[132:135], v[180:183], v[216:219]\n"
      "ds_read_b128 v[120:123], v228 offset:3200\n"
      "s_lshl1_add_u32 s52, s31, s52\n"
      "v_mfma_f32_16x16x32_f16 v[220:223], v[136:139], v[180:183], v[220:223]\n"
      "ds_read2_b32 v[92:93], v227 offset0:32 offset1:33\n"
      "v_mfma_f32_16x16x32_f16 v[216:219], v[152:155], v[160:163], v[216:219]\n"
      "buffer_load_dwordx4 a[32:35], v242, s[32:35], s67 offen offset:1024 sc1\n"
      "s_add_u32 s52, s52, 8\n"
      "v_fma_mix_f32 v25, v52, v9, 0 op_sel:[1,0,0] op_sel_hi:[1,0,0]\n"
      "v_mfma_f32_16x16x32_f16 v[220:223], v[152:155], v[164:167], v[220:223]\n"
      "ds_read2_b32 v[94:95], v227 offset0:34 offset1:35\n"
      "s_mul_i32 s53, s52, 3072\n"
      "s_waitcnt lgkmcnt(15)\n"
      "v_mfma_f32_16x16x32_f16 v[216:219], v[128:131], v[184:187], v[216:219]\n"
      "ds_read_b128 v[124:127], v228 offset:3712\n"
      "v_mfma_f32_16x16x32_f16 v[220:223], v[132:135], v[184:187], v[220:223]\n"
      "s_add_u32 s53, s53, s54\n"
      "v_fma_mix_f32 v26, v53, v9, 0 op_sel:[0,0,0] op_sel_hi:[1,0,0]\n"
      "v_mfma_f32_16x16x32_f16 v[220:223], v[156:159], v[160:163], v[220:223]\n"
      "v_mfma_f32_16x16x32_f16 v[220:223], v[128:131], v[188:191], v[220:223]\n"
      "v_add_u32 v248, s53, v2\n"
      "v_mfma_f32_16x16x32_f16 v[192:195], v[64:67], v[96:99], v[192:195]\n"
      "ds_read2_b32 v[128:129], v224 offset0:144 offset1:145\n"
      "buffer_load_dwordx4 a[36:39], v242, s[32:35], s67 offen offset:2048 sc1\n"
      "v_mfma_f32_16x16x32_f16 v[196:199], v[68:71], v[96:99], v[196:199]\n"
      "ds_read2_b32 v[130:131], v224 offset0:146 offset1:147\n"
      "s_lshl_b32 s52, s52, 14\n"
      "v_fma_mix_f32 v27, v53, v9, 0 op_sel:[1,0,0] op_sel_hi:[1,0,0]\n"
      "v_mfma_f32_16x16x32_f16 v[200:203], v[68:71], v[100:103], v[200:203]\n"
      "ds_read_b128 v[160:163], v228 offset:64\n"
      "v_mfma_f32_16x16x32_f16 v[196:199], v[64:67], v[100:103], v[196:199]\n"
      "s_mov_b64 exec, 0xf0\n"
      "v_add_u32 v245, s52, v245\n"
      "s_mov_b64 exec, -1\n"
      "v_mfma_f32_16x16x32_f16 v[200:203], v[72:75], v[96:99], v[200:203]\n"
      "ds_read2_b32 v[132:133], v224 offset0:16 offset1:17\n"
      "s_bfe_u32 s52, s48, 0x10001\n"
      "v_fma_mix_f32 v28, v54, v9, 0 op_sel:[0,0,0] op_sel_hi:[1,0,0]\n"
      "v_mfma_f32_16x16x32_f16 v[204:207], v[72:75], v[100:103], v[204:207]\n"
      "ds_read2_b32 v[134:135], v224 offset0:18 offset1:19\n"
      "s_waitcnt lgkmcnt(15)\n"
      "v_mfma_f32_16x16x32_f16 v[208:211], v[72:75], v[104:107], v[208:211]\n"
      "buffer_load_dwordx4 a[40:43], v243, s[32:35], 0 offen offset:0 sc1\n"
      "s_xor_b32 s52, s52, 1\n"
      "v_mfma_f32_16x16x32_f16 v[200:203], v[64:67], v[104:107], v[200:203]\n"
      "ds_read_b128 v[164:167], v228 offset:576\n"
      "v_fma_mix_f32 v29, v54, v9, 0 op_sel:[1,0,0] op_sel_hi:[1,0,0]\n"
      "v_mfma_f32_16x16x32_f16 v[204:207], v[68:71], v[104:107], v[204:207]\n"
      "ds_read2_b32 v[136:137], v225 offset0:144 offset1:145\n"
      "s_lshl1_add_u32 s52, s31, s52\n"
      "v_mfma_f32_16x16x32_f16 v[204:207], v[76:79], v[96:99], v[204:207]\n"
      "ds_read2_b32 v[138:139], v225 offset0:146 offset1:147\n"
      "v_mfma_f32_16x16x32_f16 v[208:211], v[76:79], v[100:103], v[208:211]\n"
      "s_mul_i32 s53, s52, 3072\n"
      "v_fma_mix_f32 v30, v55, v9, 0 op_sel:[0,0,0] op_sel_hi:[1,0,0]\n"
      "v_mfma_f32_16x16x32_f16 v[212:215], v[76:79], v[104:107], v[212:215]\n"
      "ds_read_b128 v[168:171], v228 offset:1088\n"
      "v_mfma_f32_16x16x32_f16 v[216:219], v[76:79], v[108:111], v[216:219]\n"
      "ds_read2_b32 v[140:141], v225 offset0:16 offset1:17\n"
      "buffer_load_dwordx4 a[44:47], v243, s[32:35], 0 offen offset:1024 sc1\n"
      "s_add_u32 s53, s53, s54\n"
      "v_mfma_f32_16x16x32_f16 v[204:207], v[64:67], v[108:111], v[204:207]\n"
      "v_add_u32 v249, s53, v2\n"
      "v_mfma_f32_16x16x32_f16 v[208:211], v[68:71], v[108:111], v[208:211]\n"
      "ds_read2_b32 v[142:143], v225 offset0:18 offset1:19\n"
      "v_fma_mix_f32 v31, v55, v9, 0 op_sel:[1,0,0] op_sel_hi:[1,0,0]\n"
      "v_mfma_f32_16x16x32_f16 v[212:215], v[72:75], v[108:111], v[212:215]\n"
      "ds_read_b128 v[172:175], v228 offset:1600\n"
      "s_lshl_b32 s52, s52, 14\n"
      "v_mfma_f32_16x16x32_f16 v[208:211], v[80:83], v[96:99], v[208:211]\n"
      "v_mfma_f32_16x16x32_f16 v[212:215], v[80:83], v[100:103], v[212:215]\n"
      "ds_read2_b32 v[144:145], v226 offset0:144 offset1:145\n"
      "s_mov_b64 exec, 0xf\n"
      "v_add_u32 v253, s52, v253\n"
      "s_mov_b64 exec, -1\n"
      "v_fma_mix_f32 v32, v56, v9, 0 op_sel:[0,0,0] op_sel_hi:[1,0,0]\n"
      "v_mfma_f32_16x16x32_f16 v[216:219], v[80:83], v[104:107], v[216:219]\n"
      "ds_read2_b32 v[146:147], v226 offset0:146 offset1:147\n"
      "buffer_load_dwordx4 a[48:51], v243, s[32:35], 0 offen offset:2048 sc1\n"
      "v_mfma_f32_16x16x32_f16 v[220:223], v[80:83], v[108:111], v[220:223]\n"
      "ds_read_b128 v[176:179], v228 offset:2112\n"
      "s_bfe_u32 s52, s48, 0x10001\n"
      "s_waitcnt lgkmcnt(15)\n"
      "v_mfma_f32_16x16x32_f16 v[208:211], v[64:67], v[112:115], v[208:211]\n"
      "v_fma_mix_f32 v33, v56, v9, 0 op_sel:[1,0,0] op_sel_hi:[1,0,0]\n"
      "v_mfma_f32_16x16x32_f16 v[212:215], v[68:71], v[112:115], v[212:215]\n"
      "ds_read2_b32 v[148:149], v226 offset0:16 offset1:17\n"
      "s_xor_b32 s52, s52, 1\n"
      "v_mfma_f32_16x16x32_f16 v[216:219], v[72:75], v[112:115], v[216:219]\n"
      "ds_read2_b32 v[150:151], v226 offset0:18 offset1:19\n"
      "v_mfma_f32_16x16x32_f16 v[220:223], v[76:79], v[112:115], v[220:223]\n"
      "s_lshl1_add_u32 s52, s31, s52\n"
      "v_mfma_f32_16x16x32_f16 v[212:215], v[84:87], v[96:99], v[212:215]\n"
      "ds_read_b128 v[180:183], v228 offset:2624\n"
      "buffer_load_dwordx4 a[52:55], v243, s[32:35], 0 offen offset:3072 sc1\n"
      "s_add_u32 s52, s52, 8\n"
      "v_fma_mix_f32 v34, v57, v9, 0 op_sel:[0,0,0] op_sel_hi:[1,0,0]\n"
      "v_mfma_f32_16x16x32_f16 v[216:219], v[84:87], v[100:103], v[216:219]\n"
      "ds_read2_b32 v[152:153], v227 offset0:144 offset1:145\n"
      "v_mfma_f32_16x16x32_f16 v[220:223], v[84:87], v[104:107], v[220:223]\n"
      "ds_read2_b32 v[154:155], v227 offset0:146 offset1:147\n"
      "s_mul_i32 s53, s52, 3072\n"
      "v_mfma_f32_16x16x32_f16 v[212:215], v[64:67], v[116:119], v[212:215]\n"
      "v_fma_mix_f32 v35, v57, v9, 0 op_sel:[1,0,0] op_sel_hi:[1,0,0]\n"
      "v_mfma_f32_16x16x32_f16 v[216:219], v[68:71], v[116:119], v[216:219]\n"
      "ds_read_b128 v[184:187], v228 offset:3136\n"
      "s_add_u32 s53, s53, s54\n"
      "v_mfma_f32_16x16x32_f16 v[220:223], v[72:75], v[116:119], v[220:223]\n"
      "ds_read2_b32 v[156:157], v227 offset0:16 offset1:17\n"
      "v_mfma_f32_16x16x32_f16 v[216:219], v[88:91], v[96:99], v[216:219]\n"
      "buffer_load_dwordx4 a[56:59], v243, s[32:35], s65 offen offset:0 sc1\n"
      "v_add_u32 v252, s53, v2\n"
      "v_fma_mix_f32 v36, v58, v9, 0 op_sel:[0,0,0] op_sel_hi:[1,0,0]\n"
      "v_mfma_f32_16x16x32_f16 v[220:223], v[88:91], v[100:103], v[220:223]\n"
      "ds_read2_b32 v[158:159], v227 offset0:18 offset1:19\n"
      "v_mfma_f32_16x16x32_f16 v[216:219], v[64:67], v[120:123], v[216:219]\n"
      "ds_read_b128 v[188:191], v228 offset:3648\n"
      "s_lshl_b32 s52, s52, 14\n"
      "v_mfma_f32_16x16x32_f16 v[220:223], v[68:71], v[120:123], v[220:223]\n"
      "s_mov_b64 exec, 0xf0\n"
      "v_add_u32 v253, s52, v253\n"
      "s_mov_b64 exec, -1\n"
      "v_fma_mix_f32 v37, v58, v9, 0 op_sel:[1,0,0] op_sel_hi:[1,0,0]\n"
      "v_mfma_f32_16x16x32_f16 v[220:223], v[92:95], v[96:99], v[220:223]\n"
      "v_mfma_f32_16x16x32_f16 v[220:223], v[64:67], v[124:127], v[220:223]\n"
      "s_lshr_b32 s52, s48, 3\n"
      "s_waitcnt lgkmcnt(15)\n"
      "v_mfma_f32_16x16x32_f16 v[192:195], v[128:131], v[160:163], v[192:195]\n"
      "ds_read2_b32 v[64:65], v224 offset0:128 offset1:129\n"
      "buffer_load_dwordx4 a[60:63], v243, s[32:35], s65 offen offset:1024 sc1\n"
      "v_mfma_f32_16x16x32_f16 v[196:199], v[132:135], v[160:163], v[196:199]\n"
      "ds_read2_b32 v[66:67], v224 offset0:130 offset1:131\n"
      "s_mul_i32 s52, s52, 160\n"
      "v_fma_mix_f32 v38, v59, v9, 0 op_sel:[0,0,0] op_sel_hi:[1,0,0]\n"
      "v_mfma_f32_16x16x32_f16 v[200:203], v[132:135], v[164:167], v[200:203]\n"
      "ds_read_b128 v[96:99], v228 offset:0\n"
      "v_mfma_f32_16x16x32_f16 v[196:199], v[128:131], v[164:167], v[196:199]\n"
      "s_mul_i32 s53, s49, 20\n"
      "v_mfma_f32_16x16x32_f16 v[200:203], v[136:139], v[160:163], v[200:203]\n"
      "ds_read2_b32 v[68:69], v224 offset0:0 offset1:1\n"
      "v_fma_mix_f32 v39, v59, v9, 0 op_sel:[1,0,0] op_sel_hi:[1,0,0]\n"
      "v_mfma_f32_16x16x32_f16 v[204:207], v[136:139], v[164:167], v[204:207]\n"
      "ds_read2_b32 v[70:71], v224 offset0:2 offset1:3\n"
      "s_add_u32 s52, s52, s53\n"
      "v_mfma_f32_16x16x32_f16 v[208:211], v[136:139], v[168:171], v[208:211]\n"
      "buffer_load_dwordx4 a[64:67], v243, s[32:35], s65 offen offset:2048 sc1\n"
      "s_lshl_b32 s52, s52, 7\n"
      "v_mfma_f32_16x16x32_f16 v[200:203], v[128:131], v[168:171], v[200:203]\n"
      "ds_read_b128 v[100:103], v228 offset:512\n"
      "v_fma_mix_f32 v40, v60, v9, 0 op_sel:[0,0,0] op_sel_hi:[1,0,0]\n"
      "v_mfma_f32_16x16x32_f16 v[204:207], v[132:135], v[168:171], v[204:207]\n"
      "ds_read2_b32 v[72:73], v225 offset0:128 offset1:129\n"
      "s_add_u32 s52, s52, 130048\n"
      "s_waitcnt lgkmcnt(15)\n"
      "v_mfma_f32_16x16x32_f16 v[204:207], v[140:143], v[160:163], v[204:207]\n"
      "ds_read2_b32 v[74:75], v225 offset0:130 offset1:131\n"
      "v_mfma_f32_16x16x32_f16 v[208:211], v[140:143], v[164:167], v[208:211]\n"
      "s_mov_b64 exec, 0x0fffff00\n"
      "v_add_u32 v253, s52, v11\n"
      "s_mov_b64 exec, -1\n"
      "v_mfma_f32_16x16x32_f16 v[212:215], v[140:143], v[168:171], v[212:215]\n"
      "ds_read_b128 v[104:107], v228 offset:1024\n"
      "v_fma_mix_f32 v41, v60, v9, 0 op_sel:[1,0,0] op_sel_hi:[1,0,0]\n"
      "v_mfma_f32_16x16x32_f16 v[216:219], v[140:143], v[172:175], v[216:219]\n"
      "ds_read2_b32 v[76:77], v225 offset0:0 offset1:1\n"
      "buffer_load_dwordx4 a[68:71], v243, s[32:35], s65 offen offset:3072 sc1\n"
      "s_add_u32 s52, s31, 1\n"
      "v_mfma_f32_16x16x32_f16 v[204:207], v[128:131], v[172:175], v[204:207]\n"
      "v_mfma_f32_16x16x32_f16 v[208:211], v[132:135], v[172:175], v[208:211]\n"
      "ds_read2_b32 v[78:79], v225 offset0:2 offset1:3\n"
      "s_and_b32 s52, s52, 3\n"
      "v_fma_mix_f32 v42, v61, v9, 0 op_sel:[0,0,0] op_sel_hi:[1,0,0]\n"
      "v_mfma_f32_16x16x32_f16 v[212:215], v[136:139], v[172:175], v[212:215]\n"
      "ds_read_b128 v[108:111], v228 offset:1536\n"
      "s_mul_i32 s89, s52, 49152\n"
      "v_mfma_f32_16x16x32_f16 v[208:211], v[144:147], v[160:163], v[208:211]\n"
      "v_mfma_f32_16x16x32_f16 v[212:215], v[144:147], v[164:167], v[212:215]\n"
      "ds_read2_b32 v[80:81], v226 offset0:128 offset1:129\n"
      "s_add_u32 s52, s31, 2\n"
      "v_fma_mix_f32 v43, v61, v9, 0 op_sel:[1,0,0] op_sel_hi:[1,0,0]\n"
      "v_mfma_f32_16x16x32_f16 v[216:219], v[144:147], v[168:171], v[216:219]\n"
      "ds_read2_b32 v[82:83], v226 offset0:130 offset1:131\n"
      "buffer_load_dwordx4 a[72:75], v243, s[32:35], s66 offen offset:0 sc1\n"
      "v_mfma_f32_16x16x32_f16 v[220:223], v[144:147], v[172:175], v[220:223]\n"
      "ds_read_b128 v[112:115], v228 offset:2048\n"
      "s_and_b32 s52, s52, 3\n"
      "v_mfma_f32_16x16x32_f16 v[208:211], v[128:131], v[176:179], v[208:211]\n"
      "v_fma_mix_f32 v44, v62, v9, 0 op_sel:[0,0,0] op_sel_hi:[1,0,0]\n"
      "v_mfma_f32_16x16x32_f16 v[212:215], v[132:135], v[176:179], v[212:215]\n"
      "ds_read2_b32 v[84:85], v226 offset0:0 offset1:1\n"
      "s_mul_i32 s90, s52, 49152\n"
      "v_mfma_f32_16x16x32_f16 v[216:219], v[136:139], v[176:179], v[216:219]\n"
      "ds_read2_b32 v[86:87], v226 offset0:2 offset1:3\n"
      "v_mfma_f32_16x16x32_f16 v[220:223], v[140:143], v[176:179], v[220:223]\n"
      "s_add_u32 s52, s31, 3\n"
      "s_waitcnt lgkmcnt(15)\n"
      "v_mfma_f32_16x16x32_f16 v[212:215], v[148:151], v[160:163], v[212:215]\n"
      "ds_read_b128 v[116:119], v228 offset:2560\n"
      "buffer_load_dwordx4 a[76:79], v243, s[32:35], s66 offen offset:1024 sc1\n"
      "s_and_b32 s52, s52, 3\n"
      "v_fma_mix_f32 v45, v62, v9, 0 op_sel:[1,0,0] op_sel_hi:[1,0,0]\n"
      "v_mfma_f32_16x16x32_f16 v[216:219], v[148:151], v[164:167], v[216:219]\n"
      "ds_read2_b32 v[88:89], v227 offset0:128 offset1:129\n"
      "v_mfma_f32_16x16x32_f16 v[220:223], v[148:151], v[168:171], v[220:223]\n"
      "ds_read2_b32 v[90:91], v227 offset0:130 offset1:131\n"
      "s_mul_i32 s91, s52, 49152\n"
      "v_mfma_f32_16x16x32_f16 v[212:215], v[128:131], v[180:183], v[212:215]\n"
      "v_fma_mix_f32 v46, v63, v9, 0 op_sel:[0,0,0] op_sel_hi:[1,0,0]\n"
      "v_mfma_f32_16x16x32_f16 v[216:219], v[132:135], v[180:183], v[216:219]\n"
      "ds_read_b128 v[120:123], v228 offset:3072\n"
      "s_add_u32 s52, s31, 4\n"
      "v_mfma_f32_16x16x32_f16 v[220:223], v[136:139], v[180:183], v[220:223]\n"
      "ds_read2_b32 v[92:93], v227 offset0:0 offset1:1\n"
      "v_mfma_f32_16x16x32_f16 v[216:219], v[152:155], v[160:163], v[216:219]\n"
      "buffer_load_dwordx4 a[80:83], v243, s[32:35], s66 offen offset:2048 sc1\n"
      "s_and_b32 s52, s52, 3\n"
      "v_fma_mix_f32 v47, v63, v9, 0 op_sel:[1,0,0] op_sel_hi:[1,0,0]\n"
      "v_mfma_f32_16x16x32_f16 v[220:223], v[152:155], v[164:167], v[220:223]\n"
      "ds_read2_b32 v[94:95], v227 offset0:2 offset1:3\n"
      "v_mfma_f32_16x16x32_f16 v[216:219], v[128:131], v[184:187], v[216:219]\n"
      "ds_read_b128 v[124:127], v228 offset:3584\n"
      "s_mul_i32 s95, s52, 49152\n"
      "v_mfma_f32_16x16x32_f16 v[220:223], v[132:135], v[184:187], v[220:223]\n"
      "v_mfma_f32_16x16x32_f16 v[220:223], v[156:159], v[160:163], v[220:223]\n"
      "v_mfma_f32_16x16x32_f16 v[220:223], v[128:131], v[188:191], v[220:223]\n"
      "s_waitcnt lgkmcnt(15)\n"
      "v_mfma_f32_16x16x32_f16 v[192:195], v[64:67], v[96:99], v[192:195]\n"
      "buffer_load_dwordx4 a[84:87], v243, s[32:35], s66 offen offset:3072 sc1\n"
      "v_mfma_f32_16x16x32_f16 v[196:199], v[64:67], v[100:103], v[196:199]\n"
      "v_mfma_f32_16x16x32_f16 v[196:199], v[68:71], v[96:99], v[196:199]\n"
      "s_mov_b64 exec, 0xff\n"
      "buffer_load_dword v246, v245, s[32:35], 0 offen sc1\n"
      "s_mov_b64 exec, -1\n"
      "v_mfma_f32_16x16x32_f16 v[200:203], v[64:67], v[104:107], v[200:203]\n"
      "v_mfma_f32_16x16x32_f16 v[200:203], v[68:71], v[100:103], v[200:203]\n"
      "v_mfma_f32_16x16x32_f16 v[200:203], v[72:75], v[96:99], v[200:203]\n"
      "v_fma_f32 v16, v192, v10, v16\n"
      "v_fma_f32 v17, v193, v10, v17\n"
      "s_waitcnt lgkmcnt(12)\n"
      "v_mfma_f32_16x16x32_f16 v[204:207], v[64:67], v[108:111], v[204:207]\n"
      "buffer_load_dwordx4 a[88:91], v243, s[32:35], s67 offen offset:0 sc1\n"
      "v_fma_f32 v18, v194, v10, v18\n"
      "v_mfma_f32_16x16x32_f16 v[204:207], v[68:71], v[104:107], v[204:207]\n"
      "v_fma_f32 v19, v195, v10, v19\n"
      "v_fma_f32 v20, v196, v10, v20\n"
      "v_fma_f32 v21, v197, v10, v21\n"
      "v_mfma_f32_16x16x32_f16 v[204:207], v[72:75], v[100:103], v[204:207]\n"
      "v_cvt_pk_bf16_f32 v236, v16, v17\n"
      "v_fma_f32 v22, v198, v10, v22\n"
      "v_mfma_f32_16x16x32_f16 v[204:207], v[76:79], v[96:99], v[204:207]\n"
      "v_cvt_pk_bf16_f32 v237, v18, v19\n"
      "v_fma_f32 v23, v199, v10, v23\n"
      "s_waitcnt lgkmcnt(9)\n"
      "v_mfma_f32_16x16x32_f16 v[208:211], v[64:67], v[112:115], v[208:211]\n"
      "ds_write_b64 v230, v[236:237] offset:0\n"
      "v_cvt_pk_bf16_f32 v238, v20, v21\n"
      "v_fma_f32 v24, v200, v10, v24\n"
      "v_fma_f32 v25, v201, v10, v25\n"
      "v_mfma_f32_16x16x32_f16 v[208:211], v[68:71], v[108:111], v[208:211]\n"
      "v_cvt_pk_bf16_f32 v239, v22, v23\n"
      "v_fma_f32 v26, v202, v10, v26\n"
      "v_mfma_f32_16x16x32_f16 v[208:211], v[72:75], v[104:107], v[208:211]\n"
      "buffer_load_dwordx4 a[92:95], v243, s[32:35], s67 offen offset:1024 sc1\n"
      "ds_write_b64 v230, v[238:239] offset:2048\n"
      "v_fma_f32 v27, v203, v10, v27\n"
      "v_mfma_f32_16x16x32_f16 v[208:211], v[76:79], v[100:103], v[208:211]\n"
      "v_cvt_pk_bf16_f32 v236, v24, v25\n"
      "v_mfma_f32_16x16x32_f16 v[208:211], v[80:83], v[96:99], v[208:211]\n"
      "v_cvt_pk_bf16_f32 v237, v26, v27\n"
      "v_fma_f32 v28, v204, v10, v28\n"
      "v_fma_f32 v29, v205, v10, v29\n"
      "s_waitcnt lgkmcnt(8)\n"
      "v_mfma_f32_16x16x32_f16 v[212:215], v[64:67], v[116:119], v[212:215]\n"
      "ds_write_b64 v230, v[236:237] offset:4096\n"
      "v_fma_f32 v30, v206, v10, v30\n"
      "v_mfma_f32_16x16x32_f16 v[212:215], v[68:71], v[112:115], v[212:215]\n"
      "v_fma_f32 v31, v207, v10, v31\n"
      "v_mfma_f32_16x16x32_f16 v[212:215], v[72:75], v[108:111], v[212:215]\n"
      "v_cvt_pk_bf16_f32 v238, v28, v29\n"
      "v_mfma_f32_16x16x32_f16 v[212:215], v[76:79], v[104:107], v[212:215]\n"
      "v_cvt_pk_bf16_f32 v239, v30, v31\n"
      "v_mfma_f32_16x16x32_f16 v[212:215], v[80:83], v[100:103], v[212:215]\n"
      "ds_write_b64 v230, v[238:239] offset:6144\n"
      "v_fma_f32 v32, v208, v10, v32\n"
      "v_fma_f32 v33, v209, v10, v33\n"
      "v_mfma_f32_16x16x32_f16 v[212:215], v[84:87], v[96:99], v[212:215]\n"
      "v_fma_f32 v34, v210, v10, v34\n"
      "s_waitcnt lgkmcnt(7)\n"
      "v_mfma_f32_16x16x32_f16 v[216:219], v[64:67], v[120:123], v[216:219]\n"
      "v_fma_f32 v35, v211, v10, v35\n"
      "v_mfma_f32_16x16x32_f16 v[216:219], v[68:71], v[116:119], v[216:219]\n"
      "v_cvt_pk_bf16_f32 v236, v32, v33\n"
      "v_mfma_f32_16x16x32_f16 v[216:219], v[72:75], v[112:115], v[216:219]\n"
      "v_cvt_pk_bf16_f32 v237, v34, v35\n"
      "v_mfma_f32_16x16x32_f16 v[216:219], v[76:79], v[108:111], v[216:219]\n"
      "ds_write_b64 v230, v[236:237] offset:8192\n"
      "v_mfma_f32_16x16x32_f16 v[216:219], v[80:83], v[104:107], v[216:219]\n"
      "v_fma_f32 v36, v212, v10, v36\n"
      "v_fma_f32 v37, v213, v10, v37\n"
      "v_mfma_f32_16x16x32_f16 v[216:219], v[84:87], v[100:103], v[216:219]\n"
      "v_fma_f32 v38, v214, v10, v38\n"
      "v_mfma_f32_16x16x32_f16 v[216:219], v[88:91], v[96:99], v[216:219]\n"
      "v_fma_f32 v39, v215, v10, v39\n"
      "s_waitcnt lgkmcnt(5)\n"
      "v_mfma_f32_16x16x32_f16 v[220:223], v[64:67], v[124:127], v[220:223]\n"
      "v_cvt_pk_bf16_f32 v238, v36, v37\n"
      "v_mfma_f32_16x16x32_f16 v[220:223], v[68:71], v[120:123], v[220:223]\n"
      "v_cvt_pk_bf16_f32 v239, v38, v39\n"
      "v_mfma_f32_16x16x32_f16 v[220:223], v[72:75], v[116:119], v[220:223]\n"
      "ds_write_b64 v230, v[238:239] offset:10240\n"
      "v_mfma_f32_16x16x32_f16 v[220:223], v[76:79], v[112:115], v[220:223]\n"
      "v_mfma_f32_16x16x32_f16 v[220:223], v[80:83], v[108:111], v[220:223]\n"
      "v_fma_f32 v40, v216, v10, v40\n"
      "v_fma_f32 v41, v217, v10, v41\n"
      "v_fma_f32 v42, v218, v10, v42\n"
      "v_mfma_f32_16x16x32_f16 v[220:223], v[84:87], v[104:107], v[220:223]\n"
      "v_fma_f32 v43, v219, v10, v43\n"
      "v_cvt_pk_bf16_f32 v236, v40, v41\n"
      "v_mfma_f32_16x16x32_f16 v[220:223], v[88:91], v[100:103], v[220:223]\n"
      "v_cvt_pk_bf16_f32 v237, v42, v43\n"
      "ds_write_b64 v230, v[236:237] offset:12288\n"
      "v_mfma_f32_16x16x32_f16 v[220:223], v[92:95], v[96:99], v[220:223]\n"
      "s_nop 7\n"
      "s_nop 0\n"
      "v_fma_f32 v44, v220, v10, v44\n"
      "v_fma_f32 v45, v221, v10, v45\n"
      "v_fma_f32 v46, v222, v10, v46\n"
      "v_fma_f32 v47, v223, v10, v47\n"
      "v_cvt_pk_bf16_f32 v238, v44, v45\n"
      "v_cvt_pk_bf16_f32 v239, v46, v47\n"
      "ds_write_b64 v230, v[238:239] offset:14336\n"
      "s_waitcnt lgkmcnt(0)\n"
      "s_barrier\n"
      "ds_read_b64_tr_b16 v[128:129], v231 offset:0\n"
      "ds_read_b64_tr_b16 v[130:131], v232 offset:0\n"
      "ds_read_b64_tr_b16 v[132:133], v233 offset:0\n"
      "ds_read_b64_tr_b16 v[134:135], v234 offset:0\n"
      "ds_read_b64_tr_b16 v[136:137], v231 offset:2048\n"
      "ds_read_b64_tr_b16 v[138:139], v232 offset:2048\n"
      "ds_read_b64_tr_b16 v[140:141], v233 offset:2048\n"
      "ds_read_b64_tr_b16 v[142:143], v234 offset:2048\n"
      "s_waitcnt lgkmcnt(6)\n"
      "buffer_store_dwordx4 v[128:131], v235, s[32:35], s86 offen sc1\n"
      "s_add_u32 s86, s86, 20480\n"
      "s_waitcnt lgkmcnt(4)\n"
      "buffer_store_dwordx4 v[132:135], v235, s[32:35], s86 offen sc1\n"
      "s_add_u32 s86, s86, 20480\n"
      "s_waitcnt lgkmcnt(2)\n"
      "buffer_store_dwordx4 v[136:139], v235, s[32:35], s86 offen sc1\n"
      "s_add_u32 s86, s86, 20480\n"
      "s_waitcnt lgkmcnt(0)\n"
      "buffer_store_dwordx4 v[140:143], v235, s[32:35], s86 offen sc1\n"
      "s_mov_b64 s[92:93], 1\n"
      "s_branch K2_JOIN2\n"
      "K2_NONCONV:\n"
      "s_cmp_ge_u32 s30, 224\n"
      "s_cbranch_scc1 K2_W2T\n"
      "s_sub_u32 s52, s30, 160\n"
      "s_and_b32 s53, s52, 7\n"
      "s_lshr_b32 s52, s52, 3\n"
      "s_lshl_b32 s53, s53, 5\n"
      "s_lshl2_add_u32 s53, s52, s53\n"
      "s_add_u32 s80, s53, s31\n"
      "s_mov_b32 s36, s4\n"
      "s_and_b32 s37, s5, 0xffff\n"
      "s_mov_b32 s38, 2621440\n"
      "s_mov_b32 s39, 0x20000\n"
      "s_mul_i32 s52, s80, 10240\n"
      "v_lshrrev_b32 v20, 3, v1\n"
      "v_and_b32_e64 v21, 7, v1\n"
      "v_mul_u32_u24 v22, 1280, v20\n"
      "v_lshl_add_u32 v22, v21, 4, v22\n"
      "buffer_load_dwordx4 v[24:27], v22, s[36:39], s52 offen offset:0\n"
      "buffer_load_dwordx4 v[28:31], v22, s[36:39], s52 offen offset:128\n"
      "buffer_load_dwordx4 v[32:35], v22, s[36:39], s52 offen offset:256\n"
      "buffer_load_dwordx4 v[36:39], v22, s[36:39], s52 offen offset:384\n"
      "buffer_load_dwordx4 v[40:43], v22, s[36:39], s52 offen offset:512\n"
      "buffer_load_dwordx4 v[44:47], v22, s[36:39], s52 offen offset:640\n"
      "buffer_load_dwordx4 v[48:51], v22, s[36:39], s52 offen offset:768\n"
      "buffer_load_dwordx4 v[52:55], v22, s[36:39], s52 offen offset:896\n"
      "buffer_load_dwordx4 v[56:59], v22, s[36:39], s52 offen offset:1024\n"
      "buffer_load_dwordx4 v[60:63], v22, s[36:39], s52 offen offset:1152\n"
      "s_mov_b32 s52, K2_END-K2_PC0-4\n"
      "s_lshl_b32 s53, s31, 13\n"
      "v_lshl_add_u32 v16, v1, 7, s53\n"
      "v_min_u32 v16, s52, v16\n"
      "v_and_b32 v16, -4, v16\n"
      "global_load_dword v18, v16, s[98:99]\n"
      "s_lshl_b32 s53, s31, 13\n"
      "s_add_u32 s53, s53, 32768\n"
      "v_lshl_add_u32 v17, v1, 7, s53\n"
      "v_min_u32 v17, s52, v17\n"
      "v_and_b32 v17, -4, v17\n"
      "global_load_dword v19, v17, s[98:99]\n"
      "s_mul_i32 s52, s60, 192\n"
      "s_add_u32 s40, s18, s52\n"
      "s_addc_u32 s41, s19, 0\n"
      "s_and_b32 s41, s41, 0xffff\n"
      "s_mov_b32 s42, 1966080\n"
      "s_mov_b32 s43, 0x20000\n"
      "s_sub_u32 s42, s42, s52\n"
      "s_mul_i32 s50, s31, 49152\n"
      "s_mul_i32 s51, s31, 3072\n"
      "v_lshrrev_b32 v23, 2, v21\n"
      "v_lshlrev_b32 v23, 10, v23\n"
      "v_and_b32 v240, 3, v21\n"
      "v_lshl_add_u32 v23, v240, 8, v23\n"
      "v_lshl_add_u32 v23, v20, 4, v23\n"
      "s_lshr_b32 s53, s80, 1\n"
      "s_mul_i32 s53, s53, 20480\n"
      "s_and_b32 s54, s80, 1\n"
      "s_lshl_b32 s54, s54, 7\n"
      "s_add_u32 s53, s53, s54\n"
      "s_add_u32 s82, s53, 1048576\n"
      "s_add_u32 s83, s82, 4096\n"
      "s_add_u32 s84, s82, 8192\n"
      "s_add_u32 s85, s82, 12288\n"
      "s_add_u32 s86, s82, 16384\n"
      "s_waitcnt vmcnt(11)\n"
      "v_lshlrev_b32 v64, 16, v24\n"
      "v_and_b32 v65, s63, v24\n"
      "v_lshlrev_b32 v66, 16, v25\n"
      "v_and_b32 v67, s63, v25\n"
      "v_lshlrev_b32 v68, 16, v26\n"
      "v_and_b32 v69, s63, v26\n"
      "v_lshlrev_b32 v70, 16, v27\n"
      "v_and_b32 v71, s63, v27\n"
      "s_waitcnt vmcnt(10)\n"
      "v_lshlrev_b32 v72, 16, v28\n"
      "v_and_b32 v73, s63, v28\n"
      "v_lshlrev_b32 v74, 16, v29\n"
      "v_and_b32 v75, s63, v29\n"
      "v_lshlrev_b32 v76, 16, v30\n"
      "v_and_b32 v77, s63, v30\n"
      "v_lshlrev_b32 v78, 16, v31\n"
      "v_and_b32 v79, s63, v31\n"
      "v_mov_b32 v224, v64\n"
      "v_mov_b32 v225, v65\n"
      "v_mov_b32 v226, v66\n"
      "v_mov_b32 v227, v67\n"
      "v_mov_b32 v228, v68\n"
      "v_mov_b32 v229, v69\n"
      "v_mov_b32 v230, v70\n"
      "v_mov_b32 v231, v71\n"
      "s_waitcnt vmcnt(9)\n"
      "v_lshlrev_b32 v80, 16, v32\n"
      "v_and_b32 v81, s63, v32\n"
      "v_lshlrev_b32 v82, 16, v33\n"
      "v_and_b32 v83, s63, v33\n"
      "v_lshlrev_b32 v84, 16, v34\n"
      "v_and_b32 v85, s63, v34\n"
      "v_lshlrev_b32 v86, 16, v35\n"
      "v_and_b32_e64 v87, s63, v35\n"
      "v_pk_add_f32 v[224:225], v[224:225], v[72:73]\n"
      "v_pk_add_f32 v[226:227], v[226:227], v[74:75]\n"
      "v_pk_add_f32 v[228:229], v[228:229], v[76:77]\n"
      "v_pk_add_f32 v[230:231], v[230:231], v[78:79]\n"
      "s_waitcnt vmcnt(8)\n"
      "v_lshlrev_b32 v88, 16, v36\n"
      "v_and_b32 v89, s63, v36\n"
      "v_lshlrev_b32 v90, 16, v37\n"
      "v_and_b32 v91, s63, v37\n"
      "v_lshlrev_b32 v92, 16, v38\n"
      "v_and_b32 v93, s63, v38\n"
      "v_lshlrev_b32 v94, 16, v39\n"
      "v_and_b32_e64 v95, s63, v39\n"
      "v_pk_add_f32 v[224:225], v[224:225], v[80:81]\n"
      "v_pk_add_f32 v[226:227], v[226:227], v[82:83]\n"
      "v_pk_add_f32 v[228:229], v[228:229], v[84:85]\n"
      "v_pk_add_f32 v[230:231], v[230:231], v[86:87]\n"
      "s_waitcnt vmcnt(7)\n"
      "v_lshlrev_b32 v96, 16, v40\n"
      "v_and_b32 v97, s63, v40\n"
      "v_lshlrev_b32 v98, 16, v41\n"
      "v_and_b32 v99, s63, v41\n"
      "v_lshlrev_b32 v100, 16, v42\n"
      "v_and_b32 v101, s63, v42\n"
      "v_lshlrev_b32 v102, 16, v43\n"
      "v_and_b32_e64 v103, s63, v43\n"
      "v_pk_add_f32 v[224:225], v[224:225], v[88:89]\n"
      "v_pk_add_f32 v[226:227], v[226:227], v[90:91]\n"
      "v_pk_add_f32 v[228:229], v[228:229], v[92:93]\n"
      "v_pk_add_f32 v[230:231], v[230:231], v[94:95]\n"
      "s_waitcnt vmcnt(6)\n"
      "v_lshlrev_b32 v104, 16, v44\n"
      "v_and_b32 v105, s63, v44\n"
      "v_lshlrev_b32 v106, 16, v45\n"
      "v_and_b32 v107, s63, v45\n"
      "v_lshlrev_b32 v108, 16, v46\n"
      "v_and_b32 v109, s63, v46\n"
      "v_lshlrev_b32 v110, 16, v47\n"
      "v_and_b32_e64 v111, s63, v47\n"
      "v_pk_add_f32 v[224:225], v[224:225], v[96:97]\n"
      "v_pk_add_f32 v[226:227], v[226:227], v[98:99]\n"
      "v_pk_add_f32 v[228:229], v[228:229], v[100:101]\n"
      "v_pk_add_f32 v[230:231], v[230:231], v[102:103]\n"
      "s_waitcnt vmcnt(5)\n"
      "v_lshlrev_b32 v112, 16, v48\n"
      "v_and_b32 v113, s63, v48\n"
      "v_lshlrev_b32 v114, 16, v49\n"
      "v_and_b32 v115, s63, v49\n"
      "v_lshlrev_b32 v116, 16, v50\n"
      "v_and_b32 v117, s63, v50\n"
      "v_lshlrev_b32 v118, 16, v51\n"
      "v_and_b32_e64 v119, s63, v51\n"
      "v_pk_add_f32 v[224:225], v[224:225], v[104:105]\n"
      "v_pk_add_f32 v[226:227], v[226:227], v[106:107]\n"
      "v_pk_add_f32 v[228:229], v[228:229], v[108:109]\n"
      "v_pk_add_f32 v[230:231], v[230:231], v[110:111]\n"
      "s_waitcnt vmcnt(4)\n"
      "v_lshlrev_b32 v120, 16, v52\n"
      "v_and_b32 v121, s63, v52\n"
      "v_lshlrev_b32 v122, 16, v53\n"
      "v_and_b32 v123, s63, v53\n"
      "v_lshlrev_b32 v124, 16, v54\n"
      "v_and_b32 v125, s63, v54\n"
      "v_lshlrev_b32 v126, 16, v55\n"
      "v_and_b32_e64 v127, s63, v55\n"
      "v_pk_add_f32 v[224:225], v[224:225], v[112:113]\n"
      "v_pk_add_f32 v[226:227], v[226:227], v[114:115]\n"
      "v_pk_add_f32 v[228:229], v[228:229], v[116:117]\n"
      "v_pk_add_f32 v[230:231], v[230:231], v[118:119]\n"
      "s_waitcnt vmcnt(3)\n"
      "v_lshlrev_b32 v128, 16, v56\n"
      "v_and_b32 v129, s63, v56\n"
      "v_lshlrev_b32 v130, 16, v57\n"
      "v_and_b32 v131, s63, v57\n"
      "v_lshlrev_b32 v132, 16, v58\n"
      "v_and_b32 v133, s63, v58\n"
      "v_lshlrev_b32 v134, 16, v59\n"
      "v_and_b32_e64 v135, s63, v59\n"
      "v_pk_add_f32 v[224:225], v[224:225], v[120:121]\n"
      "v_pk_add_f32 v[226:227], v[226:227], v[122:123]\n"
      "v_pk_add_f32 v[228:229], v[228:229], v[124:125]\n"
      "v_pk_add_f32 v[230:231], v[230:231], v[126:127]\n"
      "s_waitcnt vmcnt(2)\n"
      "v_lshlrev_b32 v136, 16, v60\n"
      "v_and_b32 v137, s63, v60\n"
      "v_lshlrev_b32 v138, 16, v61\n"
      "v_and_b32 v139, s63, v61\n"
      "v_lshlrev_b32 v140, 16, v62\n"
      "v_and_b32 v141, s63, v62\n"
      "v_lshlrev_b32 v142, 16, v63\n"
      "v_and_b32_e64 v143, s63, v63\n"
      "v_pk_add_f32 v[224:225], v[224:225], v[128:129]\n"
      "v_pk_add_f32 v[226:227], v[226:227], v[130:131]\n"
      "v_pk_add_f32 v[228:229], v[228:229], v[132:133]\n"
      "v_pk_add_f32 v[230:231], v[230:231], v[134:135]\n"
      "v_pk_add_f32 v[224:225], v[224:225], v[136:137]\n"
      "v_pk_add_f32 v[226:227], v[226:227], v[138:139]\n"
      "v_pk_add_f32 v[228:229], v[228:229], v[140:141]\n"
      "v_pk_add_f32 v[230:231], v[230:231], v[142:143]\n"
      "v_pk_add_f32 v[224:225], v[224:225], v[226:227]\n"
      "v_pk_add_f32 v[228:229], v[228:229], v[230:231]\n"
      "v_pk_add_f32 v[224:225], v[224:225], v[228:229]\n"
      "v_add_f32 v232, v224, v225\n"
      "s_nop 1\n"
      "v_add_f32_dpp v232, v232, v232 quad_perm:[1,0,3,2] row_mask:0xf bank_mask:0xf bound_ctrl:1\n"
      "s_nop 1\n"
      "v_add_f32_dpp v232, v232, v232 quad_perm:[2,3,0,1] row_mask:0xf bank_mask:0xf bound_ctrl:1\n"
      "s_nop 1\n"
      "v_add_f32_dpp v232, v232, v232 row_half_mirror row_mask:0xf bank_mask:0xf bound_ctrl:1\n"
      "v_mul_f32 v232, 0x3acccccd, v232\n"
      "v_pk_add_f32 v[64:65], v[64:65], v[232:233] op_sel_hi:[1,0] neg_lo:[0,1] neg_hi:[0,1]\n"
      "v_pk_add_f32 v[66:67], v[66:67], v[232:233] op_sel_hi:[1,0] neg_lo:[0,1] neg_hi:[0,1]\n"
      "v_pk_add_f32 v[68:69], v[68:69], v[232:233] op_sel_hi:[1,0] neg_lo:[0,1] neg_hi:[0,1]\n"
      "v_pk_add_f32 v[70:71], v[70:71], v[232:233] op_sel_hi:[1,0] neg_lo:[0,1] neg_hi:[0,1]\n"
      "v_pk_add_f32 v[72:73], v[72:73], v[232:233] op_sel_hi:[1,0] neg_lo:[0,1] neg_hi:[0,1]\n"
      "v_pk_add_f32 v[74:75], v[74:75], v[232:233] op_sel_hi:[1,0] neg_lo:[0,1] neg_hi:[0,1]\n"
      "v_pk_add_f32 v[76:77], v[76:77], v[232:233] op_sel_hi:[1,0] neg_lo:[0,1] neg_hi:[0,1]\n"
      "v_pk_add_f32 v[78:79], v[78:79], v[232:233] op_sel_hi:[1,0] neg_lo:[0,1] neg_hi:[0,1]\n"
      "v_pk_mul_f32 v[224:225], v[64:65], v[64:65]\n"
      "v_pk_mul_f32 v[226:227], v[66:67], v[66:67]\n"
      "v_pk_mul_f32 v[228:229], v[68:69], v[68:69]\n"
      "v_pk_mul_f32 v[230:231], v[70:71], v[70:71]\n"
      "v_pk_add_f32 v[80:81], v[80:81], v[232:233] op_sel_hi:[1,0] neg_lo:[0,1] neg_hi:[0,1]\n"
      "v_pk_add_f32 v[82:83], v[82:83], v[232:233] op_sel_hi:[1,0] neg_lo:[0,1] neg_hi:[0,1]\n"
      "v_pk_add_f32 v[84:85], v[84:85], v[232:233] op_sel_hi:[1,0] neg_lo:[0,1] neg_hi:[0,1]\n"
      "v_pk_add_f32 v[86:87], v[86:87], v[232:233] op_sel_hi:[1,0] neg_lo:[0,1] neg_hi:[0,1]\n"
      "v_pk_fma_f32 v[224:225], v[72:73], v[72:73], v[224:225]\n"
      "v_pk_fma_f32 v[226:227], v[74:75], v[74:75], v[226:227]\n"
      "v_pk_fma_f32 v[228:229], v[76:77], v[76:77], v[228:229]\n"
      "v_pk_fma_f32 v[230:231], v[78:79], v[78:79], v[230:231]\n"
      "v_pk_add_f32 v[88:89], v[88:89], v[232:233] op_sel_hi:[1,0] neg_lo:[0,1] neg_hi:[0,1]\n"
      "v_pk_add_f32 v[90:91], v[90:91], v[232:233] op_sel_hi:[1,0] neg_lo:[0,1] neg_hi:[0,1]\n"
      "v_pk_add_f32 v[92:93], v[92:93], v[232:233] op_sel_hi:[1,0] neg_lo:[0,1] neg_hi:[0,1]\n"
      "v_pk_add_f32 v[94:95], v[94:95], v[232:233] op_sel_hi:[1,0] neg_lo:[0,1] neg_hi:[0,1]\n"
      "v_pk_fma_f32 v[224:225], v[80:81], v[80:81], v[224:225]\n"
      "v_pk_fma_f32 v[226:227], v[82:83], v[82:83], v[226:227]\n"
      "v_pk_fma_f32 v[228:229], v[84:85], v[84:85], v[228:229]\n"
      "v_pk_fma_f32 v[230:231], v[86:87], v[86:87], v[230:231]\n"
      "v_pk_add_f32 v[96:97], v[96:97], v[232:233] op_sel_hi:[1,0] neg_lo:[0,1] neg_hi:[0,1]\n"
      "v_pk_add_f32 v[98:99], v[98:99], v[232:233] op_sel_hi:[1,0] neg_lo:[0,1] neg_hi:[0,1]\n"
      "v_pk_add_f32 v[100:101], v[100:101], v[232:233] op_sel_hi:[1,0] neg_lo:[0,1] neg_hi:[0,1]\n"
      "v_pk_add_f32 v[102:103], v[102:103], v[232:233] op_sel_hi:[1,0] neg_lo:[0,1] neg_hi:[0,1]\n"
      "v_pk_fma_f32 v[224:225], v[88:89], v[88:89], v[224:225]\n"
      "v_pk_fma_f32 v[226:227], v[90:91], v[90:91], v[226:227]\n"
      "v_pk_fma_f32 v[228:229], v[92:93], v[92:93], v[228:229]\n"
      "v_pk_fma_f32 v[230:231], v[94:95], v[94:95], v[230:231]\n"
      "v_pk_add_f32 v[104:105], v[104:105], v[232:233] op_sel_hi:[1,0] neg_lo:[0,1] neg_hi:[0,1]\n"
      "v_pk_add_f32 v[106:107], v[106:107], v[232:233] op_sel_hi:[1,0] neg_lo:[0,1] neg_hi:[0,1]\n"
      "v_pk_add_f32 v[108:109], v[108:109], v[232:233] op_sel_hi:[1,0] neg_lo:[0,1] neg_hi:[0,1]\n"
      "v_pk_add_f32 v[110:111], v[110:111], v[232:233] op_sel_hi:[1,0] neg_lo:[0,1] neg_hi:[0,1]\n"
      "v_pk_fma_f32 v[224:225], v[96:97], v[96:97], v[224:225]\n"
      "v_pk_fma_f32 v[226:227], v[98:99], v[98:99], v[226:227]\n"
      "v_pk_fma_f32 v[228:229], v[100:101], v[100:101], v[228:229]\n"
      "v_pk_fma_f32 v[230:231], v[102:103], v[102:103], v[230:231]\n"
      "v_pk_add_f32 v[112:113], v[112:113], v[232:233] op_sel_hi:[1,0] neg_lo:[0,1] neg_hi:[0,1]\n"
      "v_pk_add_f32 v[114:115], v[114:115], v[232:233] op_sel_hi:[1,0] neg_lo:[0,1] neg_hi:[0,1]\n"
      "v_pk_add_f32 v[116:117], v[116:117], v[232:233] op_sel_hi:[1,0] neg_lo:[0,1] neg_hi:[0,1]\n"
      "v_pk_add_f32 v[118:119], v[118:119], v[232:233] op_sel_hi:[1,0] neg_lo:[0,1] neg_hi:[0,1]\n"
      "v_pk_fma_f32 v[224:225], v[104:105], v[104:105], v[224:225]\n"
      "v_pk_fma_f32 v[226:227], v[106:107], v[106:107], v[226:227]\n"
      "v_pk_fma_f32 v[228:229], v[108:109], v[108:109], v[228:229]\n"
      "v_pk_fma_f32 v[230:231], v[110:111], v[110:111], v[230:231]\n"
      "v_pk_add_f32 v[120:121], v[120:121], v[232:233] op_sel_hi:[1,0] neg_lo:[0,1] neg_hi:[0,1]\n"
      "v_pk_add_f32 v[122:123], v[122:123], v[232:233] op_sel_hi:[1,0] neg_lo:[0,1] neg_hi:[0,1]\n"
      "v_pk_add_f32 v[124:125], v[124:125], v[232:233] op_sel_hi:[1,0] neg_lo:[0,1] neg_hi:[0,1]\n"
      "v_pk_add_f32 v[126:127], v[126:127], v[232:233] op_sel_hi:[1,0] neg_lo:[0,1] neg_hi:[0,1]\n"
      "v_pk_fma_f32 v[224:225], v[112:113], v[112:113], v[224:225]\n"
      "v_pk_fma_f32 v[226:227], v[114:115], v[114:115], v[226:227]\n"
      "v_pk_fma_f32 v[228:229], v[116:117], v[116:117], v[228:229]\n"
      "v_pk_fma_f32 v[230:231], v[118:119], v[118:119], v[230:231]\n"
      "v_pk_add_f32 v[128:129], v[128:129], v[232:233] op_sel_hi:[1,0] neg_lo:[0,1] neg_hi:[0,1]\n"
      "v_pk_add_f32 v[130:131], v[130:131], v[232:233] op_sel_hi:[1,0] neg_lo:[0,1] neg_hi:[0,1]\n"
      "v_pk_add_f32 v[132:133], v[132:133], v[232:233] op_sel_hi:[1,0] neg_lo:[0,1] neg_hi:[0,1]\n"
      "v_pk_add_f32 v[134:135], v[134:135], v[232:233] op_sel_hi:[1,0] neg_lo:[0,1] neg_hi:[0,1]\n"
      "v_pk_fma_f32 v[224:225], v[120:121], v[120:121], v[224:225]\n"
      "v_pk_fma_f32 v[226:227], v[122:123], v[122:123], v[226:227]\n"
      "v_pk_fma_f32 v[228:229], v[124:125], v[124:125], v[228:229]\n"
      "v_pk_fma_f32 v[230:231], v[126:127], v[126:127], v[230:231]\n"
      "v_pk_add_f32 v[136:137], v[136:137], v[232:233] op_sel_hi:[1,0] neg_lo:[0,1] neg_hi:[0,1]\n"
      "v_pk_add_f32 v[138:139], v[138:139], v[232:233] op_sel_hi:[1,0] neg_lo:[0,1] neg_hi:[0,1]\n"
      "v_pk_add_f32 v[140:141], v[140:141], v[232:233] op_sel_hi:[1,0] neg_lo:[0,1] neg_hi:[0,1]\n"
      "v_pk_add_f32 v[142:143], v[142:143], v[232:233] op_sel_hi:[1,0] neg_lo:[0,1] neg_hi:[0,1]\n"
      "v_pk_fma_f32 v[224:225], v[128:129], v[128:129], v[224:225]\n"
      "v_pk_fma_f32 v[226:227], v[130:131], v[130:131], v[226:227]\n"
      "v_pk_fma_f32 v[228:229], v[132:133], v[132:133], v[228:229]\n"
      "v_pk_fma_f32 v[230:231], v[134:135], v[134:135], v[230:231]\n"
      "v_pk_fma_f32 v[224:225], v[136:137], v[136:137], v[224:225]\n"
      "v_pk_fma_f32 v[226:227], v[138:139], v[138:139], v[226:227]\n"
      "v_pk_fma_f32 v[228:229], v[140:141], v[140:141], v[228:229]\n"
      "v_pk_fma_f32 v[230:231], v[142:143], v[142:143], v[230:231]\n"
      "v_pk_add_f32 v[224:225], v[224:225], v[226:227]\n"
      "v_pk_add_f32 v[228:229], v[228:229], v[230:231]\n"
      "v_cvt_pk_bf16_f32 v236, v232, v232\n"
      "v_pk_add_f32 v[224:225], v[224:225], v[228:229]\n"
      "v_and_b32 v236, s63, v236\n"
      "v_add_f32 v238, v224, v225\n"
      "v_sub_f32 v236, v232, v236\n"
      "s_nop 0\n"
      "v_add_f32_dpp v238, v238, v238 quad_perm:[1,0,3,2] row_mask:0xf bank_mask:0xf bound_ctrl:1\n"
      "s_nop 1\n"
      "v_add_f32_dpp v238, v238, v238 quad_perm:[2,3,0,1] row_mask:0xf bank_mask:0xf bound_ctrl:1\n"
      "s_nop 1\n"
      "v_add_f32_dpp v238, v238, v238 row_half_mirror row_mask:0xf bank_mask:0xf bound_ctrl:1\n"
      "v_mul_f32 v238, 0x3acccccd, v238\n"
      "v_cvt_pk_bf16_f32 v238, v238, v238\n"
      "v_and_b32 v238, s63, v238\n"
      "v_add_f32 v238, 0x3727c5ac, v238\n"
      "v_cvt_pk_bf16_f32 v238, v238, v238\n"
      "v_and_b32 v238, s63, v238\n"
      "v_sqrt_f32 v238, v238\n"
      "s_nop 0\n"
      "v_cvt_pk_bf16_f32 v238, v238, v238\n"
      "v_and_b32 v238, s63, v238\n"
      "v_rcp_f32 v234, v238\n"
      "s_nop 0\n"
      "v_mul_f32 v236, v236, v234\n"
      "v_pk_fma_f32 v[64:65], v[64:65], v[234:235], v[236:237] op_sel_hi:[1,0,0]\n"
      "v_pk_fma_f32 v[66:67], v[66:67], v[234:235], v[236:237] op_sel_hi:[1,0,0]\n"
      "v_pk_fma_f32 v[68:69], v[68:69], v[234:235], v[236:237] op_sel_hi:[1,0,0]\n"
      "v_pk_fma_f32 v[70:71], v[70:71], v[234:235], v[236:237] op_sel_hi:[1,0,0]\n"
      "v_pk_fma_f32 v[72:73], v[72:73], v[234:235], v[236:237] op_sel_hi:[1,0,0]\n"
      "v_pk_fma_f32 v[74:75], v[74:75], v[234:235], v[236:237] op_sel_hi:[1,0,0]\n"
      "v_pk_fma_f32 v[76:77], v[76:77], v[234:235], v[236:237] op_sel_hi:[1,0,0]\n"
      "v_pk_fma_f32 v[78:79], v[78:79], v[234:235], v[236:237] op_sel_hi:[1,0,0]\n"
      "v_cvt_pk_bf16_f32 v24, v64, v65\n"
      "v_cvt_pk_bf16_f32 v25, v66, v67\n"
      "v_cvt_pk_bf16_f32 v26, v68, v69\n"
      "v_cvt_pk_bf16_f32 v27, v70, v71\n"
      "v_pk_fma_f32 v[80:81], v[80:81], v[234:235], v[236:237] op_sel_hi:[1,0,0]\n"
      "v_pk_fma_f32 v[82:83], v[82:83], v[234:235], v[236:237] op_sel_hi:[1,0,0]\n"
      "v_pk_fma_f32 v[84:85], v[84:85], v[234:235], v[236:237] op_sel_hi:[1,0,0]\n"
      "v_pk_fma_f32 v[86:87], v[86:87], v[234:235], v[236:237] op_sel_hi:[1,0,0]\n"
      "v_cvt_pk_bf16_f32 v28, v72, v73\n"
      "v_cvt_pk_bf16_f32 v29, v74, v75\n"
      "v_cvt_pk_bf16_f32 v30, v76, v77\n"
      "v_cvt_pk_bf16_f32 v31, v78, v79\n"
      "v_pk_fma_f32 v[88:89], v[88:89], v[234:235], v[236:237] op_sel_hi:[1,0,0]\n"
      "v_pk_fma_f32 v[90:91], v[90:91], v[234:235], v[236:237] op_sel_hi:[1,0,0]\n"
      "v_pk_fma_f32 v[92:93], v[92:93], v[234:235], v[236:237] op_sel_hi:[1,0,0]\n"
      "v_pk_fma_f32 v[94:95], v[94:95], v[234:235], v[236:237] op_sel_hi:[1,0,0]\n"
      "buffer_store_dwordx4 v[24:27], v23, s[32:35], s82 offen offset:0 sc1\n"
      "v_cvt_pk_bf16_f32 v32, v80, v81\n"
      "v_cvt_pk_bf16_f32 v33, v82, v83\n"
      "v_cvt_pk_bf16_f32 v34, v84, v85\n"
      "v_cvt_pk_bf16_f32 v35, v86, v87\n"
      "v_pk_fma_f32 v[96:97], v[96:97], v[234:235], v[236:237] op_sel_hi:[1,0,0]\n"
      "v_pk_fma_f32 v[98:99], v[98:99], v[234:235], v[236:237] op_sel_hi:[1,0,0]\n"
      "v_pk_fma_f32 v[100:101], v[100:101], v[234:235], v[236:237] op_sel_hi:[1,0,0]\n"
      "v_pk_fma_f32 v[102:103], v[102:103], v[234:235], v[236:237] op_sel_hi:[1,0,0]\n"
      "buffer_store_dwordx4 v[28:31], v23, s[32:35], s82 offen offset:2048 sc1\n"
      "v_cvt_pk_bf16_f32 v36, v88, v89\n"
      "v_cvt_pk_bf16_f32 v37, v90, v91\n"
      "v_cvt_pk_bf16_f32 v38, v92, v93\n"
      "v_cvt_pk_bf16_f32 v39, v94, v95\n"
      "v_pk_fma_f32 v[104:105], v[104:105], v[234:235], v[236:237] op_sel_hi:[1,0,0]\n"
      "v_pk_fma_f32 v[106:107], v[106:107], v[234:235], v[236:237] op_sel_hi:[1,0,0]\n"
      "v_pk_fma_f32 v[108:109], v[108:109], v[234:235], v[236:237] op_sel_hi:[1,0,0]\n"
      "v_pk_fma_f32 v[110:111], v[110:111], v[234:235], v[236:237] op_sel_hi:[1,0,0]\n"
      "buffer_store_dwordx4 v[32:35], v23, s[32:35], s83 offen offset:0 sc1\n"
      "v_cvt_pk_bf16_f32 v40, v96, v97\n"
      "v_cvt_pk_bf16_f32 v41, v98, v99\n"
      "v_cvt_pk_bf16_f32 v42, v100, v101\n"
      "v_cvt_pk_bf16_f32 v43, v102, v103\n"
      "v_pk_fma_f32 v[112:113], v[112:113], v[234:235], v[236:237] op_sel_hi:[1,0,0]\n"
      "v_pk_fma_f32 v[114:115], v[114:115], v[234:235], v[236:237] op_sel_hi:[1,0,0]\n"
      "v_pk_fma_f32 v[116:117], v[116:117], v[234:235], v[236:237] op_sel_hi:[1,0,0]\n"
      "v_pk_fma_f32 v[118:119], v[118:119], v[234:235], v[236:237] op_sel_hi:[1,0,0]\n"
      "buffer_store_dwordx4 v[36:39], v23, s[32:35], s83 offen offset:2048 sc1\n"
      "v_cvt_pk_bf16_f32 v44, v104, v105\n"
      "v_cvt_pk_bf16_f32 v45, v106, v107\n"
      "v_cvt_pk_bf16_f32 v46, v108, v109\n"
      "v_cvt_pk_bf16_f32 v47, v110, v111\n"
      "v_pk_fma_f32 v[120:121], v[120:121], v[234:235], v[236:237] op_sel_hi:[1,0,0]\n"
      "v_pk_fma_f32 v[122:123], v[122:123], v[234:235], v[236:237] op_sel_hi:[1,0,0]\n"
      "v_pk_fma_f32 v[124:125], v[124:125], v[234:235], v[236:237] op_sel_hi:[1,0,0]\n"
      "v_pk_fma_f32 v[126:127], v[126:127], v[234:235], v[236:237] op_sel_hi:[1,0,0]\n"
      "buffer_store_dwordx4 v[40:43], v23, s[32:35], s84 offen offset:0 sc1\n"
      "v_cvt_pk_bf16_f32 v48, v112, v113\n"
      "v_cvt_pk_bf16_f32 v49, v114, v115\n"
      "v_cvt_pk_bf16_f32 v50, v116, v117\n"
      "v_cvt_pk_bf16_f32 v51, v118, v119\n"
      "v_pk_fma_f32 v[128:129], v[128:129], v[234:235], v[236:237] op_sel_hi:[1,0,0]\n"
      "v_pk_fma_f32 v[130:131], v[130:131], v[234:235], v[236:237] op_sel_hi:[1,0,0]\n"
      "v_pk_fma_f32 v[132:133], v[132:133], v[234:235], v[236:237] op_sel_hi:[1,0,0]\n"
      "v_pk_fma_f32 v[134:135], v[134:135], v[234:235], v[236:237] op_sel_hi:[1,0,0]\n"
      "buffer_store_dwordx4 v[44:47], v23, s[32:35], s84 offen offset:2048 sc1\n"
      "v_cvt_pk_bf16_f32 v52, v120, v121\n"
      "v_cvt_pk_bf16_f32 v53, v122, v123\n"
      "v_cvt_pk_bf16_f32 v54, v124, v125\n"
      "v_cvt_pk_bf16_f32 v55, v126, v127\n"
      "v_pk_fma_f32 v[136:137], v[136:137], v[234:235], v[236:237] op_sel_hi:[1,0,0]\n"
      "v_pk_fma_f32 v[138:139], v[138:139], v[234:235], v[236:237] op_sel_hi:[1,0,0]\n"
      "v_pk_fma_f32 v[140:141], v[140:141], v[234:235], v[236:237] op_sel_hi:[1,0,0]\n"
      "v_pk_fma_f32 v[142:143], v[142:143], v[234:235], v[236:237] op_sel_hi:[1,0,0]\n"
      "buffer_store_dwordx4 v[48:51], v23, s[32:35], s85 offen offset:0 sc1\n"
      "v_cvt_pk_bf16_f32 v56, v128, v129\n"
      "v_cvt_pk_bf16_f32 v57, v130, v131\n"
      "v_cvt_pk_bf16_f32 v58, v132, v133\n"
      "v_cvt_pk_bf16_f32 v59, v134, v135\n"
      "buffer_store_dwordx4 v[52:55], v23, s[32:35], s85 offen offset:2048 sc1\n"
      "v_cvt_pk_bf16_f32 v60, v136, v137\n"
      "v_cvt_pk_bf16_f32 v61, v138, v139\n"
      "v_cvt_pk_bf16_f32 v62, v140, v141\n"
      "v_cvt_pk_bf16_f32 v63, v142, v143\n"
      "buffer_store_dwordx4 v[56:59], v23, s[32:35], s86 offen offset:0 sc1\n"
      "buffer_store_dwordx4 v[60:63], v23, s[32:35], s86 offen offset:2048 sc1\n"
      "s_and_b32 s52, s31, 1\n"
      "s_lshl_b32 s52, s52, 1\n"
      "v_and_b32 v240, 1, v20\n"
      "v_bfe_u32 v241, v20, 1, 1\n"
      "v_lshlrev_b32 v240, 2, v240\n"
      "v_lshlrev_b32 v242, 3, v241\n"
      "v_xor_b32 v240, v240, v21\n"
      "v_mul_u32_u24 v243, 1280, v20\n"
      "v_xor_b32 v240, s52, v240\n"
      "v_add_u32 v243, s81, v243\n"
      "v_add_u32 v242, v242, v240\n"
      "v_cvt_pk_f16_f32 v144, v64, v65\n"
      "v_lshl_add_u32 v241, v242, 4, v243\n"
      "v_xor_b32 v242, 8, v242\n"
      "v_cvt_pk_f16_f32 v145, v66, v67\n"
      "v_cvt_pk_f16_f32 v146, v68, v69\n"
      "v_cvt_pk_f16_f32 v147, v70, v71\n"
      "v_lshl_add_u32 v242, v242, 4, v243\n"
      "v_cvt_pk_f16_f32 v148, v72, v73\n"
      "v_cvt_pk_f16_f32 v149, v74, v75\n"
      "v_cvt_pk_f16_f32 v150, v76, v77\n"
      "v_cvt_pk_f16_f32 v151, v78, v79\n"
      "ds_write_b128 v241, v[144:147] offset:0\n"
      "v_cvt_pk_f16_f32 v152, v80, v81\n"
      "v_cvt_pk_f16_f32 v153, v82, v83\n"
      "v_cvt_pk_f16_f32 v154, v84, v85\n"
      "v_cvt_pk_f16_f32 v155, v86, v87\n"
      "ds_write_b128 v242, v[148:151] offset:0\n"
      "v_cvt_pk_f16_f32 v156, v88, v89\n"
      "v_cvt_pk_f16_f32 v157, v90, v91\n"
      "v_cvt_pk_f16_f32 v158, v92, v93\n"
      "v_cvt_pk_f16_f32 v159, v94, v95\n"
      "ds_write_b128 v241, v[152:155] offset:256\n"
      "v_cvt_pk_f16_f32 v160, v96, v97\n"
      "v_cvt_pk_f16_f32 v161, v98, v99\n"
      "v_cvt_pk_f16_f32 v162, v100, v101\n"
      "v_cvt_pk_f16_f32 v163, v102, v103\n"
      "ds_write_b128 v242, v[156:159] offset:256\n"
      "v_cvt_pk_f16_f32 v164, v104, v105\n"
      "v_cvt_pk_f16_f32 v165, v106, v107\n"
      "v_cvt_pk_f16_f32 v166, v108, v109\n"
      "v_cvt_pk_f16_f32 v167, v110, v111\n"
      "ds_write_b128 v241, v[160:163] offset:512\n"
      "v_cvt_pk_f16_f32 v168, v112, v113\n"
      "v_cvt_pk_f16_f32 v169, v114, v115\n"
      "v_cvt_pk_f16_f32 v170, v116, v117\n"
      "v_cvt_pk_f16_f32 v171, v118, v119\n"
      "ds_write_b128 v242, v[164:167] offset:512\n"
      "v_cvt_pk_f16_f32 v172, v120, v121\n"
      "v_cvt_pk_f16_f32 v173, v122, v123\n"
      "v_cvt_pk_f16_f32 v174, v124, v125\n"
      "v_cvt_pk_f16_f32 v175, v126, v127\n"
      "ds_write_b128 v241, v[168:171] offset:768\n"
      "v_cvt_pk_f16_f32 v176, v128, v129\n"
      "v_cvt_pk_f16_f32 v177, v130, v131\n"
      "v_cvt_pk_f16_f32 v178, v132, v133\n"
      "v_cvt_pk_f16_f32 v179, v134, v135\n"
      "ds_write_b128 v242, v[172:175] offset:768\n"
      "v_cvt_pk_f16_f32 v180, v136, v137\n"
      "v_cvt_pk_f16_f32 v181, v138, v139\n"
      "v_cvt_pk_f16_f32 v182, v140, v141\n"
      "v_cvt_pk_f16_f32 v183, v142, v143\n"
      "ds_write_b128 v241, v[176:179] offset:1024\n"
      "ds_write_b128 v242, v[180:183] offset:1024\n"
      "s_lshl_b32 s52, s31, 1\n"
      "v_lshrrev_b32 v243, 1, v8\n"
      "v_add_u32 v243, s52, v243\n"
      "v_and_b32 v244, 1, v7\n"
      "v_lshlrev_b32 v244, 2, v244\n"
      "v_xor_b32 v243, v243, v244\n"
      "v_lshrrev_b32 v244, 1, v7\n"
      "v_lshl_add_u32 v243, v244, 3, v243\n"
      "v_and_b32 v244, 1, v3\n"
      "v_lshlrev_b32 v244, 1, v244\n"
      "v_xor_b32 v243, v243, v244\n"
      "v_mul_u32_u24 v244, 1280, v7\n"
      "v_mul_u32_u24 v246, 10240, v3\n"
      "v_add_u32 v244, v244, v246\n"
      "v_add_u32 v244, 122880, v244\n"
      "v_and_b32 v246, 1, v8\n"
      "v_lshl_add_u32 v244, v246, 3, v244\n"
      "v_lshl_add_u32 v245, v243, 4, v244\n"
      "v_xor_b32 v243, 8, v243\n"
      "v_lshl_add_u32 v246, v243, 4, v244\n"
      "v_lshlrev_b32 v240, 14, v7\n"
      "v_lshl_add_u32 v240, v3, 6, v240\n"
      "v_lshl_add_u32 v240, v8, 4, v240\n"
      "v_readfirstlane_b32 s56, v5\n"
      "s_add_u32 s56, s56, 1\n"
      "s_waitcnt lgkmcnt(0)\n"
      "s_barrier\n"
      "ds_read_b64_tr_b16 v[184:185], v245 offset:0\n"
      "ds_read_b64_tr_b16 v[186:187], v245 offset:5120\n"
      "ds_read_b64_tr_b16 v[188:189], v246 offset:0\n"
      "ds_read_b64_tr_b16 v[190:191], v246 offset:5120\n"
      "ds_read_b64_tr_b16 v[192:193], v245 offset:256\n"
      "ds_read_b64_tr_b16 v[194:195], v245 offset:5376\n"
      "ds_read_b64_tr_b16 v[196:197], v246 offset:256\n"
      "ds_read_b64_tr_b16 v[198:199], v246 offset:5376\n"
      "ds_read_b64_tr_b16 v[200:201], v245 offset:512\n"
      "ds_read_b64_tr_b16 v[202:203], v245 offset:5632\n"
      "ds_read_b64_tr_b16 v[204:205], v246 offset:512\n"
      "ds_read_b64_tr_b16 v[206:207], v246 offset:5632\n"
      "ds_read_b64_tr_b16 v[208:209], v245 offset:768\n"
      "ds_read_b64_tr_b16 v[210:211], v245 offset:5888\n"
      "ds_read_b64_tr_b16 v[212:213], v246 offset:768\n"
      "ds_read_b64_tr_b16 v[214:215], v246 offset:5888\n"
      "ds_read_b64_tr_b16 v[216:217], v245 offset:1024\n"
      "ds_read_b64_tr_b16 v[218:219], v245 offset:6144\n"
      "ds_read_b64_tr_b16 v[220:221], v246 offset:1024\n"
      "ds_read_b64_tr_b16 v[222:223], v246 offset:6144\n"
      "s_sub_u32 s53, s80, s31\n"
      "s_lshl_b32 s53, s53, 6\n"
      "s_lshl_b32 s55, s31, 16\n"
      "s_add_u32 s53, s53, s55\n"
      "s_add_u32 s53, s53, 4194304\n"
      "s_waitcnt lgkmcnt(15)\n"
      "buffer_store_dwordx4 v[184:187], v240, s[32:35], s53 offen sc1\n"
      "s_add_u32 s53, s53, 262144\n"
      "buffer_store_dwordx4 v[188:191], v240, s[32:35], s53 offen sc1\n"
      "s_add_u32 s53, s53, 262144\n"
      "s_waitcnt lgkmcnt(14)\n"
      "buffer_store_dwordx4 v[192:195], v240, s[32:35], s53 offen sc1\n"
      "s_add_u32 s53, s53, 262144\n"
      "s_waitcnt lgkmcnt(12)\n"
      "buffer_store_dwordx4 v[196:199], v240, s[32:35], s53 offen sc1\n"
      "s_add_u32 s53, s53, 262144\n"
      "s_waitcnt lgkmcnt(10)\n"
      "buffer_store_dwordx4 v[200:203], v240, s[32:35], s53 offen sc1\n"
      "s_add_u32 s53, s53, 262144\n"
      "s_waitcnt lgkmcnt(8)\n"
      "buffer_store_dwordx4 v[204:207], v240, s[32:35], s53 offen sc1\n"
      "s_add_u32 s53, s53, 262144\n"
      "s_waitcnt lgkmcnt(6)\n"
      "buffer_store_dwordx4 v[208:211], v240, s[32:35], s53 offen sc1\n"
      "s_add_u32 s53, s53, 262144\n"
      "s_waitcnt lgkmcnt(4)\n"
      "buffer_store_dwordx4 v[212:215], v240, s[32:35], s53 offen sc1\n"
      "s_add_u32 s53, s53, 262144\n"
      "s_waitcnt lgkmcnt(2)\n"
      "buffer_store_dwordx4 v[216:219], v240, s[32:35], s53 offen sc1\n"
      "s_add_u32 s53, s53, 262144\n"
      "s_waitcnt lgkmcnt(0)\n"
      "buffer_store_dwordx4 v[220:223], v240, s[32:35], s53 offen sc1\n"
      "s_lshl_b32 s52, s80, 7\n"
      "s_add_u32 s54, s52, 98304\n"
      "s_waitcnt vmcnt(10)\n"
      "v_mov_b32 v6, s56\n"
      "s_mov_b64 exec, 1\n"
      "buffer_store_dword v6, off, s[32:35], s54 sc1\n"
      "s_mov_b64 exec, -1\n"
      "s_mov_b32 m0, s51\n"
      "s_nop 0\n"
      "buffer_load_dwordx4 v13, s[40:43], s50 offen offset:0 lds\n"
      "buffer_load_dwordx4 v14, s[40:43], s50 offen offset:1024 lds\n"
      "buffer_load_dwordx4 v15, s[40:43], s50 offen offset:2048 lds\n"
      "s_add_u32 s50, s50, 196608\n"
      "s_add_u32 s51, s51, 12288\n"
      "s_mov_b32 m0, s51\n"
      "s_nop 0\n"
      "buffer_load_dwordx4 v13, s[40:43], s50 offen offset:0 lds\n"
      "buffer_load_dwordx4 v14, s[40:43], s50 offen offset:1024 lds\n"
      "buffer_load_dwordx4 v15, s[40:43], s50 offen offset:2048 lds\n"
      "s_add_u32 s50, s50, 196608\n"
      "s_add_u32 s51, s51, 12288\n"
      "s_mov_b32 m0, s51\n"
      "s_nop 0\n"
      "buffer_load_dwordx4 v13, s[40:43], s50 offen offset:0 lds\n"
      "buffer_load_dwordx4 v14, s[40:43], s50 offen offset:1024 lds\n"
      "buffer_load_dwordx4 v15, s[40:43], s50 offen offset:2048 lds\n"
      "s_add_u32 s50, s50, 196608\n"
      "s_add_u32 s51, s51, 12288\n"
      "s_mov_b32 m0, s51\n"
      "s_nop 0\n"
      "buffer_load_dwordx4 v13, s[40:43], s50 offen offset:0 lds\n"
      "buffer_load_dwordx4 v14, s[40:43], s50 offen offset:1024 lds\n"
      "buffer_load_dwordx4 v15, s[40:43], s50 offen offset:2048 lds\n"
      "s_add_u32 s50, s50, 196608\n"
      "s_add_u32 s51, s51, 12288\n"
      "s_mov_b32 m0, s51\n"
      "s_nop 0\n"
      "buffer_load_dwordx4 v13, s[40:43], s50 offen offset:0 lds\n"
      "buffer_load_dwordx4 v14, s[40:43], s50 offen offset:1024 lds\n"
      "buffer_load_dwordx4 v15, s[40:43], s50 offen offset:2048 lds\n"
      "s_add_u32 s50, s50, 196608\n"
      "s_add_u32 s51, s51, 12288\n"
      "s_mov_b32 m0, s51\n"
      "s_nop 0\n"
      "buffer_load_dwordx4 v13, s[40:43], s50 offen offset:0 lds\n"
      "buffer_load_dwordx4 v14, s[40:43], s50 offen offset:1024 lds\n"
      "buffer_load_dwordx4 v15, s[40:43], s50 offen offset:2048 lds\n"
      "s_add_u32 s50, s50, 196608\n"
      "s_add_u32 s51, s51, 12288\n"
      "s_mov_b32 m0, s51\n"
      "s_nop 0\n"
      "buffer_load_dwordx4 v13, s[40:43], s50 offen offset:0 lds\n"
      "buffer_load_dwordx4 v14, s[40:43], s50 offen offset:1024 lds\n"
      "buffer_load_dwordx4 v15, s[40:43], s50 offen offset:2048 lds\n"
      "s_add_u32 s50, s50, 196608\n"
      "s_add_u32 s51, s51, 12288\n"
      "s_mov_b32 m0, s51\n"
      "s_nop 0\n"
      "buffer_load_dwordx4 v13, s[40:43], s50 offen offset:0 lds\n"
      "buffer_load_dwordx4 v14, s[40:43], s50 offen offset:1024 lds\n"
      "buffer_load_dwordx4 v15, s[40:43], s50 offen offset:2048 lds\n"
      "s_add_u32 s50, s50, 196608\n"
      "s_add_u32 s51, s51, 12288\n"
      "s_mov_b32 m0, s51\n"
      "s_nop 0\n"
      "buffer_load_dwordx4 v13, s[40:43], s50 offen offset:0 lds\n"
      "buffer_load_dwordx4 v14, s[40:43], s50 offen offset:1024 lds\n"
      "buffer_load_dwordx4 v15, s[40:43], s50 offen offset:2048 lds\n"
      "s_add_u32 s50, s50, 196608\n"
      "s_add_u32 s51, s51, 12288\n"
      "s_lshl_b32 s52, s31, 1\n"
      "s_add_u32 s87, s59, s52\n"
      "s_lshl_b32 s87, s87, 8\n"
      "s_add_u32 s87, s87, 98304\n"
      "v_lshl_add_u32 v251, v7, 4, v8\n"
      "v_lshlrev_b32 v251, 7, v251\n"
      "s_mov_b64 exec, 255\n"
      "buffer_load_dword v250, v251, s[32:35], s87 offen sc1\n"
      "s_mov_b64 exec, -1\n"
      "s_mov_b32 m0, s51\n"
      "s_nop 0\n"
      "buffer_load_dwordx4 v13, s[40:43], s50 offen offset:0 lds\n"
      "buffer_load_dwordx4 v14, s[40:43], s50 offen offset:1024 lds\n"
      "buffer_load_dwordx4 v15, s[40:43], s50 offen offset:2048 lds\n"
      "s_cmp_eq_u32 s31, 0\n"
      "s_cbranch_scc0 K2_bf_8\n"
      "s_lshl_b32 s52, s30, 7\n"
      "s_add_u32 s52, s52, 65536\n"
      "v_mov_b32 v6, s56\n"
      "s_mov_b64 exec, 1\n"
      "buffer_store_dword v6, off, s[32:35], s52 sc1\n"
      "s_mov_b64 exec, -1\n"
      "K2_bf_8:\n"
      "s_sub_u32 s52, s30, 160\n"
      "s_lshl_b32 s53, s31, 6\n"
      "s_add_u32 s52, s52, s53\n"
      "s_lshl_b32 s52, s52, 7\n"
      "s_add_u32 s94, s52, 229376\n"
      "s_mov_b64 s[98:99], 1\n"
      "s_branch K2_JOIN0\n"
      "K2_W2T:\n"
      "s_mov_b32 s44, s22\n"
      "s_and_b32 s45, s23, 0xffff\n"
      "s_mov_b32 s46, 1966080\n"
      "s_mov_b32 s47, 0x20000\n"
      "s_sub_u32 s52, s30, 224\n"
      "s_lshl2_add_u32 s82, s52, s31\n"
      "v_mul_u32_u24 v20, 1280, v3\n"
      "v_lshl_add_u32 v20, v4, 4, v20\n"
      "v_lshlrev_b32 v21, 1, v3\n"
      "v_xor_b32 v21, v21, v4\n"
      "v_lshlrev_b32 v22, 8, v3\n"
      "v_add_u32 v22, s81, v22\n"
      "v_lshl_add_u32 v23, v21, 4, v22\n"
      "v_xor_b32 v21, 8, v21\n"
      "v_lshl_add_u32 v24, v21, 4, v22\n"
      "v_lshl_add_u32 v25, v3, 2, v7\n"
      "v_lshlrev_b32 v26, 8, v25\n"
      "v_and_b32 v25, 7, v25\n"
      "v_lshlrev_b32 v25, 1, v25\n"
      "v_lshrrev_b32 v27, 1, v8\n"
      "v_add_u32 v25, v25, v27\n"
      "v_lshl_add_u32 v26, v25, 4, v26\n"
      "v_and_b32 v27, 1, v8\n"
      "v_lshl_add_u32 v26, v27, 3, v26\n"
      "v_add_u32 v26, s81, v26\n"
      "v_xor_b32 v28, 0, v26\n"
      "v_xor_b32 v29, 32, v26\n"
      "v_xor_b32_e64 v30, 64, v26\n"
      "v_xor_b32 v31, 96, v26\n"
      "v_xor_b32 v32, 128, v26\n"
      "v_xor_b32 v33, 160, v26\n"
      "v_xor_b32 v34, 192, v26\n"
      "v_xor_b32 v35, 224, v26\n"
      "s_add_u32 s85, s82, 128\n"
      "s_min_u32 s86, s85, 239\n"
      "s_mul_i32 s83, s82, 52429\n"
      "s_lshr_b32 s83, s83, 18\n"
      "s_mul_i32 s52, s83, 5\n"
      "s_sub_u32 s84, s82, s52\n"
      "s_mul_i32 s52, s83, 40960\n"
      "s_lshl_b32 s53, s84, 8\n"
      "s_add_u32 s52, s52, s53\n"
      "s_nop 0\n"
      "buffer_load_dwordx4 v[40:43], v20, s[44:47], s52 offen\n"
      "s_add_u32 s52, s52, 5120\n"
      "buffer_load_dwordx4 v[44:47], v20, s[44:47], s52 offen\n"
      "s_add_u32 s52, s52, 5120\n"
      "buffer_load_dwordx4 v[48:51], v20, s[44:47], s52 offen\n"
      "s_add_u32 s52, s52, 5120\n"
      "buffer_load_dwordx4 v[52:55], v20, s[44:47], s52 offen\n"
      "s_add_u32 s52, s52, 5120\n"
      "buffer_load_dwordx4 v[56:59], v20, s[44:47], s52 offen\n"
      "s_add_u32 s52, s52, 5120\n"
      "buffer_load_dwordx4 v[60:63], v20, s[44:47], s52 offen\n"
      "s_add_u32 s52, s52, 5120\n"
      "buffer_load_dwordx4 v[64:67], v20, s[44:47], s52 offen\n"
      "s_add_u32 s52, s52, 5120\n"
      "buffer_load_dwordx4 v[68:71], v20, s[44:47], s52 offen\n"
      "s_mul_i32 s83, s86, 52429\n"
      "s_lshr_b32 s83, s83, 18\n"
      "s_mul_i32 s52, s83, 5\n"
      "s_sub_u32 s84, s86, s52\n"
      "s_mul_i32 s52, s83, 40960\n"
      "s_lshl_b32 s53, s84, 8\n"
      "s_add_u32 s52, s52, s53\n"
      "s_nop 0\n"
      "buffer_load_dwordx4 v[72:75], v20, s[44:47], s52 offen\n"
      "s_add_u32 s52, s52, 5120\n"
      "buffer_load_dwordx4 v[76:79], v20, s[44:47], s52 offen\n"
      "s_add_u32 s52, s52, 5120\n"
      "buffer_load_dwordx4 v[80:83], v20, s[44:47], s52 offen\n"
      "s_add_u32 s52, s52, 5120\n"
      "buffer_load_dwordx4 v[84:87], v20, s[44:47], s52 offen\n"
      "s_add_u32 s52, s52, 5120\n"
      "buffer_load_dwordx4 v[88:91], v20, s[44:47], s52 offen\n"
      "s_add_u32 s52, s52, 5120\n"
      "buffer_load_dwordx4 v[92:95], v20, s[44:47], s52 offen\n"
      "s_add_u32 s52, s52, 5120\n"
      "buffer_load_dwordx4 v[96:99], v20, s[44:47], s52 offen\n"
      "s_add_u32 s52, s52, 5120\n"
      "buffer_load_dwordx4 v[100:103], v20, s[44:47], s52 offen\n"
      "s_mov_b32 s52, K2_END-K2_PC0-4\n"
      "s_lshl_b32 s53, s31, 13\n"
      "v_lshl_add_u32 v16, v1, 7, s53\n"
      "v_min_u32 v16, s52, v16\n"
      "v_and_b32 v16, -4, v16\n"
      "global_load_dword v18, v16, s[98:99]\n"
      "s_lshl_b32 s53, s31, 13\n"
      "s_add_u32 s53, s53, 32768\n"
      "v_lshl_add_u32 v17, v1, 7, s53\n"
      "v_min_u32 v17, s52, v17\n"
      "v_and_b32 v17, -4, v17\n"
      "global_load_dword v19, v17, s[98:99]\n"
      "s_mul_i32 s52, s60, 192\n"
      "s_add_u32 s40, s18, s52\n"
      "s_addc_u32 s41, s19, 0\n"
      "s_and_b32 s41, s41, 0xffff\n"
      "s_mov_b32 s42, 1966080\n"
      "s_mov_b32 s43, 0x20000\n"
      "s_sub_u32 s42, s42, s52\n"
      "s_mul_i32 s50, s31, 49152\n"
      "s_mul_i32 s51, s31, 3072\n"
      "s_mov_b32 m0, s51\n"
      "s_nop 0\n"
      "buffer_load_dwordx4 v13, s[40:43], s50 offen offset:0 lds\n"
      "buffer_load_dwordx4 v14, s[40:43], s50 offen offset:1024 lds\n"
      "buffer_load_dwordx4 v15, s[40:43], s50 offen offset:2048 lds\n"
      "s_add_u32 s50, s50, 196608\n"
      "s_add_u32 s51, s51, 12288\n"
      "s_mov_b32 m0, s51\n"
      "s_nop 0\n"
      "buffer_load_dwordx4 v13, s[40:43], s50 offen offset:0 lds\n"
      "buffer_load_dwordx4 v14, s[40:43], s50 offen offset:1024 lds\n"
      "buffer_load_dwordx4 v15, s[40:43], s50 offen offset:2048 lds\n"
      "s_add_u32 s50, s50, 196608\n"
      "s_add_u32 s51, s51, 12288\n"
      "s_mov_b32 m0, s51\n"
      "s_nop 0\n"
      "buffer_load_dwordx4 v13, s[40:43], s50 offen offset:0 lds\n"
      "buffer_load_dwordx4 v14, s[40:43], s50 offen offset:1024 lds\n"
      "buffer_load_dwordx4 v15, s[40:43], s50 offen offset:2048 lds\n"
      "s_add_u32 s50, s50, 196608\n"
      "s_add_u32 s51, s51, 12288\n"
      "s_mov_b32 m0, s51\n"
      "s_nop 0\n"
      "buffer_load_dwordx4 v13, s[40:43], s50 offen offset:0 lds\n"
      "buffer_load_dwordx4 v14, s[40:43], s50 offen offset:1024 lds\n"
      "buffer_load_dwordx4 v15, s[40:43], s50 offen offset:2048 lds\n"
      "s_add_u32 s50, s50, 196608\n"
      "s_add_u32 s51, s51, 12288\n"
      "s_mov_b32 m0, s51\n"
      "s_nop 0\n"
      "s_nop 0\n"
      "buffer_load_dwordx4 v13, s[40:43], s50 offen offset:0 lds\n"
      "buffer_load_dwordx4 v14, s[40:43], s50 offen offset:1024 lds\n"
      "buffer_load_dwordx4 v15, s[40:43], s50 offen offset:2048 lds\n"
      "s_add_u32 s50, s50, 196608\n"
      "s_add_u32 s51, s51, 12288\n"
      "s_mul_i32 s83, s82, 52429\n"
      "s_lshr_b32 s83, s83, 18\n"
      "s_mul_i32 s52, s83, 5\n"
      "s_sub_u32 s84, s82, s52\n"
      "s_waitcnt vmcnt(32)\n"
      "ds_write_b128 v23, v[40:43] offset:0\n"
      "s_waitcnt vmcnt(31)\n"
      "ds_write_b128 v24, v[44:47] offset:1024\n"
      "s_waitcnt vmcnt(30)\n"
      "ds_write_b128 v23, v[48:51] offset:2048\n"
      "s_waitcnt vmcnt(29)\n"
      "ds_write_b128 v24, v[52:55] offset:3072\n"
      "s_waitcnt vmcnt(28)\n"
      "ds_write_b128 v23, v[56:59] offset:4096\n"
      "s_waitcnt vmcnt(27)\n"
      "ds_write_b128 v24, v[60:63] offset:5120\n"
      "s_waitcnt vmcnt(26)\n"
      "ds_write_b128 v23, v[64:67] offset:6144\n"
      "s_waitcnt vmcnt(25)\n"
      "s_nop 0\n"
      "ds_write_b128 v24, v[68:71] offset:7168\n"
      "ds_read_b64_tr_b16 v[104:105], v28\n"
      "ds_read_b64_tr_b16 v[106:107], v28 offset:4096\n"
      "ds_read_b64_tr_b16 v[108:109], v29\n"
      "ds_read_b64_tr_b16 v[110:111], v29 offset:4096\n"
      "ds_read_b64_tr_b16 v[112:113], v30\n"
      "ds_read_b64_tr_b16 v[114:115], v30 offset:4096\n"
      "ds_read_b64_tr_b16 v[116:117], v31\n"
      "ds_read_b64_tr_b16 v[118:119], v31 offset:4096\n"
      "ds_read_b64_tr_b16 v[120:121], v32\n"
      "ds_read_b64_tr_b16 v[122:123], v32 offset:4096\n"
      "ds_read_b64_tr_b16 v[124:125], v33\n"
      "ds_read_b64_tr_b16 v[126:127], v33 offset:4096\n"
      "ds_read_b64_tr_b16 v[128:129], v34\n"
      "ds_read_b64_tr_b16 v[130:131], v34 offset:4096\n"
      "ds_read_b64_tr_b16 v[132:133], v35\n"
      "ds_read_b64_tr_b16 v[134:135], v35 offset:4096\n"
      "s_mul_i32 s54, s83, 5\n"
      "s_lshl3_add_u32 s54, s84, s54\n"
      "s_lshl_b32 s55, s84, 3\n"
      "s_add_u32 s52, s55, 0\n"
      "s_mul_i32 s52, s52, 52\n"
      "s_lshr_b32 s52, s52, 8\n"
      "s_mul_i32 s52, s52, 235\n"
      "s_add_u32 s52, s52, s54\n"
      "s_add_u32 s52, s52, 0\n"
      "s_lshl_b32 s52, s52, 10\n"
      "s_add_u32 s52, s52, 8388608\n"
      "s_waitcnt lgkmcnt(14)\n"
      "buffer_store_dwordx4 v[104:107], v2, s[32:35], s52 offen sc1\n"
      "s_add_u32 s52, s55, 1\n"
      "s_mul_i32 s52, s52, 52\n"
      "s_lshr_b32 s52, s52, 8\n"
      "s_mul_i32 s52, s52, 235\n"
      "s_add_u32 s52, s52, s54\n"
      "s_add_u32 s52, s52, 1\n"
      "s_lshl_b32 s52, s52, 10\n"
      "s_add_u32 s52, s52, 8388608\n"
      "s_waitcnt lgkmcnt(12)\n"
      "buffer_store_dwordx4 v[108:111], v2, s[32:35], s52 offen sc1\n"
      "s_add_u32 s52, s55, 2\n"
      "s_mul_i32 s52, s52, 52\n"
      "s_lshr_b32 s52, s52, 8\n"
      "s_mul_i32 s52, s52, 235\n"
      "s_add_u32 s52, s52, s54\n"
      "s_add_u32 s52, s52, 2\n"
      "s_lshl_b32 s52, s52, 10\n"
      "s_add_u32 s52, s52, 8388608\n"
      "s_waitcnt lgkmcnt(10)\n"
      "buffer_store_dwordx4 v[112:115], v2, s[32:35], s52 offen sc1\n"
      "s_add_u32 s52, s55, 3\n"
      "s_mul_i32 s52, s52, 52\n"
      "s_lshr_b32 s52, s52, 8\n"
      "s_mul_i32 s52, s52, 235\n"
      "s_add_u32 s52, s52, s54\n"
      "s_add_u32 s52, s52, 3\n"
      "s_lshl_b32 s52, s52, 10\n"
      "s_add_u32 s52, s52, 8388608\n"
      "s_waitcnt lgkmcnt(8)\n"
      "buffer_store_dwordx4 v[116:119], v2, s[32:35], s52 offen sc1\n"
      "s_add_u32 s52, s55, 4\n"
      "s_mul_i32 s52, s52, 52\n"
      "s_lshr_b32 s52, s52, 8\n"
      "s_mul_i32 s52, s52, 235\n"
      "s_add_u32 s52, s52, s54\n"
      "s_add_u32 s52, s52, 4\n"
      "s_lshl_b32 s52, s52, 10\n"
      "s_add_u32 s52, s52, 8388608\n"
      "s_waitcnt lgkmcnt(6)\n"
      "buffer_store_dwordx4 v[120:123], v2, s[32:35], s52 offen sc1\n"
      "s_add_u32 s52, s55, 5\n"
      "s_mul_i32 s52, s52, 52\n"
      "s_lshr_b32 s52, s52, 8\n"
      "s_mul_i32 s52, s52, 235\n"
      "s_add_u32 s52, s52, s54\n"
      "s_add_u32 s52, s52, 5\n"
      "s_lshl_b32 s52, s52, 10\n"
      "s_add_u32 s52, s52, 8388608\n"
      "s_waitcnt lgkmcnt(4)\n"
      "buffer_store_dwordx4 v[124:127], v2, s[32:35], s52 offen sc1\n"
      "s_add_u32 s52, s55, 6\n"
      "s_mul_i32 s52, s52, 52\n"
      "s_lshr_b32 s52, s52, 8\n"
      "s_mul_i32 s52, s52, 235\n"
      "s_add_u32 s52, s52, s54\n"
      "s_add_u32 s52, s52, 6\n"
      "s_lshl_b32 s52, s52, 10\n"
      "s_add_u32 s52, s52, 8388608\n"
      "s_waitcnt lgkmcnt(2)\n"
      "buffer_store_dwordx4 v[128:131], v2, s[32:35], s52 offen sc1\n"
      "s_add_u32 s52, s55, 7\n"
      "s_mul_i32 s52, s52, 52\n"
      "s_lshr_b32 s52, s52, 8\n"
      "s_mul_i32 s52, s52, 235\n"
      "s_add_u32 s52, s52, s54\n"
      "s_add_u32 s52, s52, 7\n"
      "s_lshl_b32 s52, s52, 10\n"
      "s_add_u32 s52, s52, 8388608\n"
      "s_waitcnt lgkmcnt(0)\n"
      "buffer_store_dwordx4 v[132:135], v2, s[32:35], s52 offen sc1\n"
      "s_cmp_ge_u32 s85, 240\n"
      "s_cbranch_scc1 K2_W2T_ONE\n"
      "s_mul_i32 s83, s85, 52429\n"
      "s_lshr_b32 s83, s83, 18\n"
      "s_mul_i32 s52, s83, 5\n"
      "s_sub_u32 s84, s85, s52\n"
      "s_waitcnt vmcnt(32)\n"
      "ds_write_b128 v23, v[72:75] offset:0\n"
      "s_waitcnt vmcnt(31)\n"
      "ds_write_b128 v24, v[76:79] offset:1024\n"
      "s_waitcnt vmcnt(30)\n"
      "ds_write_b128 v23, v[80:83] offset:2048\n"
      "s_waitcnt vmcnt(29)\n"
      "ds_write_b128 v24, v[84:87] offset:3072\n"
      "s_waitcnt vmcnt(28)\n"
      "ds_write_b128 v23, v[88:91] offset:4096\n"
      "s_waitcnt vmcnt(27)\n"
      "ds_write_b128 v24, v[92:95] offset:5120\n"
      "s_waitcnt vmcnt(26)\n"
      "ds_write_b128 v23, v[96:99] offset:6144\n"
      "s_waitcnt vmcnt(25)\n"
      "s_nop 0\n"
      "ds_write_b128 v24, v[100:103] offset:7168\n"
      "ds_read_b64_tr_b16 v[136:137], v28\n"
      "ds_read_b64_tr_b16 v[138:139], v28 offset:4096\n"
      "ds_read_b64_tr_b16 v[140:141], v29\n"
      "ds_read_b64_tr_b16 v[142:143], v29 offset:4096\n"
      "ds_read_b64_tr_b16 v[144:145], v30\n"
      "ds_read_b64_tr_b16 v[146:147], v30 offset:4096\n"
      "ds_read_b64_tr_b16 v[148:149], v31\n"
      "ds_read_b64_tr_b16 v[150:151], v31 offset:4096\n"
      "ds_read_b64_tr_b16 v[152:153], v32\n"
      "ds_read_b64_tr_b16 v[154:155], v32 offset:4096\n"
      "ds_read_b64_tr_b16 v[156:157], v33\n"
      "ds_read_b64_tr_b16 v[158:159], v33 offset:4096\n"
      "ds_read_b64_tr_b16 v[160:161], v34\n"
      "ds_read_b64_tr_b16 v[162:163], v34 offset:4096\n"
      "ds_read_b64_tr_b16 v[164:165], v35\n"
      "ds_read_b64_tr_b16 v[166:167], v35 offset:4096\n"
      "s_mul_i32 s54, s83, 5\n"
      "s_lshl3_add_u32 s54, s84, s54\n"
      "s_lshl_b32 s55, s84, 3\n"
      "s_add_u32 s52, s55, 0\n"
      "s_mul_i32 s52, s52, 52\n"
      "s_lshr_b32 s52, s52, 8\n"
      "s_mul_i32 s52, s52, 235\n"
      "s_add_u32 s52, s52, s54\n"
      "s_add_u32 s52, s52, 0\n"
      "s_lshl_b32 s52, s52, 10\n"
      "s_add_u32 s52, s52, 8388608\n"
      "s_waitcnt lgkmcnt(14)\n"
      "buffer_store_dwordx4 v[136:139], v2, s[32:35], s52 offen sc1\n"
      "s_add_u32 s52, s55, 1\n"
      "s_mul_i32 s52, s52, 52\n"
      "s_lshr_b32 s52, s52, 8\n"
      "s_mul_i32 s52, s52, 235\n"
      "s_add_u32 s52, s52, s54\n"
      "s_add_u32 s52, s52, 1\n"
      "s_lshl_b32 s52, s52, 10\n"
      "s_add_u32 s52, s52, 8388608\n"
      "s_waitcnt lgkmcnt(12)\n"
      "buffer_store_dwordx4 v[140:143], v2, s[32:35], s52 offen sc1\n"
      "s_add_u32 s52, s55, 2\n"
      "s_mul_i32 s52, s52, 52\n"
      "s_lshr_b32 s52, s52, 8\n"
      "s_mul_i32 s52, s52, 235\n"
      "s_add_u32 s52, s52, s54\n"
      "s_add_u32 s52, s52, 2\n"
      "s_lshl_b32 s52, s52, 10\n"
      "s_add_u32 s52, s52, 8388608\n"
      "s_waitcnt lgkmcnt(10)\n"
      "buffer_store_dwordx4 v[144:147], v2, s[32:35], s52 offen sc1\n"
      "s_add_u32 s52, s55, 3\n"
      "s_mul_i32 s52, s52, 52\n"
      "s_lshr_b32 s52, s52, 8\n"
      "s_mul_i32 s52, s52, 235\n"
      "s_add_u32 s52, s52, s54\n"
      "s_add_u32 s52, s52, 3\n"
      "s_lshl_b32 s52, s52, 10\n"
      "s_add_u32 s52, s52, 8388608\n"
      "s_waitcnt lgkmcnt(8)\n"
      "buffer_store_dwordx4 v[148:151], v2, s[32:35], s52 offen sc1\n"
      "s_add_u32 s52, s55, 4\n"
      "s_mul_i32 s52, s52, 52\n"
      "s_lshr_b32 s52, s52, 8\n"
      "s_mul_i32 s52, s52, 235\n"
      "s_add_u32 s52, s52, s54\n"
      "s_add_u32 s52, s52, 4\n"
      "s_lshl_b32 s52, s52, 10\n"
      "s_add_u32 s52, s52, 8388608\n"
      "s_waitcnt lgkmcnt(6)\n"
      "buffer_store_dwordx4 v[152:155], v2, s[32:35], s52 offen sc1\n"
      "s_add_u32 s52, s55, 5\n"
      "s_mul_i32 s52, s52, 52\n"
      "s_lshr_b32 s52, s52, 8\n"
      "s_mul_i32 s52, s52, 235\n"
      "s_add_u32 s52, s52, s54\n"
      "s_add_u32 s52, s52, 5\n"
      "s_lshl_b32 s52, s52, 10\n"
      "s_add_u32 s52, s52, 8388608\n"
      "s_waitcnt lgkmcnt(4)\n"
      "buffer_store_dwordx4 v[156:159], v2, s[32:35], s52 offen sc1\n"
      "s_add_u32 s52, s55, 6\n"
      "s_mul_i32 s52, s52, 52\n"
      "s_lshr_b32 s52, s52, 8\n"
      "s_mul_i32 s52, s52, 235\n"
      "s_add_u32 s52, s52, s54\n"
      "s_add_u32 s52, s52, 6\n"
      "s_lshl_b32 s52, s52, 10\n"
      "s_add_u32 s52, s52, 8388608\n"
      "s_waitcnt lgkmcnt(2)\n"
      "buffer_store_dwordx4 v[160:163], v2, s[32:35], s52 offen sc1\n"
      "s_add_u32 s52, s55, 7\n"
      "s_mul_i32 s52, s52, 52\n"
      "s_lshr_b32 s52, s52, 8\n"
      "s_mul_i32 s52, s52, 235\n"
      "s_add_u32 s52, s52, s54\n"
      "s_add_u32 s52, s52, 7\n"
      "s_lshl_b32 s52, s52, 10\n"
      "s_add_u32 s52, s52, 8388608\n"
      "s_waitcnt lgkmcnt(0)\n"
      "buffer_store_dwordx4 v[164:167], v2, s[32:35], s52 offen sc1\n"
      "K2_W2T_ONE:\n"
      "s_mov_b32 m0, s51\n"
      "s_nop 0\n"
      "buffer_load_dwordx4 v13, s[40:43], s50 offen offset:0 lds\n"
      "buffer_load_dwordx4 v14, s[40:43], s50 offen offset:1024 lds\n"
      "buffer_load_dwordx4 v15, s[40:43], s50 offen offset:2048 lds\n"
      "s_add_u32 s50, s50, 196608\n"
      "s_add_u32 s51, s51, 12288\n"
      "s_mov_b32 m0, s51\n"
      "s_nop 0\n"
      "buffer_load_dwordx4 v13, s[40:43], s50 offen offset:0 lds\n"
      "buffer_load_dwordx4 v14, s[40:43], s50 offen offset:1024 lds\n"
      "buffer_load_dwordx4 v15, s[40:43], s50 offen offset:2048 lds\n"
      "s_add_u32 s50, s50, 196608\n"
      "s_add_u32 s51, s51, 12288\n"
      "s_mov_b32 m0, s51\n"
      "s_nop 0\n"
      "buffer_load_dwordx4 v13, s[40:43], s50 offen offset:0 lds\n"
      "buffer_load_dwordx4 v14, s[40:43], s50 offen offset:1024 lds\n"
      "buffer_load_dwordx4 v15, s[40:43], s50 offen offset:2048 lds\n"
      "s_add_u32 s50, s50, 196608\n"
      "s_add_u32 s51, s51, 12288\n"
      "s_mov_b32 m0, s51\n"
      "s_nop 0\n"
      "buffer_load_dwordx4 v13, s[40:43], s50 offen offset:0 lds\n"
      "buffer_load_dwordx4 v14, s[40:43], s50 offen offset:1024 lds\n"
      "buffer_load_dwordx4 v15, s[40:43], s50 offen offset:2048 lds\n"
      "s_add_u32 s50, s50, 196608\n"
      "s_add_u32 s51, s51, 12288\n"
      "s_mov_b32 m0, s51\n"
      "s_nop 0\n"
      "buffer_load_dwordx4 v13, s[40:43], s50 offen offset:0 lds\n"
      "buffer_load_dwordx4 v14, s[40:43], s50 offen offset:1024 lds\n"
      "buffer_load_dwordx4 v15, s[40:43], s50 offen offset:2048 lds\n"
      "v_readfirstlane_b32 s56, v5\n"
      "s_add_u32 s56, s56, 1\n"
      "s_waitcnt vmcnt(15)\n"
      "s_barrier\n"
      "s_cmp_eq_u32 s31, 0\n"
      "s_cbranch_scc0 K2_bf_9\n"
      "s_lshl_b32 s52, s30, 7\n"
      "s_add_u32 s52, s52, 65536\n"
      "v_mov_b32 v6, s56\n"
      "s_mov_b64 exec, 1\n"
      "buffer_store_dword v6, off, s[32:35], s52 sc1\n"
      "s_mov_b64 exec, -1\n"
      "K2_bf_9:\n"
      "s_lshl_b32 s52, s31, 1\n"
      "s_add_u32 s87, s59, s52\n"
      "s_lshl_b32 s87, s87, 8\n"
      "s_add_u32 s87, s87, 98304\n"
      "v_lshl_add_u32 v251, v7, 4, v8\n"
      "v_lshlrev_b32 v251, 7, v251\n"
      "s_mov_b64 exec, 255\n"
      "buffer_load_dword v250, v251, s[32:35], s87 offen sc1\n"
      "s_mov_b64 exec, -1\n"
      "s_mov_b32 s94, 229376\n"
      "s_mov_b64 s[98:99], 0\n"
      "K2_JOIN0:\n"
      "s_nop 0\n"
      "s_mul_i32 s52, s60, 192\n"
      "v_lshl_add_u32 v220, v3, 3, s52\n"
      "global_load_dwordx2 v[208:209], v220, s[20:21] offset:0\n"
      "global_load_dwordx2 v[210:211], v220, s[20:21] offset:32\n"
      "global_load_dwordx2 v[212:213], v220, s[20:21] offset:64\n"
      "global_load_dwordx2 v[214:215], v220, s[20:21] offset:96\n"
      "global_load_dwordx2 v[216:217], v220, s[20:21] offset:128\n"
      "global_load_dwordx2 v[218:219], v220, s[20:21] offset:160\n"
      "v_lshl_add_u32 v180, v3, 3, v7\n"
      "v_mul_u32_u24 v180, 192, v180\n"
      "v_lshl_add_u32 v180, v8, 3, v180\n"
      "v_and_b32 v181, 1, v3\n"
      "v_lshlrev_b32 v181, 5, v181\n"
      "v_add_u32 v168, v180, v181\n"
      "v_xor_b32 v181, 32, v181\n"
      "v_add_u32 v169, v180, v181\n"
      "v_add_u32 v170, 61440, v168\n"
      "v_add_u32 v171, 61440, v169\n"
      "s_and_b32 s55, s60, 1\n"
      "s_lshl_b32 s55, s55, 3\n"
      "s_lshl1_add_u32 s52, s31, s59\n"
      "s_add_u32 s52, s52, s55\n"
      "s_lshl_b32 s55, s55, 1\n"
      "s_sub_u32 s55, 8, s55\n"
      "s_mul_i32 s53, s52, 20480\n"
      "s_add_u32 s53, s53, 1048576\n"
      "v_add_u32 v172, s53, v2\n"
      "v_add_u32 v173, 20480, v172\n"
      "s_mul_i32 s54, s55, 20480\n"
      "v_add_u32 v174, s54, v172\n"
      "v_add_u32 v175, s54, v173\n"
      "s_mul_i32 s53, s52, 48\n"
      "s_mul_i32 s54, s60, 3\n"
      "s_add_u32 s53, s53, s54\n"
      "s_lshl_b32 s53, s53, 10\n"
      "s_add_u32 s83, s53, 12582912\n"
      "s_add_u32 s84, s83, 49152\n"
      "s_mul_i32 s54, s55, 49152\n"
      "s_add_u32 s85, s83, s54\n"
      "s_add_u32 s86, s84, s54\n"
      "s_lshl_b32 s53, s60, 7\n"
      "s_add_u32 s53, s53, s52\n"
      "s_lshl_b32 s53, s53, 7\n"
      "s_add_u32 s82, s53, 262144\n"
      "s_lshl_b32 s54, s55, 7\n"
      "s_add_u32 s88, s82, s54\n"
      "s_mul_i32 s52, s49, 240\n"
      "s_bfe_u32 s53, s48, 0x10001\n"
      "s_lshl1_add_u32 s53, s31, s53\n"
      "s_mul_i32 s53, s53, 15\n"
      "s_add_u32 s53, s52, s53\n"
      "s_lshl_b32 s53, s53, 10\n"
      "s_add_u32 s53, s53, 8388608\n"
      "v_add_u32 v12, s53, v2\n"
      "s_bfe_u32 s53, s48, 0x10001\n"
      "s_lshl1_add_u32 s53, s31, s53\n"
      "s_add_u32 s53, s53, 8\n"
      "s_mul_i32 s53, s53, 15\n"
      "s_add_u32 s53, s52, s53\n"
      "s_lshl_b32 s53, s53, 10\n"
      "s_add_u32 s53, s53, 8388608\n"
      "v_add_u32 v242, s53, v2\n"
      "s_bfe_u32 s53, s48, 0x10001\n"
      "s_xor_b32 s53, s53, 1\n"
      "s_lshl1_add_u32 s53, s31, s53\n"
      "s_mul_i32 s53, s53, 15\n"
      "s_add_u32 s53, s52, s53\n"
      "s_lshl_b32 s53, s53, 10\n"
      "s_add_u32 s53, s53, 8388608\n"
      "v_add_u32 v243, s53, v2\n"
      "s_bfe_u32 s53, s48, 0x10001\n"
      "s_xor_b32 s53, s53, 1\n"
      "s_lshl1_add_u32 s53, s31, s53\n"
      "s_add_u32 s53, s53, 8\n"
      "s_mul_i32 s53, s53, 15\n"
      "s_add_u32 s53, s52, s53\n"
      "s_lshl_b32 s53, s53, 10\n"
      "s_add_u32 s53, s53, 8388608\n"
      "v_add_u32 v244, s53, v2\n"
      "s_mov_b32 s92, 94208\n"
      "s_waitcnt vmcnt(38)\n"
      "v_mov_b32 v6, s56\n"
      "s_mov_b64 exec, s[98:99]\n"
      "buffer_store_dword v6, off, s[32:35], s94 sc1\n"
      "s_mov_b64 exec, -1\n"
      "s_waitcnt vmcnt(7)\n"
      "s_mov_b64 exec, 255\n"
      "v_cmp_ne_u32_e64 vcc, s56, v250\n"
      "s_mov_b64 exec, -1\n"
      "s_cbranch_vccnz K2_spin_10\n"
      "K2_cont_11:\n"
      "buffer_load_dwordx4 a[0:3], v172, s[32:35], 0 offen offset:0 sc1\n"
      "buffer_load_dwordx4 a[4:7], v173, s[32:35], 0 offen offset:0 sc1\n"
      "buffer_load_dwordx4 a[8:11], v172, s[32:35], 0 offen offset:1024 sc1\n"
      "buffer_load_dwordx4 a[12:15], v173, s[32:35], 0 offen offset:1024 sc1\n"
      "buffer_load_dwordx4 a[16:19], v172, s[32:35], 0 offen offset:2048 sc1\n"
      "buffer_load_dwordx4 a[20:23], v173, s[32:35], 0 offen offset:2048 sc1\n"
      "buffer_load_dwordx4 a[24:27], v172, s[32:35], 0 offen offset:3072 sc1\n"
      "buffer_load_dwordx4 a[28:31], v173, s[32:35], 0 offen offset:3072 sc1\n"
      "buffer_load_dwordx4 a[32:35], v172, s[32:35], s65 offen offset:0 sc1\n"
      "buffer_load_dwordx4 a[36:39], v173, s[32:35], s65 offen offset:0 sc1\n"
      "buffer_load_dwordx4 a[40:43], v172, s[32:35], s65 offen offset:1024 sc1\n"
      "buffer_load_dwordx4 a[44:47], v173, s[32:35], s65 offen offset:1024 sc1\n"
      "buffer_load_dwordx4 a[48:51], v172, s[32:35], s65 offen offset:2048 sc1\n"
      "buffer_load_dwordx4 a[52:55], v173, s[32:35], s65 offen offset:2048 sc1\n"
      "buffer_load_dwordx4 a[56:59], v172, s[32:35], s65 offen offset:3072 sc1\n"
      "buffer_load_dwordx4 a[60:63], v173, s[32:35], s65 offen offset:3072 sc1\n"
      "s_waitcnt vmcnt(22)\n"
      "v_lshlrev_b32 v144, 16, v208\n"
      "v_and_b32 v145, s63, v208\n"
      "v_lshlrev_b32 v146, 16, v209\n"
      "v_and_b32 v147, s63, v209\n"
      "s_waitcnt vmcnt(21)\n"
      "v_lshlrev_b32 v148, 16, v210\n"
      "v_and_b32 v149, s63, v210\n"
      "v_lshlrev_b32 v150, 16, v211\n"
      "v_and_b32 v151, s63, v211\n"
      "s_waitcnt vmcnt(20)\n"
      "v_lshlrev_b32 v152, 16, v212\n"
      "v_and_b32 v153, s63, v212\n"
      "v_lshlrev_b32 v154, 16, v213\n"
      "v_and_b32 v155, s63, v213\n"
      "s_waitcnt vmcnt(19)\n"
      "v_lshlrev_b32 v156, 16, v214\n"
      "v_and_b32 v157, s63, v214\n"
      "v_lshlrev_b32 v158, 16, v215\n"
      "v_and_b32 v159, s63, v215\n"
      "s_waitcnt vmcnt(18)\n"
      "v_lshlrev_b32 v160, 16, v216\n"
      "v_and_b32 v161, s63, v216\n"
      "v_lshlrev_b32 v162, 16, v217\n"
      "v_and_b32 v163, s63, v217\n"
      "s_waitcnt vmcnt(17)\n"
      "v_lshlrev_b32 v164, 16, v218\n"
      "v_and_b32 v165, s63, v218\n"
      "v_lshlrev_b32 v166, 16, v219\n"
      "v_and_b32_e64 v167, s63, v219\n"
      "s_barrier\n"
      "ds_read_b64_tr_b16 v[112:113], v168 offset:0\n"
      "ds_read_b64_tr_b16 v[114:115], v168 offset:768\n"
      "ds_read_b64_tr_b16 v[116:117], v169 offset:0\n"
      "ds_read_b64_tr_b16 v[118:119], v169 offset:768\n"
      "ds_read_b64_tr_b16 v[120:121], v168 offset:64\n"
      "ds_read_b64_tr_b16 v[122:123], v168 offset:832\n"
      "ds_read_b64_tr_b16 v[124:125], v169 offset:64\n"
      "ds_read_b64_tr_b16 v[126:127], v169 offset:832\n"
      "ds_read_b64_tr_b16 v[128:129], v168 offset:128\n"
      "ds_read_b64_tr_b16 v[130:131], v168 offset:896\n"
      "ds_read_b64_tr_b16 v[132:133], v169 offset:128\n"
      "ds_read_b64_tr_b16 v[134:135], v169 offset:896\n"
      "ds_read_b64_tr_b16 v[136:137], v168 offset:6144\n"
      "ds_read_b64_tr_b16 v[138:139], v168 offset:6912\n"
      "ds_read_b64_tr_b16 v[140:141], v169 offset:6144\n"
      "ds_read_b64_tr_b16 v[142:143], v169 offset:6912\n"
      "s_waitcnt vmcnt(15) lgkmcnt(14)\n"
      "v_mfma_f32_16x16x32_bf16 v[16:19], v[112:115], a[0:3], v[144:147]\n"
      "s_waitcnt vmcnt(14)\n"
      "v_mfma_f32_16x16x32_bf16 v[40:43], v[112:115], a[4:7], v[144:147]\n"
      "ds_read_b64_tr_b16 v[112:113], v168 offset:6208\n"
      "buffer_load_dwordx4 a[64:67], v172, s[32:35], s66 offen offset:0 sc1\n"
      "s_waitcnt lgkmcnt(13)\n"
      "v_mfma_f32_16x16x32_bf16 v[20:23], v[116:119], a[0:3], v[148:151]\n"
      "ds_read_b64_tr_b16 v[114:115], v168 offset:6976\n"
      "v_mfma_f32_16x16x32_bf16 v[44:47], v[116:119], a[4:7], v[148:151]\n"
      "ds_read_b64_tr_b16 v[116:117], v169 offset:6208\n"
      "s_waitcnt lgkmcnt(11)\n"
      "v_mfma_f32_16x16x32_bf16 v[24:27], v[120:123], a[0:3], v[152:155]\n"
      "ds_read_b64_tr_b16 v[118:119], v169 offset:6976\n"
      "v_mfma_f32_16x16x32_bf16 v[48:51], v[120:123], a[4:7], v[152:155]\n"
      "ds_read_b64_tr_b16 v[120:121], v168 offset:6272\n"
      "buffer_load_dwordx4 a[68:71], v173, s[32:35], s66 offen offset:0 sc1\n"
      "v_mfma_f32_16x16x32_bf16 v[28:31], v[124:127], a[0:3], v[156:159]\n"
      "ds_read_b64_tr_b16 v[122:123], v168 offset:7040\n"
      "v_mfma_f32_16x16x32_bf16 v[52:55], v[124:127], a[4:7], v[156:159]\n"
      "ds_read_b64_tr_b16 v[124:125], v169 offset:6272\n"
      "s_waitcnt lgkmcnt(11)\n"
      "v_mfma_f32_16x16x32_bf16 v[32:35], v[128:131], a[0:3], v[160:163]\n"
      "ds_read_b64_tr_b16 v[126:127], v169 offset:7040\n"
      "v_mfma_f32_16x16x32_bf16 v[56:59], v[128:131], a[4:7], v[160:163]\n"
      "ds_read_b64_tr_b16 v[128:129], v168 offset:12288\n"
      "buffer_load_dwordx4 a[72:75], v172, s[32:35], s66 offen offset:1024 sc1\n"
      "v_mfma_f32_16x16x32_bf16 v[36:39], v[132:135], a[0:3], v[164:167]\n"
      "ds_read_b64_tr_b16 v[130:131], v168 offset:13056\n"
      "v_mfma_f32_16x16x32_bf16 v[60:63], v[132:135], a[4:7], v[164:167]\n"
      "ds_read_b64_tr_b16 v[132:133], v169 offset:12288\n"
      "s_waitcnt lgkmcnt(11)\n"
      "s_waitcnt vmcnt(16)\n"
      "v_mfma_f32_16x16x32_bf16 v[16:19], v[136:139], a[8:11], v[16:19]\n"
      "ds_read_b64_tr_b16 v[134:135], v169 offset:13056\n"
      "s_waitcnt vmcnt(15)\n"
      "v_mfma_f32_16x16x32_bf16 v[40:43], v[136:139], a[12:15], v[40:43]\n"
      "ds_read_b64_tr_b16 v[136:137], v168 offset:12352\n"
      "buffer_load_dwordx4 a[76:79], v173, s[32:35], s66 offen offset:1024 sc1\n"
      "v_mfma_f32_16x16x32_bf16 v[20:23], v[140:143], a[8:11], v[20:23]\n"
      "ds_read_b64_tr_b16 v[138:139], v168 offset:13120\n"
      "v_mfma_f32_16x16x32_bf16 v[44:47], v[140:143], a[12:15], v[44:47]\n"
      "ds_read_b64_tr_b16 v[140:141], v169 offset:12352\n"
      "s_waitcnt lgkmcnt(11)\n"
      "v_mfma_f32_16x16x32_bf16 v[24:27], v[112:115], a[8:11], v[24:27]\n"
      "ds_read_b64_tr_b16 v[142:143], v169 offset:13120\n"
      "v_mfma_f32_16x16x32_bf16 v[48:51], v[112:115], a[12:15], v[48:51]\n"
      "ds_read_b64_tr_b16 v[112:113], v168 offset:12416\n"
      "buffer_load_dwordx4 a[80:83], v172, s[32:35], s66 offen offset:2048 sc1\n"
      "v_mfma_f32_16x16x32_bf16 v[28:31], v[116:119], a[8:11], v[28:31]\n"
      "ds_read_b64_tr_b16 v[114:115], v168 offset:13184\n"
      "v_mfma_f32_16x16x32_bf16 v[52:55], v[116:119], a[12:15], v[52:55]\n"
      "ds_read_b64_tr_b16 v[116:117], v169 offset:12416\n"
      "s_waitcnt lgkmcnt(11)\n"
      "v_mfma_f32_16x16x32_bf16 v[32:35], v[120:123], a[8:11], v[32:35]\n"
      "ds_read_b64_tr_b16 v[118:119], v169 offset:13184\n"
      "v_mfma_f32_16x16x32_bf16 v[56:59], v[120:123], a[12:15], v[56:59]\n"
      "ds_read_b64_tr_b16 v[120:121], v168 offset:18432\n"
      "buffer_load_dwordx4 a[84:87], v173, s[32:35], s66 offen offset:2048 sc1\n"
      "v_mfma_f32_16x16x32_bf16 v[36:39], v[124:127], a[8:11], v[36:39]\n"
      "ds_read_b64_tr_b16 v[122:123], v168 offset:19200\n"
      "v_mfma_f32_16x16x32_bf16 v[60:63], v[124:127], a[12:15], v[60:63]\n"
      "ds_read_b64_tr_b16 v[124:125], v169 offset:18432\n"
      "s_waitcnt lgkmcnt(11)\n"
      "s_waitcnt vmcnt(17)\n"
      "v_mfma_f32_16x16x32_bf16 v[16:19], v[128:131], a[16:19], v[16:19]\n"
      "ds_read_b64_tr_b16 v[126:127], v169 offset:19200\n"
      "s_waitcnt vmcnt(16)\n"
      "v_mfma_f32_16x16x32_bf16 v[40:43], v[128:131], a[20:23], v[40:43]\n"
      "ds_read_b64_tr_b16 v[128:129], v168 offset:18496\n"
      "buffer_load_dwordx4 a[88:91], v172, s[32:35], s66 offen offset:3072 sc1\n"
      "v_mfma_f32_16x16x32_bf16 v[20:23], v[132:135], a[16:19], v[20:23]\n"
      "ds_read_b64_tr_b16 v[130:131], v168 offset:19264\n"
      "v_mfma_f32_16x16x32_bf16 v[44:47], v[132:135], a[20:23], v[44:47]\n"
      "ds_read_b64_tr_b16 v[132:133], v169 offset:18496\n"
      "s_waitcnt lgkmcnt(11)\n"
      "v_mfma_f32_16x16x32_bf16 v[24:27], v[136:139], a[16:19], v[24:27]\n"
      "ds_read_b64_tr_b16 v[134:135], v169 offset:19264\n"
      "v_mfma_f32_16x16x32_bf16 v[48:51], v[136:139], a[20:23], v[48:51]\n"
      "ds_read_b64_tr_b16 v[136:137], v168 offset:18560\n"
      "buffer_load_dwordx4 a[92:95], v173, s[32:35], s66 offen offset:3072 sc1\n"
      "v_mfma_f32_16x16x32_bf16 v[28:31], v[140:143], a[16:19], v[28:31]\n"
      "ds_read_b64_tr_b16 v[138:139], v168 offset:19328\n"
      "v_mfma_f32_16x16x32_bf16 v[52:55], v[140:143], a[20:23], v[52:55]\n"
      "ds_read_b64_tr_b16 v[140:141], v169 offset:18560\n"
      "s_waitcnt lgkmcnt(11)\n"
      "v_mfma_f32_16x16x32_bf16 v[32:35], v[112:115], a[16:19], v[32:35]\n"
      "ds_read_b64_tr_b16 v[142:143], v169 offset:19328\n"
      "v_mfma_f32_16x16x32_bf16 v[56:59], v[112:115], a[20:23], v[56:59]\n"
      "ds_read_b64_tr_b16 v[112:113], v168 offset:24576\n"
      "buffer_load_dwordx4 a[96:99], v172, s[32:35], s67 offen offset:0 sc1\n"
      "v_mfma_f32_16x16x32_bf16 v[36:39], v[116:119], a[16:19], v[36:39]\n"
      "ds_read_b64_tr_b16 v[114:115], v168 offset:25344\n"
      "v_mfma_f32_16x16x32_bf16 v[60:63], v[116:119], a[20:23], v[60:63]\n"
      "ds_read_b64_tr_b16 v[116:117], v169 offset:24576\n"
      "s_waitcnt lgkmcnt(11)\n"
      "s_waitcnt vmcnt(18)\n"
      "v_mfma_f32_16x16x32_bf16 v[16:19], v[120:123], a[24:27], v[16:19]\n"
      "ds_read_b64_tr_b16 v[118:119], v169 offset:25344\n"
      "s_waitcnt vmcnt(17)\n"
      "v_mfma_f32_16x16x32_bf16 v[40:43], v[120:123], a[28:31], v[40:43]\n"
      "ds_read_b64_tr_b16 v[120:121], v168 offset:24640\n"
      "buffer_load_dwordx4 a[100:103], v173, s[32:35], s67 offen offset:0 sc1\n"
      "v_mfma_f32_16x16x32_bf16 v[20:23], v[124:127], a[24:27], v[20:23]\n"
      "ds_read_b64_tr_b16 v[122:123], v168 offset:25408\n"
      "v_mfma_f32_16x16x32_bf16 v[44:47], v[124:127], a[28:31], v[44:47]\n"
      "ds_read_b64_tr_b16 v[124:125], v169 offset:24640\n"
      "s_waitcnt lgkmcnt(11)\n"
      "v_mfma_f32_16x16x32_bf16 v[24:27], v[128:131], a[24:27], v[24:27]\n"
      "ds_read_b64_tr_b16 v[126:127], v169 offset:25408\n"
      "v_mfma_f32_16x16x32_bf16 v[48:51], v[128:131], a[28:31], v[48:51]\n"
      "ds_read_b64_tr_b16 v[128:129], v168 offset:24704\n"
      "buffer_load_dwordx4 a[104:107], v172, s[32:35], s67 offen offset:1024 sc1\n"
      "v_mfma_f32_16x16x32_bf16 v[28:31], v[132:135], a[24:27], v[28:31]\n"
      "ds_read_b64_tr_b16 v[130:131], v168 offset:25472\n"
      "v_mfma_f32_16x16x32_bf16 v[52:55], v[132:135], a[28:31], v[52:55]\n"
      "ds_read_b64_tr_b16 v[132:133], v169 offset:24704\n"
      "s_waitcnt lgkmcnt(11)\n"
      "v_mfma_f32_16x16x32_bf16 v[32:35], v[136:139], a[24:27], v[32:35]\n"
      "ds_read_b64_tr_b16 v[134:135], v169 offset:25472\n"
      "v_mfma_f32_16x16x32_bf16 v[56:59], v[136:139], a[28:31], v[56:59]\n"
      "ds_read_b64_tr_b16 v[136:137], v168 offset:30720\n"
      "buffer_load_dwordx4 a[108:111], v173, s[32:35], s67 offen offset:1024 sc1\n"
      "v_mfma_f32_16x16x32_bf16 v[36:39], v[140:143], a[24:27], v[36:39]\n"
      "ds_read_b64_tr_b16 v[138:139], v168 offset:31488\n"
      "v_mfma_f32_16x16x32_bf16 v[60:63], v[140:143], a[28:31], v[60:63]\n"
      "ds_read_b64_tr_b16 v[140:141], v169 offset:30720\n"
      "s_waitcnt lgkmcnt(11)\n"
      "s_waitcnt vmcnt(19)\n"
      "v_mfma_f32_16x16x32_bf16 v[16:19], v[112:115], a[32:35], v[16:19]\n"
      "ds_read_b64_tr_b16 v[142:143], v169 offset:31488\n"
      "s_waitcnt vmcnt(18)\n"
      "v_mfma_f32_16x16x32_bf16 v[40:43], v[112:115], a[36:39], v[40:43]\n"
      "ds_read_b64_tr_b16 v[112:113], v168 offset:30784\n"
      "buffer_load_dwordx4 a[112:115], v172, s[32:35], s67 offen offset:2048 sc1\n"
      "v_mfma_f32_16x16x32_bf16 v[20:23], v[116:119], a[32:35], v[20:23]\n"
      "ds_read_b64_tr_b16 v[114:115], v168 offset:31552\n"
      "v_mfma_f32_16x16x32_bf16 v[44:47], v[116:119], a[36:39], v[44:47]\n"
      "ds_read_b64_tr_b16 v[116:117], v169 offset:30784\n"
      "s_waitcnt lgkmcnt(11)\n"
      "v_mfma_f32_16x16x32_bf16 v[24:27], v[120:123], a[32:35], v[24:27]\n"
      "ds_read_b64_tr_b16 v[118:119], v169 offset:31552\n"
      "v_mfma_f32_16x16x32_bf16 v[48:51], v[120:123], a[36:39], v[48:51]\n"
      "ds_read_b64_tr_b16 v[120:121], v168 offset:30848\n"
      "buffer_load_dwordx4 a[116:119], v173, s[32:35], s67 offen offset:2048 sc1\n"
      "v_mfma_f32_16x16x32_bf16 v[28:31], v[124:127], a[32:35], v[28:31]\n"
      "ds_read_b64_tr_b16 v[122:123], v168 offset:31616\n"
      "v_mfma_f32_16x16x32_bf16 v[52:55], v[124:127], a[36:39], v[52:55]\n"
      "ds_read_b64_tr_b16 v[124:125], v169 offset:30848\n"
      "s_waitcnt lgkmcnt(11)\n"
      "v_mfma_f32_16x16x32_bf16 v[32:35], v[128:131], a[32:35], v[32:35]\n"
      "ds_read_b64_tr_b16 v[126:127], v169 offset:31616\n"
      "v_mfma_f32_16x16x32_bf16 v[56:59], v[128:131], a[36:39], v[56:59]\n"
      "ds_read_b64_tr_b16 v[128:129], v168 offset:36864\n"
      "buffer_load_dwordx4 a[120:123], v172, s[32:35], s67 offen offset:3072 sc1\n"
      "v_mfma_f32_16x16x32_bf16 v[36:39], v[132:135], a[32:35], v[36:39]\n"
      "ds_read_b64_tr_b16 v[130:131], v168 offset:37632\n"
      "v_mfma_f32_16x16x32_bf16 v[60:63], v[132:135], a[36:39], v[60:63]\n"
      "ds_read_b64_tr_b16 v[132:133], v169 offset:36864\n"
      "s_waitcnt lgkmcnt(11)\n"
      "s_waitcnt vmcnt(20)\n"
      "v_mfma_f32_16x16x32_bf16 v[16:19], v[136:139], a[40:43], v[16:19]\n"
      "ds_read_b64_tr_b16 v[134:135], v169 offset:37632\n"
      "s_waitcnt vmcnt(19)\n"
      "v_mfma_f32_16x16x32_bf16 v[40:43], v[136:139], a[44:47], v[40:43]\n"
      "ds_read_b64_tr_b16 v[136:137], v168 offset:36928\n"
      "buffer_load_dwordx4 a[124:127], v173, s[32:35], s67 offen offset:3072 sc1\n"
      "v_mfma_f32_16x16x32_bf16 v[20:23], v[140:143], a[40:43], v[20:23]\n"
      "ds_read_b64_tr_b16 v[138:139], v168 offset:37696\n"
      "v_mfma_f32_16x16x32_bf16 v[44:47], v[140:143], a[44:47], v[44:47]\n"
      "ds_read_b64_tr_b16 v[140:141], v169 offset:36928\n"
      "s_waitcnt lgkmcnt(11)\n"
      "v_mfma_f32_16x16x32_bf16 v[24:27], v[112:115], a[40:43], v[24:27]\n"
      "ds_read_b64_tr_b16 v[142:143], v169 offset:37696\n"
      "v_mfma_f32_16x16x32_bf16 v[48:51], v[112:115], a[44:47], v[48:51]\n"
      "ds_read_b64_tr_b16 v[112:113], v168 offset:36992\n"
      "buffer_load_dwordx4 a[128:131], v172, s[32:35], s68 offen offset:0 sc1\n"
      "v_mfma_f32_16x16x32_bf16 v[28:31], v[116:119], a[40:43], v[28:31]\n"
      "ds_read_b64_tr_b16 v[114:115], v168 offset:37760\n"
      "v_mfma_f32_16x16x32_bf16 v[52:55], v[116:119], a[44:47], v[52:55]\n"
      "ds_read_b64_tr_b16 v[116:117], v169 offset:36992\n"
      "s_waitcnt lgkmcnt(11)\n"
      "v_mfma_f32_16x16x32_bf16 v[32:35], v[120:123], a[40:43], v[32:35]\n"
      "ds_read_b64_tr_b16 v[118:119], v169 offset:37760\n"
      "v_mfma_f32_16x16x32_bf16 v[56:59], v[120:123], a[44:47], v[56:59]\n"
      "ds_read_b64_tr_b16 v[120:121], v168 offset:43008\n"
      "buffer_load_dwordx4 a[132:135], v173, s[32:35], s68 offen offset:0 sc1\n"
      "v_mfma_f32_16x16x32_bf16 v[36:39], v[124:127], a[40:43], v[36:39]\n"
      "ds_read_b64_tr_b16 v[122:123], v168 offset:43776\n"
      "v_mfma_f32_16x16x32_bf16 v[60:63], v[124:127], a[44:47], v[60:63]\n"
      "ds_read_b64_tr_b16 v[124:125], v169 offset:43008\n"
      "s_waitcnt lgkmcnt(11)\n"
      "s_waitcnt vmcnt(21)\n"
      "v_mfma_f32_16x16x32_bf16 v[16:19], v[128:131], a[48:51], v[16:19]\n"
      "ds_read_b64_tr_b16 v[126:127], v169 offset:43776\n"
      "s_waitcnt vmcnt(20)\n"
      "v_mfma_f32_16x16x32_bf16 v[40:43], v[128:131], a[52:55], v[40:43]\n"
      "ds_read_b64_tr_b16 v[128:129], v168 offset:43072\n"
      "buffer_load_dwordx4 a[136:139], v172, s[32:35], s68 offen offset:1024 sc1\n"
      "v_mfma_f32_16x16x32_bf16 v[20:23], v[132:135], a[48:51], v[20:23]\n"
      "ds_read_b64_tr_b16 v[130:131], v168 offset:43840\n"
      "v_mfma_f32_16x16x32_bf16 v[44:47], v[132:135], a[52:55], v[44:47]\n"
      "ds_read_b64_tr_b16 v[132:133], v169 offset:43072\n"
      "s_waitcnt lgkmcnt(11)\n"
      "v_mfma_f32_16x16x32_bf16 v[24:27], v[136:139], a[48:51], v[24:27]\n"
      "ds_read_b64_tr_b16 v[134:135], v169 offset:43840\n"
      "v_mfma_f32_16x16x32_bf16 v[48:51], v[136:139], a[52:55], v[48:51]\n"
      "ds_read_b64_tr_b16 v[136:137], v168 offset:43136\n"
      "buffer_load_dwordx4 a[140:143], v173, s[32:35], s68 offen offset:1024 sc1\n"
      "v_mfma_f32_16x16x32_bf16 v[28:31], v[140:143], a[48:51], v[28:31]\n"
      "ds_read_b64_tr_b16 v[138:139], v168 offset:43904\n"
      "v_mfma_f32_16x16x32_bf16 v[52:55], v[140:143], a[52:55], v[52:55]\n"
      "ds_read_b64_tr_b16 v[140:141], v169 offset:43136\n"
      "s_waitcnt lgkmcnt(11)\n"
      "v_mfma_f32_16x16x32_bf16 v[32:35], v[112:115], a[48:51], v[32:35]\n"
      "ds_read_b64_tr_b16 v[142:143], v169 offset:43904\n"
      "v_mfma_f32_16x16x32_bf16 v[56:59], v[112:115], a[52:55], v[56:59]\n"
      "ds_read_b64_tr_b16 v[112:113], v168 offset:49152\n"
      "buffer_load_dwordx4 a[144:147], v172, s[32:35], s68 offen offset:2048 sc1\n"
      "v_mfma_f32_16x16x32_bf16 v[36:39], v[116:119], a[48:51], v[36:39]\n"
      "ds_read_b64_tr_b16 v[114:115], v168 offset:49920\n"
      "v_mfma_f32_16x16x32_bf16 v[60:63], v[116:119], a[52:55], v[60:63]\n"
      "ds_read_b64_tr_b16 v[116:117], v169 offset:49152\n"
      "s_waitcnt lgkmcnt(11)\n"
      "s_waitcnt vmcnt(22)\n"
      "v_mfma_f32_16x16x32_bf16 v[16:19], v[120:123], a[56:59], v[16:19]\n"
      "ds_read_b64_tr_b16 v[118:119], v169 offset:49920\n"
      "s_waitcnt vmcnt(21)\n"
      "v_mfma_f32_16x16x32_bf16 v[40:43], v[120:123], a[60:63], v[40:43]\n"
      "ds_read_b64_tr_b16 v[120:121], v168 offset:49216\n"
      "buffer_load_dwordx4 a[148:151], v173, s[32:35], s68 offen offset:2048 sc1\n"
      "v_mfma_f32_16x16x32_bf16 v[20:23], v[124:127], a[56:59], v[20:23]\n"
      "ds_read_b64_tr_b16 v[122:123], v168 offset:49984\n"
      "v_mfma_f32_16x16x32_bf16 v[44:47], v[124:127], a[60:63], v[44:47]\n"
      "ds_read_b64_tr_b16 v[124:125], v169 offset:49216\n"
      "s_waitcnt lgkmcnt(11)\n"
      "v_mfma_f32_16x16x32_bf16 v[24:27], v[128:131], a[56:59], v[24:27]\n"
      "ds_read_b64_tr_b16 v[126:127], v169 offset:49984\n"
      "v_mfma_f32_16x16x32_bf16 v[48:51], v[128:131], a[60:63], v[48:51]\n"
      "ds_read_b64_tr_b16 v[128:129], v168 offset:49280\n"
      "buffer_load_dwordx4 a[152:155], v172, s[32:35], s68 offen offset:3072 sc1\n"
      "v_mfma_f32_16x16x32_bf16 v[28:31], v[132:135], a[56:59], v[28:31]\n"
      "ds_read_b64_tr_b16 v[130:131], v168 offset:50048\n"
      "v_mfma_f32_16x16x32_bf16 v[52:55], v[132:135], a[60:63], v[52:55]\n"
      "ds_read_b64_tr_b16 v[132:133], v169 offset:49280\n"
      "s_waitcnt lgkmcnt(11)\n"
      "v_mfma_f32_16x16x32_bf16 v[32:35], v[136:139], a[56:59], v[32:35]\n"
      "ds_read_b64_tr_b16 v[134:135], v169 offset:50048\n"
      "v_mfma_f32_16x16x32_bf16 v[56:59], v[136:139], a[60:63], v[56:59]\n"
      "ds_read_b64_tr_b16 v[136:137], v168 offset:55296\n"
      "buffer_load_dwordx4 a[156:159], v173, s[32:35], s68 offen offset:3072 sc1\n"
      "v_mfma_f32_16x16x32_bf16 v[36:39], v[140:143], a[56:59], v[36:39]\n"
      "ds_read_b64_tr_b16 v[138:139], v168 offset:56064\n"
      "v_mfma_f32_16x16x32_bf16 v[60:63], v[140:143], a[60:63], v[60:63]\n"
      "ds_read_b64_tr_b16 v[140:141], v169 offset:55296\n"
      "s_waitcnt lgkmcnt(11)\n"
      "s_waitcnt vmcnt(23)\n"
      "v_mfma_f32_16x16x32_bf16 v[16:19], v[112:115], a[64:67], v[16:19]\n"
      "ds_read_b64_tr_b16 v[142:143], v169 offset:56064\n"
      "s_waitcnt vmcnt(22)\n"
      "v_mfma_f32_16x16x32_bf16 v[40:43], v[112:115], a[68:71], v[40:43]\n"
      "ds_read_b64_tr_b16 v[112:113], v168 offset:55360\n"
      "buffer_load_dwordx4 a[0:3], v174, s[32:35], 0 offen offset:0 sc1\n"
      "v_mfma_f32_16x16x32_bf16 v[20:23], v[116:119], a[64:67], v[20:23]\n"
      "ds_read_b64_tr_b16 v[114:115], v168 offset:56128\n"
      "v_mfma_f32_16x16x32_bf16 v[44:47], v[116:119], a[68:71], v[44:47]\n"
      "ds_read_b64_tr_b16 v[116:117], v169 offset:55360\n"
      "s_waitcnt lgkmcnt(11)\n"
      "v_mfma_f32_16x16x32_bf16 v[24:27], v[120:123], a[64:67], v[24:27]\n"
      "ds_read_b64_tr_b16 v[118:119], v169 offset:56128\n"
      "v_mfma_f32_16x16x32_bf16 v[48:51], v[120:123], a[68:71], v[48:51]\n"
      "ds_read_b64_tr_b16 v[120:121], v168 offset:55424\n"
      "buffer_load_dwordx4 a[4:7], v175, s[32:35], 0 offen offset:0 sc1\n"
      "v_mfma_f32_16x16x32_bf16 v[28:31], v[124:127], a[64:67], v[28:31]\n"
      "ds_read_b64_tr_b16 v[122:123], v168 offset:56192\n"
      "v_mfma_f32_16x16x32_bf16 v[52:55], v[124:127], a[68:71], v[52:55]\n"
      "ds_read_b64_tr_b16 v[124:125], v169 offset:55424\n"
      "s_waitcnt lgkmcnt(11)\n"
      "v_mfma_f32_16x16x32_bf16 v[32:35], v[128:131], a[64:67], v[32:35]\n"
      "ds_read_b64_tr_b16 v[126:127], v169 offset:56192\n"
      "v_mfma_f32_16x16x32_bf16 v[56:59], v[128:131], a[68:71], v[56:59]\n"
      "ds_read_b64_tr_b16 v[128:129], v170 offset:0\n"
      "buffer_load_dwordx4 a[8:11], v174, s[32:35], 0 offen offset:1024 sc1\n"
      "v_mfma_f32_16x16x32_bf16 v[36:39], v[132:135], a[64:67], v[36:39]\n"
      "ds_read_b64_tr_b16 v[130:131], v170 offset:768\n"
      "v_mfma_f32_16x16x32_bf16 v[60:63], v[132:135], a[68:71], v[60:63]\n"
      "ds_read_b64_tr_b16 v[132:133], v171 offset:0\n"
      "s_waitcnt lgkmcnt(11)\n"
      "s_waitcnt vmcnt(24)\n"
      "v_mfma_f32_16x16x32_bf16 v[16:19], v[136:139], a[72:75], v[16:19]\n"
      "ds_read_b64_tr_b16 v[134:135], v171 offset:768\n"
      "s_waitcnt vmcnt(23)\n"
      "v_mfma_f32_16x16x32_bf16 v[40:43], v[136:139], a[76:79], v[40:43]\n"
      "ds_read_b64_tr_b16 v[136:137], v170 offset:64\n"
      "buffer_load_dwordx4 a[12:15], v175, s[32:35], 0 offen offset:1024 sc1\n"
      "v_mfma_f32_16x16x32_bf16 v[20:23], v[140:143], a[72:75], v[20:23]\n"
      "ds_read_b64_tr_b16 v[138:139], v170 offset:832\n"
      "v_mfma_f32_16x16x32_bf16 v[44:47], v[140:143], a[76:79], v[44:47]\n"
      "ds_read_b64_tr_b16 v[140:141], v171 offset:64\n"
      "s_waitcnt lgkmcnt(11)\n"
      "v_mfma_f32_16x16x32_bf16 v[24:27], v[112:115], a[72:75], v[24:27]\n"
      "ds_read_b64_tr_b16 v[142:143], v171 offset:832\n"
      "v_mfma_f32_16x16x32_bf16 v[48:51], v[112:115], a[76:79], v[48:51]\n"
      "ds_read_b64_tr_b16 v[112:113], v170 offset:128\n"
      "buffer_load_dwordx4 a[16:19], v174, s[32:35], 0 offen offset:2048 sc1\n"
      "v_mfma_f32_16x16x32_bf16 v[28:31], v[116:119], a[72:75], v[28:31]\n"
      "ds_read_b64_tr_b16 v[114:115], v170 offset:896\n"
      "v_mfma_f32_16x16x32_bf16 v[52:55], v[116:119], a[76:79], v[52:55]\n"
      "ds_read_b64_tr_b16 v[116:117], v171 offset:128\n"
      "s_waitcnt lgkmcnt(11)\n"
      "v_mfma_f32_16x16x32_bf16 v[32:35], v[120:123], a[72:75], v[32:35]\n"
      "ds_read_b64_tr_b16 v[118:119], v171 offset:896\n"
      "v_mfma_f32_16x16x32_bf16 v[56:59], v[120:123], a[76:79], v[56:59]\n"
      "ds_read_b64_tr_b16 v[120:121], v170 offset:6144\n"
      "buffer_load_dwordx4 a[20:23], v175, s[32:35], 0 offen offset:2048 sc1\n"
      "v_mfma_f32_16x16x32_bf16 v[36:39], v[124:127], a[72:75], v[36:39]\n"
      "ds_read_b64_tr_b16 v[122:123], v170 offset:6912\n"
      "v_mfma_f32_16x16x32_bf16 v[60:63], v[124:127], a[76:79], v[60:63]\n"
      "ds_read_b64_tr_b16 v[124:125], v171 offset:6144\n"
      "s_waitcnt lgkmcnt(11)\n"
      "s_waitcnt vmcnt(25)\n"
      "v_mfma_f32_16x16x32_bf16 v[16:19], v[128:131], a[80:83], v[16:19]\n"
      "ds_read_b64_tr_b16 v[126:127], v171 offset:6912\n"
      "s_waitcnt vmcnt(24)\n"
      "v_mfma_f32_16x16x32_bf16 v[40:43], v[128:131], a[84:87], v[40:43]\n"
      "ds_read_b64_tr_b16 v[128:129], v170 offset:6208\n"
      "buffer_load_dwordx4 a[24:27], v174, s[32:35], 0 offen offset:3072 sc1\n"
      "v_mfma_f32_16x16x32_bf16 v[20:23], v[132:135], a[80:83], v[20:23]\n"
      "ds_read_b64_tr_b16 v[130:131], v170 offset:6976\n"
      "v_mfma_f32_16x16x32_bf16 v[44:47], v[132:135], a[84:87], v[44:47]\n"
      "ds_read_b64_tr_b16 v[132:133], v171 offset:6208\n"
      "s_waitcnt lgkmcnt(11)\n"
      "v_mfma_f32_16x16x32_bf16 v[24:27], v[136:139], a[80:83], v[24:27]\n"
      "ds_read_b64_tr_b16 v[134:135], v171 offset:6976\n"
      "v_mfma_f32_16x16x32_bf16 v[48:51], v[136:139], a[84:87], v[48:51]\n"
      "ds_read_b64_tr_b16 v[136:137], v170 offset:6272\n"
      "buffer_load_dwordx4 a[28:31], v175, s[32:35], 0 offen offset:3072 sc1\n"
      "v_mfma_f32_16x16x32_bf16 v[28:31], v[140:143], a[80:83], v[28:31]\n"
      "ds_read_b64_tr_b16 v[138:139], v170 offset:7040\n"
      "v_mfma_f32_16x16x32_bf16 v[52:55], v[140:143], a[84:87], v[52:55]\n"
      "ds_read_b64_tr_b16 v[140:141], v171 offset:6272\n"
      "s_waitcnt lgkmcnt(11)\n"
      "v_mfma_f32_16x16x32_bf16 v[32:35], v[112:115], a[80:83], v[32:35]\n"
      "ds_read_b64_tr_b16 v[142:143], v171 offset:7040\n"
      "v_mfma_f32_16x16x32_bf16 v[56:59], v[112:115], a[84:87], v[56:59]\n"
      "ds_read_b64_tr_b16 v[112:113], v170 offset:12288\n"
      "buffer_load_dwordx4 a[32:35], v174, s[32:35], s65 offen offset:0 sc1\n"
      "v_mfma_f32_16x16x32_bf16 v[36:39], v[116:119], a[80:83], v[36:39]\n"
      "ds_read_b64_tr_b16 v[114:115], v170 offset:13056\n"
      "v_mfma_f32_16x16x32_bf16 v[60:63], v[116:119], a[84:87], v[60:63]\n"
      "ds_read_b64_tr_b16 v[116:117], v171 offset:12288\n"
      "s_waitcnt lgkmcnt(11)\n"
      "s_waitcnt vmcnt(26)\n"
      "v_mfma_f32_16x16x32_bf16 v[16:19], v[120:123], a[88:91], v[16:19]\n"
      "ds_read_b64_tr_b16 v[118:119], v171 offset:13056\n"
      "s_waitcnt vmcnt(25)\n"
      "v_mfma_f32_16x16x32_bf16 v[40:43], v[120:123], a[92:95], v[40:43]\n"
      "ds_read_b64_tr_b16 v[120:121], v170 offset:12352\n"
      "buffer_load_dwordx4 a[36:39], v175, s[32:35], s65 offen offset:0 sc1\n"
      "v_mfma_f32_16x16x32_bf16 v[20:23], v[124:127], a[88:91], v[20:23]\n"
      "ds_read_b64_tr_b16 v[122:123], v170 offset:13120\n"
      "v_mfma_f32_16x16x32_bf16 v[44:47], v[124:127], a[92:95], v[44:47]\n"
      "ds_read_b64_tr_b16 v[124:125], v171 offset:12352\n"
      "s_waitcnt lgkmcnt(11)\n"
      "v_mfma_f32_16x16x32_bf16 v[24:27], v[128:131], a[88:91], v[24:27]\n"
      "ds_read_b64_tr_b16 v[126:127], v171 offset:13120\n"
      "v_mfma_f32_16x16x32_bf16 v[48:51], v[128:131], a[92:95], v[48:51]\n"
      "ds_read_b64_tr_b16 v[128:129], v170 offset:12416\n"
      "buffer_load_dwordx4 a[40:43], v174, s[32:35], s65 offen offset:1024 sc1\n"
      "v_mfma_f32_16x16x32_bf16 v[28:31], v[132:135], a[88:91], v[28:31]\n"
      "ds_read_b64_tr_b16 v[130:131], v170 offset:13184\n"
      "v_mfma_f32_16x16x32_bf16 v[52:55], v[132:135], a[92:95], v[52:55]\n"
      "ds_read_b64_tr_b16 v[132:133], v171 offset:12416\n"
      "s_waitcnt lgkmcnt(11)\n"
      "v_mfma_f32_16x16x32_bf16 v[32:35], v[136:139], a[88:91], v[32:35]\n"
      "ds_read_b64_tr_b16 v[134:135], v171 offset:13184\n"
      "v_mfma_f32_16x16x32_bf16 v[56:59], v[136:139], a[92:95], v[56:59]\n"
      "ds_read_b64_tr_b16 v[136:137], v170 offset:18432\n"
      "buffer_load_dwordx4 a[44:47], v175, s[32:35], s65 offen offset:1024 sc1\n"
      "v_mfma_f32_16x16x32_bf16 v[36:39], v[140:143], a[88:91], v[36:39]\n"
      "ds_read_b64_tr_b16 v[138:139], v170 offset:19200\n"
      "v_mfma_f32_16x16x32_bf16 v[60:63], v[140:143], a[92:95], v[60:63]\n"
      "ds_read_b64_tr_b16 v[140:141], v171 offset:18432\n"
      "s_waitcnt lgkmcnt(11)\n"
      "s_waitcnt vmcnt(27)\n"
      "v_mfma_f32_16x16x32_bf16 v[16:19], v[112:115], a[96:99], v[16:19]\n"
      "ds_read_b64_tr_b16 v[142:143], v171 offset:19200\n"
      "s_waitcnt vmcnt(26)\n"
      "v_mfma_f32_16x16x32_bf16 v[40:43], v[112:115], a[100:103], v[40:43]\n"
      "ds_read_b64_tr_b16 v[112:113], v170 offset:18496\n"
      "buffer_load_dwordx4 a[48:51], v174, s[32:35], s65 offen offset:2048 sc1\n"
      "v_mfma_f32_16x16x32_bf16 v[20:23], v[116:119], a[96:99], v[20:23]\n"
      "ds_read_b64_tr_b16 v[114:115], v170 offset:19264\n"
      "v_mfma_f32_16x16x32_bf16 v[44:47], v[116:119], a[100:103], v[44:47]\n"
      "ds_read_b64_tr_b16 v[116:117], v171 offset:18496\n"
      "s_waitcnt lgkmcnt(11)\n"
      "v_mfma_f32_16x16x32_bf16 v[24:27], v[120:123], a[96:99], v[24:27]\n"
      "ds_read_b64_tr_b16 v[118:119], v171 offset:19264\n"
      "v_mfma_f32_16x16x32_bf16 v[48:51], v[120:123], a[100:103], v[48:51]\n"
      "ds_read_b64_tr_b16 v[120:121], v170 offset:18560\n"
      "buffer_load_dwordx4 a[52:55], v175, s[32:35], s65 offen offset:2048 sc1\n"
      "v_mfma_f32_16x16x32_bf16 v[28:31], v[124:127], a[96:99], v[28:31]\n"
      "ds_read_b64_tr_b16 v[122:123], v170 offset:19328\n"
      "v_mfma_f32_16x16x32_bf16 v[52:55], v[124:127], a[100:103], v[52:55]\n"
      "ds_read_b64_tr_b16 v[124:125], v171 offset:18560\n"
      "s_waitcnt lgkmcnt(11)\n"
      "v_mfma_f32_16x16x32_bf16 v[32:35], v[128:131], a[96:99], v[32:35]\n"
      "ds_read_b64_tr_b16 v[126:127], v171 offset:19328\n"
      "v_mfma_f32_16x16x32_bf16 v[56:59], v[128:131], a[100:103], v[56:59]\n"
      "ds_read_b64_tr_b16 v[128:129], v170 offset:24576\n"
      "buffer_load_dwordx4 a[56:59], v174, s[32:35], s65 offen offset:3072 sc1\n"
      "v_mfma_f32_16x16x32_bf16 v[36:39], v[132:135], a[96:99], v[36:39]\n"
      "ds_read_b64_tr_b16 v[130:131], v170 offset:25344\n"
      "v_mfma_f32_16x16x32_bf16 v[60:63], v[132:135], a[100:103], v[60:63]\n"
      "ds_read_b64_tr_b16 v[132:133], v171 offset:24576\n"
      "s_waitcnt lgkmcnt(11)\n"
      "s_waitcnt vmcnt(28)\n"
      "v_mfma_f32_16x16x32_bf16 v[16:19], v[136:139], a[104:107], v[16:19]\n"
      "ds_read_b64_tr_b16 v[134:135], v171 offset:25344\n"
      "s_waitcnt vmcnt(27)\n"
      "v_mfma_f32_16x16x32_bf16 v[40:43], v[136:139], a[108:111], v[40:43]\n"
      "ds_read_b64_tr_b16 v[136:137], v170 offset:24640\n"
      "buffer_load_dwordx4 a[60:63], v175, s[32:35], s65 offen offset:3072 sc1\n"
      "v_mfma_f32_16x16x32_bf16 v[20:23], v[140:143], a[104:107], v[20:23]\n"
      "ds_read_b64_tr_b16 v[138:139], v170 offset:25408\n"
      "v_mfma_f32_16x16x32_bf16 v[44:47], v[140:143], a[108:111], v[44:47]\n"
      "ds_read_b64_tr_b16 v[140:141], v171 offset:24640\n"
      "s_waitcnt lgkmcnt(11)\n"
      "v_mfma_f32_16x16x32_bf16 v[24:27], v[112:115], a[104:107], v[24:27]\n"
      "ds_read_b64_tr_b16 v[142:143], v171 offset:25408\n"
      "v_mfma_f32_16x16x32_bf16 v[48:51], v[112:115], a[108:111], v[48:51]\n"
      "ds_read_b64_tr_b16 v[112:113], v170 offset:24704\n"
      "buffer_load_dwordx4 a[64:67], v174, s[32:35], s66 offen offset:0 sc1\n"
      "v_mfma_f32_16x16x32_bf16 v[28:31], v[116:119], a[104:107], v[28:31]\n"
      "ds_read_b64_tr_b16 v[114:115], v170 offset:25472\n"
      "v_mfma_f32_16x16x32_bf16 v[52:55], v[116:119], a[108:111], v[52:55]\n"
      "ds_read_b64_tr_b16 v[116:117], v171 offset:24704\n"
      "s_waitcnt lgkmcnt(11)\n"
      "v_mfma_f32_16x16x32_bf16 v[32:35], v[120:123], a[104:107], v[32:35]\n"
      "ds_read_b64_tr_b16 v[118:119], v171 offset:25472\n"
      "v_mfma_f32_16x16x32_bf16 v[56:59], v[120:123], a[108:111], v[56:59]\n"
      "ds_read_b64_tr_b16 v[120:121], v170 offset:30720\n"
      "buffer_load_dwordx4 a[68:71], v175, s[32:35], s66 offen offset:0 sc1\n"
      "v_mfma_f32_16x16x32_bf16 v[36:39], v[124:127], a[104:107], v[36:39]\n"
      "ds_read_b64_tr_b16 v[122:123], v170 offset:31488\n"
      "v_mfma_f32_16x16x32_bf16 v[60:63], v[124:127], a[108:111], v[60:63]\n"
      "ds_read_b64_tr_b16 v[124:125], v171 offset:30720\n"
      "s_waitcnt lgkmcnt(11)\n"
      "s_waitcnt vmcnt(29)\n"
      "v_mfma_f32_16x16x32_bf16 v[16:19], v[128:131], a[112:115], v[16:19]\n"
      "ds_read_b64_tr_b16 v[126:127], v171 offset:31488\n"
      "s_waitcnt vmcnt(28)\n"
      "v_mfma_f32_16x16x32_bf16 v[40:43], v[128:131], a[116:119], v[40:43]\n"
      "ds_read_b64_tr_b16 v[128:129], v170 offset:30784\n"
      "buffer_load_dwordx4 a[72:75], v174, s[32:35], s66 offen offset:1024 sc1\n"
      "v_mfma_f32_16x16x32_bf16 v[20:23], v[132:135], a[112:115], v[20:23]\n"
      "ds_read_b64_tr_b16 v[130:131], v170 offset:31552\n"
      "v_mfma_f32_16x16x32_bf16 v[44:47], v[132:135], a[116:119], v[44:47]\n"
      "ds_read_b64_tr_b16 v[132:133], v171 offset:30784\n"
      "s_waitcnt lgkmcnt(11)\n"
      "v_mfma_f32_16x16x32_bf16 v[24:27], v[136:139], a[112:115], v[24:27]\n"
      "ds_read_b64_tr_b16 v[134:135], v171 offset:31552\n"
      "v_mfma_f32_16x16x32_bf16 v[48:51], v[136:139], a[116:119], v[48:51]\n"
      "ds_read_b64_tr_b16 v[136:137], v170 offset:30848\n"
      "buffer_load_dwordx4 a[76:79], v175, s[32:35], s66 offen offset:1024 sc1\n"
      "v_mfma_f32_16x16x32_bf16 v[28:31], v[140:143], a[112:115], v[28:31]\n"
      "ds_read_b64_tr_b16 v[138:139], v170 offset:31616\n"
      "v_mfma_f32_16x16x32_bf16 v[52:55], v[140:143], a[116:119], v[52:55]\n"
      "ds_read_b64_tr_b16 v[140:141], v171 offset:30848\n"
      "s_waitcnt lgkmcnt(11)\n"
      "v_mfma_f32_16x16x32_bf16 v[32:35], v[112:115], a[112:115], v[32:35]\n"
      "ds_read_b64_tr_b16 v[142:143], v171 offset:31616\n"
      "v_mfma_f32_16x16x32_bf16 v[56:59], v[112:115], a[116:119], v[56:59]\n"
      "ds_read_b64_tr_b16 v[112:113], v170 offset:36864\n"
      "buffer_load_dwordx4 a[80:83], v174, s[32:35], s66 offen offset:2048 sc1\n"
      "v_mfma_f32_16x16x32_bf16 v[36:39], v[116:119], a[112:115], v[36:39]\n"
      "ds_read_b64_tr_b16 v[114:115], v170 offset:37632\n"
      "v_mfma_f32_16x16x32_bf16 v[60:63], v[116:119], a[116:119], v[60:63]\n"
      "ds_read_b64_tr_b16 v[116:117], v171 offset:36864\n"
      "s_waitcnt lgkmcnt(11)\n"
      "s_waitcnt vmcnt(30)\n"
      "v_mfma_f32_16x16x32_bf16 v[16:19], v[120:123], a[120:123], v[16:19]\n"
      "ds_read_b64_tr_b16 v[118:119], v171 offset:37632\n"
      "s_waitcnt vmcnt(29)\n"
      "v_mfma_f32_16x16x32_bf16 v[40:43], v[120:123], a[124:127], v[40:43]\n"
      "ds_read_b64_tr_b16 v[120:121], v170 offset:36928\n"
      "buffer_load_dwordx4 a[84:87], v175, s[32:35], s66 offen offset:2048 sc1\n"
      "v_mfma_f32_16x16x32_bf16 v[20:23], v[124:127], a[120:123], v[20:23]\n"
      "ds_read_b64_tr_b16 v[122:123], v170 offset:37696\n"
      "v_mfma_f32_16x16x32_bf16 v[44:47], v[124:127], a[124:127], v[44:47]\n"
      "ds_read_b64_tr_b16 v[124:125], v171 offset:36928\n"
      "s_waitcnt lgkmcnt(11)\n"
      "v_mfma_f32_16x16x32_bf16 v[24:27], v[128:131], a[120:123], v[24:27]\n"
      "ds_read_b64_tr_b16 v[126:127], v171 offset:37696\n"
      "v_mfma_f32_16x16x32_bf16 v[48:51], v[128:131], a[124:127], v[48:51]\n"
      "ds_read_b64_tr_b16 v[128:129], v170 offset:36992\n"
      "buffer_load_dwordx4 a[88:91], v174, s[32:35], s66 offen offset:3072 sc1\n"
      "v_mfma_f32_16x16x32_bf16 v[28:31], v[132:135], a[120:123], v[28:31]\n"
      "ds_read_b64_tr_b16 v[130:131], v170 offset:37760\n"
      "v_mfma_f32_16x16x32_bf16 v[52:55], v[132:135], a[124:127], v[52:55]\n"
      "ds_read_b64_tr_b16 v[132:133], v171 offset:36992\n"
      "s_waitcnt lgkmcnt(11)\n"
      "v_mfma_f32_16x16x32_bf16 v[32:35], v[136:139], a[120:123], v[32:35]\n"
      "ds_read_b64_tr_b16 v[134:135], v171 offset:37760\n"
      "v_mfma_f32_16x16x32_bf16 v[56:59], v[136:139], a[124:127], v[56:59]\n"
      "ds_read_b64_tr_b16 v[136:137], v170 offset:43008\n"
      "buffer_load_dwordx4 a[92:95], v175, s[32:35], s66 offen offset:3072 sc1\n"
      "v_mfma_f32_16x16x32_bf16 v[36:39], v[140:143], a[120:123], v[36:39]\n"
      "ds_read_b64_tr_b16 v[138:139], v170 offset:43776\n"
      "v_mfma_f32_16x16x32_bf16 v[60:63], v[140:143], a[124:127], v[60:63]\n"
      "ds_read_b64_tr_b16 v[140:141], v171 offset:43008\n"
      "s_waitcnt lgkmcnt(11)\n"
      "s_waitcnt vmcnt(31)\n"
      "v_mfma_f32_16x16x32_bf16 v[16:19], v[112:115], a[128:131], v[16:19]\n"
      "ds_read_b64_tr_b16 v[142:143], v171 offset:43776\n"
      "s_waitcnt vmcnt(30)\n"
      "v_mfma_f32_16x16x32_bf16 v[40:43], v[112:115], a[132:135], v[40:43]\n"
      "ds_read_b64_tr_b16 v[112:113], v170 offset:43072\n"
      "buffer_load_dwordx4 a[96:99], v174, s[32:35], s67 offen offset:0 sc1\n"
      "v_mfma_f32_16x16x32_bf16 v[20:23], v[116:119], a[128:131], v[20:23]\n"
      "ds_read_b64_tr_b16 v[114:115], v170 offset:43840\n"
      "v_mfma_f32_16x16x32_bf16 v[44:47], v[116:119], a[132:135], v[44:47]\n"
      "ds_read_b64_tr_b16 v[116:117], v171 offset:43072\n"
      "s_waitcnt lgkmcnt(11)\n"
      "v_mfma_f32_16x16x32_bf16 v[24:27], v[120:123], a[128:131], v[24:27]\n"
      "ds_read_b64_tr_b16 v[118:119], v171 offset:43840\n"
      "v_mfma_f32_16x16x32_bf16 v[48:51], v[120:123], a[132:135], v[48:51]\n"
      "ds_read_b64_tr_b16 v[120:121], v170 offset:43136\n"
      "buffer_load_dwordx4 a[100:103], v175, s[32:35], s67 offen offset:0 sc1\n"
      "v_mfma_f32_16x16x32_bf16 v[28:31], v[124:127], a[128:131], v[28:31]\n"
      "ds_read_b64_tr_b16 v[122:123], v170 offset:43904\n"
      "v_mfma_f32_16x16x32_bf16 v[52:55], v[124:127], a[132:135], v[52:55]\n"
      "ds_read_b64_tr_b16 v[124:125], v171 offset:43136\n"
      "s_waitcnt lgkmcnt(11)\n"
      "v_mfma_f32_16x16x32_bf16 v[32:35], v[128:131], a[128:131], v[32:35]\n"
      "ds_read_b64_tr_b16 v[126:127], v171 offset:43904\n"
      "v_mfma_f32_16x16x32_bf16 v[56:59], v[128:131], a[132:135], v[56:59]\n"
      "ds_read_b64_tr_b16 v[128:129], v170 offset:49152\n"
      "buffer_load_dwordx4 a[104:107], v174, s[32:35], s67 offen offset:1024 sc1\n"
      "v_mfma_f32_16x16x32_bf16 v[36:39], v[132:135], a[128:131], v[36:39]\n"
      "ds_read_b64_tr_b16 v[130:131], v170 offset:49920\n"
      "v_mfma_f32_16x16x32_bf16 v[60:63], v[132:135], a[132:135], v[60:63]\n"
      "ds_read_b64_tr_b16 v[132:133], v171 offset:49152\n"
      "s_waitcnt lgkmcnt(11)\n"
      "s_waitcnt vmcnt(32)\n"
      "v_mfma_f32_16x16x32_bf16 v[16:19], v[136:139], a[136:139], v[16:19]\n"
      "ds_read_b64_tr_b16 v[134:135], v171 offset:49920\n"
      "s_waitcnt vmcnt(31)\n"
      "v_mfma_f32_16x16x32_bf16 v[40:43], v[136:139], a[140:143], v[40:43]\n"
      "ds_read_b64_tr_b16 v[136:137], v170 offset:49216\n"
      "buffer_load_dwordx4 a[108:111], v175, s[32:35], s67 offen offset:1024 sc1\n"
      "v_mfma_f32_16x16x32_bf16 v[20:23], v[140:143], a[136:139], v[20:23]\n"
      "ds_read_b64_tr_b16 v[138:139], v170 offset:49984\n"
      "v_mfma_f32_16x16x32_bf16 v[44:47], v[140:143], a[140:143], v[44:47]\n"
      "ds_read_b64_tr_b16 v[140:141], v171 offset:49216\n"
      "s_waitcnt lgkmcnt(11)\n"
      "v_mfma_f32_16x16x32_bf16 v[24:27], v[112:115], a[136:139], v[24:27]\n"
      "ds_read_b64_tr_b16 v[142:143], v171 offset:49984\n"
      "v_mfma_f32_16x16x32_bf16 v[48:51], v[112:115], a[140:143], v[48:51]\n"
      "ds_read_b64_tr_b16 v[112:113], v170 offset:49280\n"
      "buffer_load_dwordx4 a[112:115], v174, s[32:35], s67 offen offset:2048 sc1\n"
      "v_mfma_f32_16x16x32_bf16 v[28:31], v[116:119], a[136:139], v[28:31]\n"
      "ds_read_b64_tr_b16 v[114:115], v170 offset:50048\n"
      "v_mfma_f32_16x16x32_bf16 v[52:55], v[116:119], a[140:143], v[52:55]\n"
      "ds_read_b64_tr_b16 v[116:117], v171 offset:49280\n"
      "s_waitcnt lgkmcnt(11)\n"
      "v_mfma_f32_16x16x32_bf16 v[32:35], v[120:123], a[136:139], v[32:35]\n"
      "ds_read_b64_tr_b16 v[118:119], v171 offset:50048\n"
      "s_mov_b32 exec_hi, 0\n"
      "buffer_load_dword v207, v11, s[32:35], s92 offen sc1\n"
      "s_mov_b32 exec_hi, -1\n"
      "v_mfma_f32_16x16x32_bf16 v[56:59], v[120:123], a[140:143], v[56:59]\n"
      "ds_read_b64_tr_b16 v[120:121], v170 offset:55296\n"
      "buffer_load_dwordx4 a[116:119], v175, s[32:35], s67 offen offset:2048 sc1\n"
      "v_mfma_f32_16x16x32_bf16 v[36:39], v[124:127], a[136:139], v[36:39]\n"
      "ds_read_b64_tr_b16 v[122:123], v170 offset:56064\n"
      "v_mfma_f32_16x16x32_bf16 v[60:63], v[124:127], a[140:143], v[60:63]\n"
      "ds_read_b64_tr_b16 v[124:125], v171 offset:55296\n"
      "s_waitcnt lgkmcnt(11)\n"
      "s_waitcnt vmcnt(34)\n"
      "v_mfma_f32_16x16x32_bf16 v[16:19], v[128:131], a[144:147], v[16:19]\n"
      "ds_read_b64_tr_b16 v[126:127], v171 offset:56064\n"
      "s_waitcnt vmcnt(33)\n"
      "v_mfma_f32_16x16x32_bf16 v[40:43], v[128:131], a[148:151], v[40:43]\n"
      "ds_read_b64_tr_b16 v[128:129], v170 offset:55360\n"
      "buffer_load_dwordx4 a[120:123], v174, s[32:35], s67 offen offset:3072 sc1\n"
      "v_mfma_f32_16x16x32_bf16 v[20:23], v[132:135], a[144:147], v[20:23]\n"
      "ds_read_b64_tr_b16 v[130:131], v170 offset:56128\n"
      "v_mfma_f32_16x16x32_bf16 v[44:47], v[132:135], a[148:151], v[44:47]\n"
      "ds_read_b64_tr_b16 v[132:133], v171 offset:55360\n"
      "s_waitcnt lgkmcnt(11)\n"
      "v_mfma_f32_16x16x32_bf16 v[24:27], v[136:139], a[144:147], v[24:27]\n"
      "ds_read_b64_tr_b16 v[134:135], v171 offset:56128\n"
      "v_mfma_f32_16x16x32_bf16 v[48:51], v[136:139], a[148:151], v[48:51]\n"
      "ds_read_b64_tr_b16 v[136:137], v170 offset:55424\n"
      "buffer_load_dwordx4 a[124:127], v175, s[32:35], s67 offen offset:3072 sc1\n"
      "v_mfma_f32_16x16x32_bf16 v[28:31], v[140:143], a[144:147], v[28:31]\n"
      "ds_read_b64_tr_b16 v[138:139], v170 offset:56192\n"
      "v_mfma_f32_16x16x32_bf16 v[52:55], v[140:143], a[148:151], v[52:55]\n"
      "ds_read_b64_tr_b16 v[140:141], v171 offset:55424\n"
      "s_waitcnt lgkmcnt(11)\n"
      "v_mfma_f32_16x16x32_bf16 v[32:35], v[112:115], a[144:147], v[32:35]\n"
      "ds_read_b64_tr_b16 v[142:143], v171 offset:56192\n"
      "v_mfma_f32_16x16x32_bf16 v[56:59], v[112:115], a[148:151], v[56:59]\n"
      "ds_read_b64_tr_b16 v[112:113], v168 offset:0\n"
      "buffer_load_dwordx4 a[128:131], v174, s[32:35], s68 offen offset:0 sc1\n"
      "v_mfma_f32_16x16x32_bf16 v[36:39], v[116:119], a[144:147], v[36:39]\n"
      "ds_read_b64_tr_b16 v[114:115], v168 offset:768\n"
      "v_mfma_f32_16x16x32_bf16 v[60:63], v[116:119], a[148:151], v[60:63]\n"
      "ds_read_b64_tr_b16 v[116:117], v169 offset:0\n"
      "s_waitcnt lgkmcnt(11)\n"
      "s_waitcnt vmcnt(35)\n"
      "v_mfma_f32_16x16x32_bf16 v[16:19], v[120:123], a[152:155], v[16:19]\n"
      "ds_read_b64_tr_b16 v[118:119], v169 offset:768\n"
      "s_waitcnt vmcnt(34)\n"
      "v_mfma_f32_16x16x32_bf16 v[40:43], v[120:123], a[156:159], v[40:43]\n"
      "ds_read_b64_tr_b16 v[120:121], v168 offset:64\n"
      "buffer_load_dwordx4 a[132:135], v175, s[32:35], s68 offen offset:0 sc1\n"
      "v_mfma_f32_16x16x32_bf16 v[20:23], v[124:127], a[152:155], v[20:23]\n"
      "ds_read_b64_tr_b16 v[122:123], v168 offset:832\n"
      "v_mfma_f32_16x16x32_bf16 v[44:47], v[124:127], a[156:159], v[44:47]\n"
      "ds_read_b64_tr_b16 v[124:125], v169 offset:64\n"
      "s_waitcnt lgkmcnt(11)\n"
      "v_mfma_f32_16x16x32_bf16 v[24:27], v[128:131], a[152:155], v[24:27]\n"
      "ds_read_b64_tr_b16 v[126:127], v169 offset:832\n"
      "v_mfma_f32_16x16x32_bf16 v[48:51], v[128:131], a[156:159], v[48:51]\n"
      "ds_read_b64_tr_b16 v[128:129], v168 offset:128\n"
      "buffer_load_dwordx4 a[136:139], v174, s[32:35], s68 offen offset:1024 sc1\n"
      "v_mfma_f32_16x16x32_bf16 v[28:31], v[132:135], a[152:155], v[28:31]\n"
      "ds_read_b64_tr_b16 v[130:131], v168 offset:896\n"
      "v_mfma_f32_16x16x32_bf16 v[52:55], v[132:135], a[156:159], v[52:55]\n"
      "ds_read_b64_tr_b16 v[132:133], v169 offset:128\n"
      "s_waitcnt lgkmcnt(11)\n"
      "v_mfma_f32_16x16x32_bf16 v[32:35], v[136:139], a[152:155], v[32:35]\n"
      "ds_read_b64_tr_b16 v[134:135], v169 offset:896\n"
      "v_mfma_f32_16x16x32_bf16 v[56:59], v[136:139], a[156:159], v[56:59]\n"
      "ds_read_b64_tr_b16 v[136:137], v168 offset:6144\n"
      "buffer_load_dwordx4 a[140:143], v175, s[32:35], s68 offen offset:1024 sc1\n"
      "v_mfma_f32_16x16x32_bf16 v[36:39], v[140:143], a[152:155], v[36:39]\n"
      "ds_read_b64_tr_b16 v[138:139], v168 offset:6912\n"
      "v_mfma_f32_16x16x32_bf16 v[60:63], v[140:143], a[156:159], v[60:63]\n"
      "ds_read_b64_tr_b16 v[140:141], v169 offset:6144\n"
      "s_waitcnt lgkmcnt(11)\n"
      "s_waitcnt vmcnt(36)\n"
      "v_mfma_f32_16x16x32_bf16 v[64:67], v[112:115], a[0:3], v[144:147]\n"
      "ds_read_b64_tr_b16 v[142:143], v169 offset:6912\n"
      "s_waitcnt vmcnt(35)\n"
      "v_mfma_f32_16x16x32_bf16 v[88:91], v[112:115], a[4:7], v[144:147]\n"
      "ds_read_b64_tr_b16 v[112:113], v168 offset:6208\n"
      "buffer_load_dwordx4 a[144:147], v174, s[32:35], s68 offen offset:2048 sc1\n"
      "v_mfma_f32_16x16x32_bf16 v[68:71], v[116:119], a[0:3], v[148:151]\n"
      "ds_read_b64_tr_b16 v[114:115], v168 offset:6976\n"
      "v_mfma_f32_16x16x32_bf16 v[92:95], v[116:119], a[4:7], v[148:151]\n"
      "ds_read_b64_tr_b16 v[116:117], v169 offset:6208\n"
      "s_waitcnt lgkmcnt(11)\n"
      "v_mfma_f32_16x16x32_bf16 v[72:75], v[120:123], a[0:3], v[152:155]\n"
      "ds_read_b64_tr_b16 v[118:119], v169 offset:6976\n"
      "v_mul_f32 v180, v16, |v16|\n"
      "v_mul_f32 v181, v17, |v17|\n"
      "v_mfma_f32_16x16x32_bf16 v[96:99], v[120:123], a[4:7], v[152:155]\n"
      "ds_read_b64_tr_b16 v[120:121], v168 offset:6272\n"
      "buffer_load_dwordx4 a[148:151], v175, s[32:35], s68 offen offset:2048 sc1\n"
      "v_mul_f32 v182, v18, |v18|\n"
      "v_mfma_f32_16x16x32_bf16 v[76:79], v[124:127], a[0:3], v[156:159]\n"
      "ds_read_b64_tr_b16 v[122:123], v168 offset:7040\n"
      "v_mul_f32 v183, v19, |v19|\n"
      "v_mfma_f32_16x16x32_bf16 v[100:103], v[124:127], a[4:7], v[156:159]\n"
      "ds_read_b64_tr_b16 v[124:125], v169 offset:6272\n"
      "s_waitcnt lgkmcnt(11)\n"
      "v_mul_f32 v184, v20, |v20|\n"
      "v_mfma_f32_16x16x32_bf16 v[80:83], v[128:131], a[0:3], v[160:163]\n"
      "ds_read_b64_tr_b16 v[126:127], v169 offset:7040\n"
      "v_mul_f32 v185, v21, |v21|\n"
      "v_mfma_f32_16x16x32_bf16 v[104:107], v[128:131], a[4:7], v[160:163]\n"
      "ds_read_b64_tr_b16 v[128:129], v168 offset:12288\n"
      "buffer_load_dwordx4 a[152:155], v174, s[32:35], s68 offen offset:3072 sc1\n"
      "v_mul_f32 v186, v22, |v22|\n"
      "v_mfma_f32_16x16x32_bf16 v[84:87], v[132:135], a[0:3], v[164:167]\n"
      "ds_read_b64_tr_b16 v[130:131], v168 offset:13056\n"
      "v_mul_f32 v187, v23, |v23|\n"
      "v_mfma_f32_16x16x32_bf16 v[108:111], v[132:135], a[4:7], v[164:167]\n"
      "ds_read_b64_tr_b16 v[132:133], v169 offset:12288\n"
      "s_waitcnt lgkmcnt(11)\n"
      "v_cvt_pk_bf16_f32 v188, v180, v181\n"
      "s_waitcnt vmcnt(37)\n"
      "v_mfma_f32_16x16x32_bf16 v[64:67], v[136:139], a[8:11], v[64:67]\n"
      "ds_read_b64_tr_b16 v[134:135], v169 offset:13056\n"
      "v_cvt_pk_bf16_f32 v189, v182, v183\n"
      "s_waitcnt vmcnt(36)\n"
      "v_mfma_f32_16x16x32_bf16 v[88:91], v[136:139], a[12:15], v[88:91]\n"
      "ds_read_b64_tr_b16 v[136:137], v168 offset:12352\n"
      "buffer_load_dwordx4 a[156:159], v175, s[32:35], s68 offen offset:3072 sc1\n"
      "v_cvt_pk_bf16_f32 v190, v184, v185\n"
      "v_mfma_f32_16x16x32_bf16 v[68:71], v[140:143], a[8:11], v[68:71]\n"
      "ds_read_b64_tr_b16 v[138:139], v168 offset:13120\n"
      "v_cvt_pk_bf16_f32 v191, v186, v187\n"
      "v_mfma_f32_16x16x32_bf16 v[92:95], v[140:143], a[12:15], v[92:95]\n"
      "ds_read_b64_tr_b16 v[140:141], v169 offset:12352\n"
      "s_waitcnt lgkmcnt(11)\n"
      "s_waitcnt vmcnt(11)\n"
      "s_mov_b32 exec_hi, 0\n"
      "v_cmp_ne_u32 vcc, s56, v207\n"
      "s_mov_b32 exec_hi, -1\n"
      "s_cbranch_vccnz K2_spin_12\n"
      "K2_cont_13:\n"
      "v_pk_max_i16 v188, v188, 0\n"
      "v_mfma_f32_16x16x32_bf16 v[72:75], v[112:115], a[8:11], v[72:75]\n"
      "ds_read_b64_tr_b16 v[142:143], v169 offset:13120\n"
      "v_pk_max_i16 v189, v189, 0\n"
      "v_mfma_f32_16x16x32_bf16 v[96:99], v[112:115], a[12:15], v[96:99]\n"
      "ds_read_b64_tr_b16 v[112:113], v168 offset:12416\n"
      "buffer_load_dwordx4 a[160:163], v12, s[32:35], 0 offen offset:0 sc1\n"
      "v_pk_max_i16 v190, v190, 0\n"
      "v_mfma_f32_16x16x32_bf16 v[76:79], v[116:119], a[8:11], v[76:79]\n"
      "ds_read_b64_tr_b16 v[114:115], v168 offset:13184\n"
      "v_pk_max_i16 v191, v191, 0\n"
      "v_mfma_f32_16x16x32_bf16 v[100:103], v[116:119], a[12:15], v[100:103]\n"
      "ds_read_b64_tr_b16 v[116:117], v169 offset:12416\n"
      "s_waitcnt lgkmcnt(11)\n"
      "buffer_store_dwordx4 v[188:191], v2, s[32:35], s83 offen offset:0 sc1\n"
      "v_mfma_f32_16x16x32_bf16 v[80:83], v[120:123], a[8:11], v[80:83]\n"
      "ds_read_b64_tr_b16 v[118:119], v169 offset:13184\n"
      "v_mul_f32 v192, v24, |v24|\n"
      "v_mfma_f32_16x16x32_bf16 v[104:107], v[120:123], a[12:15], v[104:107]\n"
      "ds_read_b64_tr_b16 v[120:121], v168 offset:18432\n"
      "buffer_load_dwordx4 a[164:167], v12, s[32:35], 0 offen offset:1024 sc1\n"
      "v_mul_f32 v193, v25, |v25|\n"
      "v_mfma_f32_16x16x32_bf16 v[84:87], v[124:127], a[8:11], v[84:87]\n"
      "ds_read_b64_tr_b16 v[122:123], v168 offset:19200\n"
      "v_mul_f32 v194, v26, |v26|\n"
      "v_mfma_f32_16x16x32_bf16 v[108:111], v[124:127], a[12:15], v[108:111]\n"
      "ds_read_b64_tr_b16 v[124:125], v169 offset:18432\n"
      "s_waitcnt lgkmcnt(11)\n"
      "v_mul_f32 v195, v27, |v27|\n"
      "v_mfma_f32_16x16x32_bf16 v[64:67], v[128:131], a[16:19], v[64:67]\n"
      "ds_read_b64_tr_b16 v[126:127], v169 offset:19200\n"
      "v_mul_f32 v196, v28, |v28|\n"
      "v_mfma_f32_16x16x32_bf16 v[88:91], v[128:131], a[20:23], v[88:91]\n"
      "ds_read_b64_tr_b16 v[128:129], v168 offset:18496\n"
      "buffer_load_dwordx4 a[168:171], v12, s[32:35], 0 offen offset:2048 sc1\n"
      "v_mul_f32 v197, v29, |v29|\n"
      "v_mfma_f32_16x16x32_bf16 v[68:71], v[132:135], a[16:19], v[68:71]\n"
      "ds_read_b64_tr_b16 v[130:131], v168 offset:19264\n"
      "v_mul_f32 v198, v30, |v30|\n"
      "v_mfma_f32_16x16x32_bf16 v[92:95], v[132:135], a[20:23], v[92:95]\n"
      "ds_read_b64_tr_b16 v[132:133], v169 offset:18496\n"
      "s_waitcnt lgkmcnt(11)\n"
      "v_mul_f32 v199, v31, |v31|\n"
      "v_mfma_f32_16x16x32_bf16 v[72:75], v[136:139], a[16:19], v[72:75]\n"
      "ds_read_b64_tr_b16 v[134:135], v169 offset:19264\n"
      "v_cvt_pk_bf16_f32 v200, v192, v193\n"
      "v_mfma_f32_16x16x32_bf16 v[96:99], v[136:139], a[20:23], v[96:99]\n"
      "ds_read_b64_tr_b16 v[136:137], v168 offset:18560\n"
      "buffer_load_dwordx4 a[172:175], v12, s[32:35], 0 offen offset:3072 sc1\n"
      "v_cvt_pk_bf16_f32 v201, v194, v195\n"
      "v_mfma_f32_16x16x32_bf16 v[76:79], v[140:143], a[16:19], v[76:79]\n"
      "ds_read_b64_tr_b16 v[138:139], v168 offset:19328\n"
      "v_cvt_pk_bf16_f32 v202, v196, v197\n"
      "v_mfma_f32_16x16x32_bf16 v[100:103], v[140:143], a[20:23], v[100:103]\n"
      "ds_read_b64_tr_b16 v[140:141], v169 offset:18560\n"
      "s_waitcnt lgkmcnt(11)\n"
      "v_cvt_pk_bf16_f32 v203, v198, v199\n"
      "v_mfma_f32_16x16x32_bf16 v[80:83], v[112:115], a[16:19], v[80:83]\n"
      "ds_read_b64_tr_b16 v[142:143], v169 offset:19328\n"
      "v_pk_max_i16 v200, v200, 0\n"
      "v_mfma_f32_16x16x32_bf16 v[104:107], v[112:115], a[20:23], v[104:107]\n"
      "ds_read_b64_tr_b16 v[112:113], v168 offset:24576\n"
      "buffer_load_dwordx4 a[176:179], v12, s[32:35], s65 offen offset:0 sc1\n"
      "v_pk_max_i16 v201, v201, 0\n"
      "v_mfma_f32_16x16x32_bf16 v[84:87], v[116:119], a[16:19], v[84:87]\n"
      "ds_read_b64_tr_b16 v[114:115], v168 offset:25344\n"
      "v_pk_max_i16 v202, v202, 0\n"
      "v_mfma_f32_16x16x32_bf16 v[108:111], v[116:119], a[20:23], v[108:111]\n"
      "ds_read_b64_tr_b16 v[116:117], v169 offset:24576\n"
      "s_waitcnt lgkmcnt(11)\n"
      "v_pk_max_i16 v203, v203, 0\n"
      "v_mfma_f32_16x16x32_bf16 v[64:67], v[120:123], a[24:27], v[64:67]\n"
      "ds_read_b64_tr_b16 v[118:119], v169 offset:25344\n"
      "buffer_store_dwordx4 v[200:203], v2, s[32:35], s83 offen offset:1024 sc1\n"
      "v_mfma_f32_16x16x32_bf16 v[88:91], v[120:123], a[28:31], v[88:91]\n"
      "ds_read_b64_tr_b16 v[120:121], v168 offset:24640\n"
      "buffer_load_dwordx4 a[180:183], v12, s[32:35], s65 offen offset:1024 sc1\n"
      "v_mul_f32 v180, v32, |v32|\n"
      "v_mfma_f32_16x16x32_bf16 v[68:71], v[124:127], a[24:27], v[68:71]\n"
      "ds_read_b64_tr_b16 v[122:123], v168 offset:25408\n"
      "v_mul_f32 v181, v33, |v33|\n"
      "v_mfma_f32_16x16x32_bf16 v[92:95], v[124:127], a[28:31], v[92:95]\n"
      "ds_read_b64_tr_b16 v[124:125], v169 offset:24640\n"
      "s_waitcnt lgkmcnt(11)\n"
      "v_mul_f32 v182, v34, |v34|\n"
      "v_mfma_f32_16x16x32_bf16 v[72:75], v[128:131], a[24:27], v[72:75]\n"
      "ds_read_b64_tr_b16 v[126:127], v169 offset:25408\n"
      "v_mul_f32 v183, v35, |v35|\n"
      "v_mfma_f32_16x16x32_bf16 v[96:99], v[128:131], a[28:31], v[96:99]\n"
      "ds_read_b64_tr_b16 v[128:129], v168 offset:24704\n"
      "buffer_load_dwordx4 a[184:187], v12, s[32:35], s65 offen offset:2048 sc1\n"
      "v_mul_f32 v184, v36, |v36|\n"
      "v_mfma_f32_16x16x32_bf16 v[76:79], v[132:135], a[24:27], v[76:79]\n"
      "ds_read_b64_tr_b16 v[130:131], v168 offset:25472\n"
      "v_mul_f32 v185, v37, |v37|\n"
      "v_mfma_f32_16x16x32_bf16 v[100:103], v[132:135], a[28:31], v[100:103]\n"
      "ds_read_b64_tr_b16 v[132:133], v169 offset:24704\n"
      "s_waitcnt lgkmcnt(11)\n"
      "v_mul_f32 v186, v38, |v38|\n"
      "v_mfma_f32_16x16x32_bf16 v[80:83], v[136:139], a[24:27], v[80:83]\n"
      "ds_read_b64_tr_b16 v[134:135], v169 offset:25472\n"
      "v_mul_f32 v187, v39, |v39|\n"
      "v_mfma_f32_16x16x32_bf16 v[104:107], v[136:139], a[28:31], v[104:107]\n"
      "ds_read_b64_tr_b16 v[136:137], v168 offset:30720\n"
      "buffer_load_dwordx4 a[188:191], v12, s[32:35], s65 offen offset:3072 sc1\n"
      "v_cvt_pk_bf16_f32 v188, v180, v181\n"
      "v_mfma_f32_16x16x32_bf16 v[84:87], v[140:143], a[24:27], v[84:87]\n"
      "ds_read_b64_tr_b16 v[138:139], v168 offset:31488\n"
      "v_cvt_pk_bf16_f32 v189, v182, v183\n"
      "v_mfma_f32_16x16x32_bf16 v[108:111], v[140:143], a[28:31], v[108:111]\n"
      "ds_read_b64_tr_b16 v[140:141], v169 offset:30720\n"
      "s_waitcnt lgkmcnt(11)\n"
      "v_cvt_pk_bf16_f32 v190, v184, v185\n"
      "v_mfma_f32_16x16x32_bf16 v[64:67], v[112:115], a[32:35], v[64:67]\n"
      "ds_read_b64_tr_b16 v[142:143], v169 offset:31488\n"
      "v_cvt_pk_bf16_f32 v191, v186, v187\n"
      "v_mfma_f32_16x16x32_bf16 v[88:91], v[112:115], a[36:39], v[88:91]\n"
      "ds_read_b64_tr_b16 v[112:113], v168 offset:30784\n"
      "buffer_load_dwordx4 a[192:195], v12, s[32:35], s66 offen offset:0 sc1\n"
      "v_pk_max_i16 v188, v188, 0\n"
      "v_mfma_f32_16x16x32_bf16 v[68:71], v[116:119], a[32:35], v[68:71]\n"
      "ds_read_b64_tr_b16 v[114:115], v168 offset:31552\n"
      "v_pk_max_i16 v189, v189, 0\n"
      "v_mfma_f32_16x16x32_bf16 v[92:95], v[116:119], a[36:39], v[92:95]\n"
      "ds_read_b64_tr_b16 v[116:117], v169 offset:30784\n"
      "s_waitcnt lgkmcnt(11)\n"
      "v_pk_max_i16 v190, v190, 0\n"
      "v_mfma_f32_16x16x32_bf16 v[72:75], v[120:123], a[32:35], v[72:75]\n"
      "ds_read_b64_tr_b16 v[118:119], v169 offset:31552\n"
      "v_pk_max_i16 v191, v191, 0\n"
      "v_mfma_f32_16x16x32_bf16 v[96:99], v[120:123], a[36:39], v[96:99]\n"
      "ds_read_b64_tr_b16 v[120:121], v168 offset:30848\n"
      "buffer_load_dwordx4 a[196:199], v12, s[32:35], s66 offen offset:1024 sc1\n"
      "buffer_store_dwordx4 v[188:191], v2, s[32:35], s83 offen offset:2048 sc1\n"
      "v_mfma_f32_16x16x32_bf16 v[76:79], v[124:127], a[32:35], v[76:79]\n"
      "ds_read_b64_tr_b16 v[122:123], v168 offset:31616\n"
      "v_mul_f32 v192, v40, |v40|\n"
      "v_mul_f32 v193, v41, |v41|\n"
      "v_mfma_f32_16x16x32_bf16 v[100:103], v[124:127], a[36:39], v[100:103]\n"
      "ds_read_b64_tr_b16 v[124:125], v169 offset:30848\n"
      "s_waitcnt lgkmcnt(11)\n"
      "v_mul_f32 v194, v42, |v42|\n"
      "v_mfma_f32_16x16x32_bf16 v[80:83], v[128:131], a[32:35], v[80:83]\n"
      "ds_read_b64_tr_b16 v[126:127], v169 offset:31616\n"
      "v_mul_f32 v195, v43, |v43|\n"
      "v_mfma_f32_16x16x32_bf16 v[104:107], v[128:131], a[36:39], v[104:107]\n"
      "ds_read_b64_tr_b16 v[128:129], v168 offset:36864\n"
      "buffer_load_dwordx4 a[200:203], v12, s[32:35], s66 offen offset:2048 sc1\n"
      "v_mul_f32 v196, v44, |v44|\n"
      "v_mfma_f32_16x16x32_bf16 v[84:87], v[132:135], a[32:35], v[84:87]\n"
      "ds_read_b64_tr_b16 v[130:131], v168 offset:37632\n"
      "v_mul_f32 v197, v45, |v45|\n"
      "v_mfma_f32_16x16x32_bf16 v[108:111], v[132:135], a[36:39], v[108:111]\n"
      "ds_read_b64_tr_b16 v[132:133], v169 offset:36864\n"
      "s_waitcnt lgkmcnt(11)\n"
      "v_mul_f32 v198, v46, |v46|\n"
      "v_mfma_f32_16x16x32_bf16 v[64:67], v[136:139], a[40:43], v[64:67]\n"
      "ds_read_b64_tr_b16 v[134:135], v169 offset:37632\n"
      "v_mul_f32 v199, v47, |v47|\n"
      "v_mfma_f32_16x16x32_bf16 v[88:91], v[136:139], a[44:47], v[88:91]\n"
      "ds_read_b64_tr_b16 v[136:137], v168 offset:36928\n"
      "buffer_load_dwordx4 a[204:207], v12, s[32:35], s66 offen offset:3072 sc1\n"
      "v_cvt_pk_bf16_f32 v200, v192, v193\n"
      "v_mfma_f32_16x16x32_bf16 v[68:71], v[140:143], a[40:43], v[68:71]\n"
      "ds_read_b64_tr_b16 v[138:139], v168 offset:37696\n"
      "v_cvt_pk_bf16_f32 v201, v194, v195\n"
      "v_mfma_f32_16x16x32_bf16 v[92:95], v[140:143], a[44:47], v[92:95]\n"
      "ds_read_b64_tr_b16 v[140:141], v169 offset:36928\n"
      "s_waitcnt lgkmcnt(11)\n"
      "v_cvt_pk_bf16_f32 v202, v196, v197\n"
      "v_mfma_f32_16x16x32_bf16 v[72:75], v[112:115], a[40:43], v[72:75]\n"
      "ds_read_b64_tr_b16 v[142:143], v169 offset:37696\n"
      "v_cvt_pk_bf16_f32 v203, v198, v199\n"
      "v_mfma_f32_16x16x32_bf16 v[96:99], v[112:115], a[44:47], v[96:99]\n"
      "ds_read_b64_tr_b16 v[112:113], v168 offset:36992\n"
      "buffer_load_dwordx4 a[208:211], v12, s[32:35], s67 offen offset:0 sc1\n"
      "v_pk_max_i16 v200, v200, 0\n"
      "v_mfma_f32_16x16x32_bf16 v[76:79], v[116:119], a[40:43], v[76:79]\n"
      "ds_read_b64_tr_b16 v[114:115], v168 offset:37760\n"
      "v_pk_max_i16 v201, v201, 0\n"
      "v_mfma_f32_16x16x32_bf16 v[100:103], v[116:119], a[44:47], v[100:103]\n"
      "ds_read_b64_tr_b16 v[116:117], v169 offset:36992\n"
      "s_waitcnt lgkmcnt(11)\n"
      "v_pk_max_i16 v202, v202, 0\n"
      "v_mfma_f32_16x16x32_bf16 v[80:83], v[120:123], a[40:43], v[80:83]\n"
      "ds_read_b64_tr_b16 v[118:119], v169 offset:37760\n"
      "v_pk_max_i16 v203, v203, 0\n"
      "v_mfma_f32_16x16x32_bf16 v[104:107], v[120:123], a[44:47], v[104:107]\n"
      "ds_read_b64_tr_b16 v[120:121], v168 offset:43008\n"
      "buffer_load_dwordx4 a[212:215], v12, s[32:35], s67 offen offset:1024 sc1\n"
      "buffer_store_dwordx4 v[200:203], v2, s[32:35], s84 offen offset:0 sc1\n"
      "v_mfma_f32_16x16x32_bf16 v[84:87], v[124:127], a[40:43], v[84:87]\n"
      "ds_read_b64_tr_b16 v[122:123], v168 offset:43776\n"
      "v_mul_f32 v180, v48, |v48|\n"
      "v_mfma_f32_16x16x32_bf16 v[108:111], v[124:127], a[44:47], v[108:111]\n"
      "ds_read_b64_tr_b16 v[124:125], v169 offset:43008\n"
      "s_waitcnt lgkmcnt(11)\n"
      "v_mul_f32 v181, v49, |v49|\n"
      "v_mfma_f32_16x16x32_bf16 v[64:67], v[128:131], a[48:51], v[64:67]\n"
      "ds_read_b64_tr_b16 v[126:127], v169 offset:43776\n"
      "v_mul_f32 v182, v50, |v50|\n"
      "v_mfma_f32_16x16x32_bf16 v[88:91], v[128:131], a[52:55], v[88:91]\n"
      "ds_read_b64_tr_b16 v[128:129], v168 offset:43072\n"
      "buffer_load_dwordx4 a[216:219], v12, s[32:35], s67 offen offset:2048 sc1\n"
      "v_mul_f32 v183, v51, |v51|\n"
      "v_mfma_f32_16x16x32_bf16 v[68:71], v[132:135], a[48:51], v[68:71]\n"
      "ds_read_b64_tr_b16 v[130:131], v168 offset:43840\n"
      "v_mul_f32 v184, v52, |v52|\n"
      "v_mfma_f32_16x16x32_bf16 v[92:95], v[132:135], a[52:55], v[92:95]\n"
      "ds_read_b64_tr_b16 v[132:133], v169 offset:43072\n"
      "s_waitcnt lgkmcnt(11)\n"
      "v_mul_f32 v185, v53, |v53|\n"
      "v_mfma_f32_16x16x32_bf16 v[72:75], v[136:139], a[48:51], v[72:75]\n"
      "ds_read_b64_tr_b16 v[134:135], v169 offset:43840\n"
      "v_mul_f32 v186, v54, |v54|\n"
      "v_mfma_f32_16x16x32_bf16 v[96:99], v[136:139], a[52:55], v[96:99]\n"
      "ds_read_b64_tr_b16 v[136:137], v168 offset:43136\n"
      "buffer_load_dwordx4 a[220:223], v242, s[32:35], 0 offen offset:0 sc1\n"
      "v_mul_f32 v187, v55, |v55|\n"
      "v_mfma_f32_16x16x32_bf16 v[76:79], v[140:143], a[48:51], v[76:79]\n"
      "ds_read_b64_tr_b16 v[138:139], v168 offset:43904\n"
      "v_cvt_pk_bf16_f32 v188, v180, v181\n"
      "v_mfma_f32_16x16x32_bf16 v[100:103], v[140:143], a[52:55], v[100:103]\n"
      "ds_read_b64_tr_b16 v[140:141], v169 offset:43136\n"
      "s_waitcnt lgkmcnt(11)\n"
      "v_cvt_pk_bf16_f32 v189, v182, v183\n"
      "v_mfma_f32_16x16x32_bf16 v[80:83], v[112:115], a[48:51], v[80:83]\n"
      "ds_read_b64_tr_b16 v[142:143], v169 offset:43904\n"
      "v_cvt_pk_bf16_f32 v190, v184, v185\n"
      "v_mfma_f32_16x16x32_bf16 v[104:107], v[112:115], a[52:55], v[104:107]\n"
      "ds_read_b64_tr_b16 v[112:113], v168 offset:49152\n"
      "buffer_load_dwordx4 a[224:227], v242, s[32:35], 0 offen offset:1024 sc1\n"
      "v_cvt_pk_bf16_f32 v191, v186, v187\n"
      "v_mfma_f32_16x16x32_bf16 v[84:87], v[116:119], a[48:51], v[84:87]\n"
      "ds_read_b64_tr_b16 v[114:115], v168 offset:49920\n"
      "v_pk_max_i16 v188, v188, 0\n"
      "v_mfma_f32_16x16x32_bf16 v[108:111], v[116:119], a[52:55], v[108:111]\n"
      "ds_read_b64_tr_b16 v[116:117], v169 offset:49152\n"
      "s_waitcnt lgkmcnt(11)\n"
      "v_pk_max_i16 v189, v189, 0\n"
      "v_mfma_f32_16x16x32_bf16 v[64:67], v[120:123], a[56:59], v[64:67]\n"
      "ds_read_b64_tr_b16 v[118:119], v169 offset:49920\n"
      "v_pk_max_i16 v190, v190, 0\n"
      "v_mfma_f32_16x16x32_bf16 v[88:91], v[120:123], a[60:63], v[88:91]\n"
      "ds_read_b64_tr_b16 v[120:121], v168 offset:49216\n"
      "buffer_load_dwordx4 a[228:231], v242, s[32:35], 0 offen offset:2048 sc1\n"
      "v_pk_max_i16 v191, v191, 0\n"
      "v_mfma_f32_16x16x32_bf16 v[68:71], v[124:127], a[56:59], v[68:71]\n"
      "ds_read_b64_tr_b16 v[122:123], v168 offset:49984\n"
      "buffer_store_dwordx4 v[188:191], v2, s[32:35], s84 offen offset:1024 sc1\n"
      "v_mfma_f32_16x16x32_bf16 v[92:95], v[124:127], a[60:63], v[92:95]\n"
      "ds_read_b64_tr_b16 v[124:125], v169 offset:49216\n"
      "s_waitcnt lgkmcnt(11)\n"
      "v_mul_f32 v192, v56, |v56|\n"
      "v_mfma_f32_16x16x32_bf16 v[72:75], v[128:131], a[56:59], v[72:75]\n"
      "ds_read_b64_tr_b16 v[126:127], v169 offset:49984\n"
      "v_mul_f32 v193, v57, |v57|\n"
      "v_mfma_f32_16x16x32_bf16 v[96:99], v[128:131], a[60:63], v[96:99]\n"
      "ds_read_b64_tr_b16 v[128:129], v168 offset:49280\n"
      "buffer_load_dwordx4 a[232:235], v242, s[32:35], 0 offen offset:3072 sc1\n"
      "v_mul_f32 v194, v58, |v58|\n"
      "v_mfma_f32_16x16x32_bf16 v[76:79], v[132:135], a[56:59], v[76:79]\n"
      "ds_read_b64_tr_b16 v[130:131], v168 offset:50048\n"
      "v_mul_f32 v195, v59, |v59|\n"
      "v_mfma_f32_16x16x32_bf16 v[100:103], v[132:135], a[60:63], v[100:103]\n"
      "ds_read_b64_tr_b16 v[132:133], v169 offset:49280\n"
      "s_waitcnt lgkmcnt(11)\n"
      "v_mul_f32 v196, v60, |v60|\n"
      "v_mfma_f32_16x16x32_bf16 v[80:83], v[136:139], a[56:59], v[80:83]\n"
      "ds_read_b64_tr_b16 v[134:135], v169 offset:50048\n"
      "v_mul_f32 v197, v61, |v61|\n"
      "v_mfma_f32_16x16x32_bf16 v[104:107], v[136:139], a[60:63], v[104:107]\n"
      "ds_read_b64_tr_b16 v[136:137], v168 offset:55296\n"
      "buffer_load_dwordx4 a[236:239], v242, s[32:35], s65 offen offset:0 sc1\n"
      "v_mul_f32 v198, v62, |v62|\n"
      "v_mfma_f32_16x16x32_bf16 v[84:87], v[140:143], a[56:59], v[84:87]\n"
      "ds_read_b64_tr_b16 v[138:139], v168 offset:56064\n"
      "v_mul_f32 v199, v63, |v63|\n"
      "v_mfma_f32_16x16x32_bf16 v[108:111], v[140:143], a[60:63], v[108:111]\n"
      "ds_read_b64_tr_b16 v[140:141], v169 offset:55296\n"
      "s_waitcnt lgkmcnt(11)\n"
      "v_cvt_pk_bf16_f32 v200, v192, v193\n"
      "v_mfma_f32_16x16x32_bf16 v[64:67], v[112:115], a[64:67], v[64:67]\n"
      "ds_read_b64_tr_b16 v[142:143], v169 offset:56064\n"
      "v_cvt_pk_bf16_f32 v201, v194, v195\n"
      "v_mfma_f32_16x16x32_bf16 v[88:91], v[112:115], a[68:71], v[88:91]\n"
      "ds_read_b64_tr_b16 v[112:113], v168 offset:55360\n"
      "buffer_load_dwordx4 a[0:3], v242, s[32:35], s65 offen offset:1024 sc1\n"
      "v_cvt_pk_bf16_f32 v202, v196, v197\n"
      "v_mfma_f32_16x16x32_bf16 v[68:71], v[116:119], a[64:67], v[68:71]\n"
      "ds_read_b64_tr_b16 v[114:115], v168 offset:56128\n"
      "v_cvt_pk_bf16_f32 v203, v198, v199\n"
      "v_mfma_f32_16x16x32_bf16 v[92:95], v[116:119], a[68:71], v[92:95]\n"
      "ds_read_b64_tr_b16 v[116:117], v169 offset:55360\n"
      "s_waitcnt lgkmcnt(11)\n"
      "v_pk_max_i16 v200, v200, 0\n"
      "v_mfma_f32_16x16x32_bf16 v[72:75], v[120:123], a[64:67], v[72:75]\n"
      "ds_read_b64_tr_b16 v[118:119], v169 offset:56128\n"
      "v_pk_max_i16 v201, v201, 0\n"
      "v_mfma_f32_16x16x32_bf16 v[96:99], v[120:123], a[68:71], v[96:99]\n"
      "ds_read_b64_tr_b16 v[120:121], v168 offset:55424\n"
      "buffer_load_dwordx4 a[4:7], v242, s[32:35], s65 offen offset:2048 sc1\n"
      "v_pk_max_i16 v202, v202, 0\n"
      "v_mfma_f32_16x16x32_bf16 v[76:79], v[124:127], a[64:67], v[76:79]\n"
      "ds_read_b64_tr_b16 v[122:123], v168 offset:56192\n"
      "v_pk_max_i16 v203, v203, 0\n"
      "v_mfma_f32_16x16x32_bf16 v[100:103], v[124:127], a[68:71], v[100:103]\n"
      "ds_read_b64_tr_b16 v[124:125], v169 offset:55424\n"
      "s_waitcnt lgkmcnt(11)\n"
      "buffer_store_dwordx4 v[200:203], v2, s[32:35], s84 offen offset:2048 sc1\n"
      "v_mfma_f32_16x16x32_bf16 v[80:83], v[128:131], a[64:67], v[80:83]\n"
      "ds_read_b64_tr_b16 v[126:127], v169 offset:56192\n"
      "v_mfma_f32_16x16x32_bf16 v[104:107], v[128:131], a[68:71], v[104:107]\n"
      "ds_read_b64_tr_b16 v[128:129], v170 offset:0\n"
      "buffer_load_dwordx4 a[8:11], v242, s[32:35], s65 offen offset:3072 sc1\n"
      "s_lshl_b32 s52, s48, 9\n"
      "v_mfma_f32_16x16x32_bf16 v[84:87], v[132:135], a[64:67], v[84:87]\n"
      "ds_read_b64_tr_b16 v[130:131], v170 offset:768\n"
      "s_add_u32 s52, s52, 262144\n"
      "v_mfma_f32_16x16x32_bf16 v[108:111], v[132:135], a[68:71], v[108:111]\n"
      "ds_read_b64_tr_b16 v[132:133], v171 offset:0\n"
      "s_waitcnt lgkmcnt(11)\n"
      "v_mfma_f32_16x16x32_bf16 v[64:67], v[136:139], a[72:75], v[64:67]\n"
      "ds_read_b64_tr_b16 v[134:135], v171 offset:768\n"
      "v_lshl_add_u32 v245, v8, 7, s52\n"
      "v_mfma_f32_16x16x32_bf16 v[88:91], v[136:139], a[76:79], v[88:91]\n"
      "ds_read_b64_tr_b16 v[136:137], v170 offset:64\n"
      "buffer_load_dwordx4 a[12:15], v242, s[32:35], s66 offen offset:0 sc1\n"
      "v_mfma_f32_16x16x32_bf16 v[68:71], v[140:143], a[72:75], v[68:71]\n"
      "ds_read_b64_tr_b16 v[138:139], v170 offset:832\n"
      "v_mov_b32 v253, v245\n"
      "v_mfma_f32_16x16x32_bf16 v[92:95], v[140:143], a[76:79], v[92:95]\n"
      "ds_read_b64_tr_b16 v[140:141], v171 offset:64\n"
      "s_waitcnt lgkmcnt(11)\n"
      "v_mfma_f32_16x16x32_bf16 v[72:75], v[112:115], a[72:75], v[72:75]\n"
      "ds_read_b64_tr_b16 v[142:143], v171 offset:832\n"
      "s_mul_i32 s54, s48, 196608\n"
      "v_mfma_f32_16x16x32_bf16 v[96:99], v[112:115], a[76:79], v[96:99]\n"
      "ds_read_b64_tr_b16 v[112:113], v170 offset:128\n"
      "buffer_load_dwordx4 a[16:19], v242, s[32:35], s66 offen offset:1024 sc1\n"
      "s_add_u32 s54, s54, 12582912\n"
      "v_mfma_f32_16x16x32_bf16 v[76:79], v[116:119], a[72:75], v[76:79]\n"
      "ds_read_b64_tr_b16 v[114:115], v170 offset:896\n"
      "v_mfma_f32_16x16x32_bf16 v[100:103], v[116:119], a[76:79], v[100:103]\n"
      "ds_read_b64_tr_b16 v[116:117], v171 offset:128\n"
      "s_waitcnt lgkmcnt(11)\n"
      "s_bfe_u32 s52, s48, 0x10001\n"
      "v_mfma_f32_16x16x32_bf16 v[80:83], v[120:123], a[72:75], v[80:83]\n"
      "ds_read_b64_tr_b16 v[118:119], v171 offset:896\n"
      "v_mfma_f32_16x16x32_bf16 v[104:107], v[120:123], a[76:79], v[104:107]\n"
      "ds_read_b64_tr_b16 v[120:121], v170 offset:6144\n"
      "buffer_load_dwordx4 a[20:23], v242, s[32:35], s66 offen offset:2048 sc1\n"
      "s_lshl1_add_u32 s52, s31, s52\n"
      "v_mfma_f32_16x16x32_bf16 v[84:87], v[124:127], a[72:75], v[84:87]\n"
      "ds_read_b64_tr_b16 v[122:123], v170 offset:6912\n"
      "v_mfma_f32_16x16x32_bf16 v[108:111], v[124:127], a[76:79], v[108:111]\n"
      "ds_read_b64_tr_b16 v[124:125], v171 offset:6144\n"
      "s_waitcnt lgkmcnt(11)\n"
      "s_mul_i32 s53, s52, 3072\n"
      "v_mfma_f32_16x16x32_bf16 v[64:67], v[128:131], a[80:83], v[64:67]\n"
      "ds_read_b64_tr_b16 v[126:127], v171 offset:6912\n"
      "s_add_u32 s53, s53, s54\n"
      "v_mfma_f32_16x16x32_bf16 v[88:91], v[128:131], a[84:87], v[88:91]\n"
      "ds_read_b64_tr_b16 v[128:129], v170 offset:6208\n"
      "buffer_load_dwordx4 a[24:27], v242, s[32:35], s66 offen offset:3072 sc1\n"
      "v_mfma_f32_16x16x32_bf16 v[68:71], v[132:135], a[80:83], v[68:71]\n"
      "ds_read_b64_tr_b16 v[130:131], v170 offset:6976\n"
      "v_add_u32 v247, s53, v2\n"
      "v_mfma_f32_16x16x32_bf16 v[92:95], v[132:135], a[84:87], v[92:95]\n"
      "ds_read_b64_tr_b16 v[132:133], v171 offset:6208\n"
      "s_waitcnt lgkmcnt(11)\n"
      "v_mfma_f32_16x16x32_bf16 v[72:75], v[136:139], a[80:83], v[72:75]\n"
      "ds_read_b64_tr_b16 v[134:135], v171 offset:6976\n"
      "s_lshl_b32 s52, s52, 14\n"
      "v_mfma_f32_16x16x32_bf16 v[96:99], v[136:139], a[84:87], v[96:99]\n"
      "ds_read_b64_tr_b16 v[136:137], v170 offset:6272\n"
      "buffer_load_dwordx4 a[28:31], v242, s[32:35], s67 offen offset:0 sc1\n"
      "v_mfma_f32_16x16x32_bf16 v[76:79], v[140:143], a[80:83], v[76:79]\n"
      "ds_read_b64_tr_b16 v[138:139], v170 offset:7040\n"
      "s_mov_b64 exec, 0xf\n"
      "v_add_u32 v245, s52, v245\n"
      "s_mov_b64 exec, -1\n"
      "v_mfma_f32_16x16x32_bf16 v[100:103], v[140:143], a[84:87], v[100:103]\n"
      "ds_read_b64_tr_b16 v[140:141], v171 offset:6272\n"
      "s_waitcnt lgkmcnt(11)\n"
      "v_mfma_f32_16x16x32_bf16 v[80:83], v[112:115], a[80:83], v[80:83]\n"
      "ds_read_b64_tr_b16 v[142:143], v171 offset:7040\n"
      "s_bfe_u32 s52, s48, 0x10001\n"
      "v_mfma_f32_16x16x32_bf16 v[104:107], v[112:115], a[84:87], v[104:107]\n"
      "ds_read_b64_tr_b16 v[112:113], v170 offset:12288\n"
      "buffer_load_dwordx4 a[32:35], v242, s[32:35], s67 offen offset:1024 sc1\n"
      "s_lshl1_add_u32 s52, s31, s52\n"
      "v_mfma_f32_16x16x32_bf16 v[84:87], v[116:119], a[80:83], v[84:87]\n"
      "ds_read_b64_tr_b16 v[114:115], v170 offset:13056\n"
      "v_mfma_f32_16x16x32_bf16 v[108:111], v[116:119], a[84:87], v[108:111]\n"
      "ds_read_b64_tr_b16 v[116:117], v171 offset:12288\n"
      "s_waitcnt lgkmcnt(11)\n"
      "s_add_u32 s52, s52, 8\n"
      "v_mfma_f32_16x16x32_bf16 v[64:67], v[120:123], a[88:91], v[64:67]\n"
      "ds_read_b64_tr_b16 v[118:119], v171 offset:13056\n"
      "v_mfma_f32_16x16x32_bf16 v[88:91], v[120:123], a[92:95], v[88:91]\n"
      "ds_read_b64_tr_b16 v[120:121], v170 offset:18432\n"
      "buffer_load_dwordx4 a[36:39], v242, s[32:35], s67 offen offset:2048 sc1\n"
      "s_mul_i32 s53, s52, 3072\n"
      "v_mfma_f32_16x16x32_bf16 v[68:71], v[124:127], a[88:91], v[68:71]\n"
      "ds_read_b64_tr_b16 v[122:123], v170 offset:19200\n"
      "v_mfma_f32_16x16x32_bf16 v[92:95], v[124:127], a[92:95], v[92:95]\n"
      "ds_read_b64_tr_b16 v[124:125], v171 offset:18432\n"
      "s_waitcnt lgkmcnt(11)\n"
      "s_add_u32 s53, s53, s54\n"
      "v_mfma_f32_16x16x32_bf16 v[72:75], v[128:131], a[88:91], v[72:75]\n"
      "ds_read_b64_tr_b16 v[126:127], v171 offset:19200\n"
      "v_add_u32 v248, s53, v2\n"
      "v_mfma_f32_16x16x32_bf16 v[96:99], v[128:131], a[92:95], v[96:99]\n"
      "ds_read_b64_tr_b16 v[128:129], v170 offset:24576\n"
      "buffer_load_dwordx4 a[40:43], v243, s[32:35], 0 offen offset:0 sc1\n"
      "v_mfma_f32_16x16x32_bf16 v[76:79], v[132:135], a[88:91], v[76:79]\n"
      "ds_read_b64_tr_b16 v[130:131], v170 offset:25344\n"
      "s_lshl_b32 s52, s52, 14\n"
      "v_mfma_f32_16x16x32_bf16 v[100:103], v[132:135], a[92:95], v[100:103]\n"
      "ds_read_b64_tr_b16 v[132:133], v171 offset:24576\n"
      "s_waitcnt lgkmcnt(11)\n"
      "v_mfma_f32_16x16x32_bf16 v[80:83], v[136:139], a[88:91], v[80:83]\n"
      "ds_read_b64_tr_b16 v[134:135], v171 offset:25344\n"
      "s_mov_b64 exec, 0xf0\n"
      "v_add_u32 v245, s52, v245\n"
      "s_mov_b64 exec, -1\n"
      "v_mfma_f32_16x16x32_bf16 v[104:107], v[136:139], a[92:95], v[104:107]\n"
      "ds_read_b64_tr_b16 v[136:137], v170 offset:30720\n"
      "buffer_load_dwordx4 a[44:47], v243, s[32:35], 0 offen offset:1024 sc1\n"
      "v_mfma_f32_16x16x32_bf16 v[84:87], v[140:143], a[88:91], v[84:87]\n"
      "ds_read_b64_tr_b16 v[138:139], v170 offset:31488\n"
      "s_bfe_u32 s52, s48, 0x10001\n"
      "v_mfma_f32_16x16x32_bf16 v[108:111], v[140:143], a[92:95], v[108:111]\n"
      "ds_read_b64_tr_b16 v[140:141], v171 offset:30720\n"
      "s_waitcnt lgkmcnt(11)\n"
      "s_xor_b32 s52, s52, 1\n"
      "v_mfma_f32_16x16x32_bf16 v[64:67], v[112:115], a[96:99], v[64:67]\n"
      "ds_read_b64_tr_b16 v[142:143], v171 offset:31488\n"
      "v_mfma_f32_16x16x32_bf16 v[88:91], v[112:115], a[100:103], v[88:91]\n"
      "ds_read_b64_tr_b16 v[112:113], v170 offset:36864\n"
      "buffer_load_dwordx4 a[48:51], v243, s[32:35], 0 offen offset:2048 sc1\n"
      "s_lshl1_add_u32 s52, s31, s52\n"
      "v_mfma_f32_16x16x32_bf16 v[68:71], v[116:119], a[96:99], v[68:71]\n"
      "ds_read_b64_tr_b16 v[114:115], v170 offset:37632\n"
      "v_mfma_f32_16x16x32_bf16 v[92:95], v[116:119], a[100:103], v[92:95]\n"
      "ds_read_b64_tr_b16 v[116:117], v171 offset:36864\n"
      "s_waitcnt lgkmcnt(11)\n"
      "s_mul_i32 s53, s52, 3072\n"
      "v_mfma_f32_16x16x32_bf16 v[64:67], v[120:123], a[104:107], v[64:67]\n"
      "ds_read_b64_tr_b16 v[118:119], v171 offset:37632\n"
      "v_mfma_f32_16x16x32_bf16 v[88:91], v[120:123], a[108:111], v[88:91]\n"
      "ds_read_b64_tr_b16 v[120:121], v170 offset:43008\n"
      "buffer_load_dwordx4 a[52:55], v243, s[32:35], 0 offen offset:3072 sc1\n"
      "s_add_u32 s53, s53, s54\n"
      "v_mfma_f32_16x16x32_bf16 v[68:71], v[124:127], a[104:107], v[68:71]\n"
      "ds_read_b64_tr_b16 v[122:123], v170 offset:43776\n"
      "v_mfma_f32_16x16x32_bf16 v[92:95], v[124:127], a[108:111], v[92:95]\n"
      "ds_read_b64_tr_b16 v[124:125], v171 offset:43008\n"
      "s_waitcnt lgkmcnt(11)\n"
      "v_add_u32 v249, s53, v2\n"
      "v_mfma_f32_16x16x32_bf16 v[64:67], v[128:131], a[112:115], v[64:67]\n"
      "ds_read_b64_tr_b16 v[126:127], v171 offset:43776\n"
      "s_lshl_b32 s52, s52, 14\n"
      "s_waitcnt vmcnt(50)\n"
      "v_mfma_f32_16x16x32_bf16 v[88:91], v[128:131], a[116:119], v[88:91]\n"
      "ds_read_b64_tr_b16 v[128:129], v170 offset:49152\n"
      "buffer_load_dwordx4 a[56:59], v243, s[32:35], s65 offen offset:0 sc1\n"
      "v_mfma_f32_16x16x32_bf16 v[68:71], v[132:135], a[112:115], v[68:71]\n"
      "ds_read_b64_tr_b16 v[130:131], v170 offset:49920\n"
      "s_mov_b64 exec, 0xf\n"
      "v_add_u32 v253, s52, v253\n"
      "s_mov_b64 exec, -1\n"
      "v_mfma_f32_16x16x32_bf16 v[92:95], v[132:135], a[116:119], v[92:95]\n"
      "ds_read_b64_tr_b16 v[132:133], v171 offset:49152\n"
      "s_waitcnt lgkmcnt(11)\n"
      "s_waitcnt vmcnt(50)\n"
      "v_mfma_f32_16x16x32_bf16 v[64:67], v[136:139], a[120:123], v[64:67]\n"
      "ds_read_b64_tr_b16 v[134:135], v171 offset:49920\n"
      "s_bfe_u32 s52, s48, 0x10001\n"
      "s_waitcnt vmcnt(49)\n"
      "v_mfma_f32_16x16x32_bf16 v[88:91], v[136:139], a[124:127], v[88:91]\n"
      "ds_read_b64_tr_b16 v[136:137], v170 offset:55296\n"
      "buffer_load_dwordx4 a[60:63], v243, s[32:35], s65 offen offset:1024 sc1\n"
      "v_mfma_f32_16x16x32_bf16 v[68:71], v[140:143], a[120:123], v[68:71]\n"
      "ds_read_b64_tr_b16 v[138:139], v170 offset:56064\n"
      "s_xor_b32 s52, s52, 1\n"
      "v_mfma_f32_16x16x32_bf16 v[92:95], v[140:143], a[124:127], v[92:95]\n"
      "ds_read_b64_tr_b16 v[140:141], v171 offset:55296\n"
      "s_waitcnt lgkmcnt(11)\n"
      "s_lshl1_add_u32 s52, s31, s52\n"
      "s_waitcnt vmcnt(49)\n"
      "v_mfma_f32_16x16x32_bf16 v[64:67], v[112:115], a[128:131], v[64:67]\n"
      "ds_read_b64_tr_b16 v[142:143], v171 offset:56064\n"
      "s_waitcnt vmcnt(48)\n"
      "v_mfma_f32_16x16x32_bf16 v[88:91], v[112:115], a[132:135], v[88:91]\n"
      "ds_read_b64_tr_b16 v[112:113], v170 offset:12352\n"
      "buffer_load_dwordx4 a[64:67], v243, s[32:35], s65 offen offset:2048 sc1\n"
      "s_add_u32 s52, s52, 8\n"
      "v_mfma_f32_16x16x32_bf16 v[68:71], v[116:119], a[128:131], v[68:71]\n"
      "ds_read_b64_tr_b16 v[114:115], v170 offset:13120\n"
      "v_mfma_f32_16x16x32_bf16 v[92:95], v[116:119], a[132:135], v[92:95]\n"
      "ds_read_b64_tr_b16 v[116:117], v171 offset:12352\n"
      "s_waitcnt lgkmcnt(11)\n"
      "s_mul_i32 s53, s52, 3072\n"
      "s_waitcnt vmcnt(48)\n"
      "v_mfma_f32_16x16x32_bf16 v[64:67], v[120:123], a[136:139], v[64:67]\n"
      "ds_read_b64_tr_b16 v[118:119], v171 offset:13120\n"
      "s_waitcnt vmcnt(15)\n"
      "v_mov_b32 v6, s56\n"
      "s_mov_b64 exec, 3\n"
      "buffer_store_dword v6, v11, s[32:35], s82 offen sc1\n"
      "s_mov_b64 exec, -1\n"
      "v_mfma_f32_16x16x32_bf16 v[88:91], v[120:123], a[140:143], v[88:91]\n"
      "ds_read_b64_tr_b16 v[120:121], v170 offset:18496\n"
      "buffer_load_dwordx4 a[68:71], v243, s[32:35], s65 offen offset:3072 sc1\n"
      "s_add_u32 s53, s53, s54\n"
      "v_mfma_f32_16x16x32_bf16 v[68:71], v[124:127], a[136:139], v[68:71]\n"
      "ds_read_b64_tr_b16 v[122:123], v170 offset:19264\n"
      "v_mfma_f32_16x16x32_bf16 v[92:95], v[124:127], a[140:143], v[92:95]\n"
      "ds_read_b64_tr_b16 v[124:125], v171 offset:18496\n"
      "s_waitcnt lgkmcnt(11)\n"
      "v_add_u32 v252, s53, v2\n"
      "v_mfma_f32_16x16x32_bf16 v[64:67], v[128:131], a[144:147], v[64:67]\n"
      "ds_read_b64_tr_b16 v[126:127], v171 offset:19264\n"
      "s_lshl_b32 s52, s52, 14\n"
      "v_mfma_f32_16x16x32_bf16 v[88:91], v[128:131], a[148:151], v[88:91]\n"
      "ds_read_b64_tr_b16 v[128:129], v170 offset:24640\n"
      "buffer_load_dwordx4 a[72:75], v243, s[32:35], s66 offen offset:0 sc1\n"
      "v_mfma_f32_16x16x32_bf16 v[68:71], v[132:135], a[144:147], v[68:71]\n"
      "ds_read_b64_tr_b16 v[130:131], v170 offset:25408\n"
      "s_mov_b64 exec, 0xf0\n"
      "v_add_u32 v253, s52, v253\n"
      "s_mov_b64 exec, -1\n"
      "v_mfma_f32_16x16x32_bf16 v[92:95], v[132:135], a[148:151], v[92:95]\n"
      "ds_read_b64_tr_b16 v[132:133], v171 offset:24640\n"
      "s_waitcnt lgkmcnt(11)\n"
      "v_mfma_f32_16x16x32_bf16 v[64:67], v[136:139], a[152:155], v[64:67]\n"
      "ds_read_b64_tr_b16 v[134:135], v171 offset:25408\n"
      "s_lshr_b32 s52, s48, 3\n"
      "v_mfma_f32_16x16x32_bf16 v[88:91], v[136:139], a[156:159], v[88:91]\n"
      "ds_read_b64_tr_b16 v[136:137], v170 offset:30784\n"
      "buffer_load_dwordx4 a[76:79], v243, s[32:35], s66 offen offset:1024 sc1\n"
      "v_mfma_f32_16x16x32_bf16 v[68:71], v[140:143], a[152:155], v[68:71]\n"
      "ds_read_b64_tr_b16 v[138:139], v170 offset:31552\n"
      "s_mul_i32 s52, s52, 160\n"
      "v_mfma_f32_16x16x32_bf16 v[92:95], v[140:143], a[156:159], v[92:95]\n"
      "ds_read_b64_tr_b16 v[140:141], v171 offset:30784\n"
      "s_waitcnt lgkmcnt(11)\n"
      "s_mul_i32 s53, s49, 20\n"
      "v_mfma_f32_16x16x32_bf16 v[72:75], v[112:115], a[96:99], v[72:75]\n"
      "ds_read_b64_tr_b16 v[142:143], v171 offset:31552\n"
      "v_mfma_f32_16x16x32_bf16 v[96:99], v[112:115], a[100:103], v[96:99]\n"
      "ds_read_b64_tr_b16 v[112:113], v170 offset:36928\n"
      "buffer_load_dwordx4 a[80:83], v243, s[32:35], s66 offen offset:2048 sc1\n"
      "s_add_u32 s52, s52, s53\n"
      "v_mfma_f32_16x16x32_bf16 v[76:79], v[116:119], a[96:99], v[76:79]\n"
      "ds_read_b64_tr_b16 v[114:115], v170 offset:37696\n"
      "v_mul_f32 v180, v64, |v64|\n"
      "v_mul_f32 v181, v65, |v65|\n"
      "v_mfma_f32_16x16x32_bf16 v[100:103], v[116:119], a[100:103], v[100:103]\n"
      "ds_read_b64_tr_b16 v[116:117], v171 offset:36928\n"
      "s_waitcnt lgkmcnt(11)\n"
      "s_lshl_b32 s52, s52, 7\n"
      "v_mul_f32 v182, v66, |v66|\n"
      "v_mfma_f32_16x16x32_bf16 v[72:75], v[120:123], a[104:107], v[72:75]\n"
      "ds_read_b64_tr_b16 v[118:119], v171 offset:37696\n"
      "v_mul_f32 v183, v67, |v67|\n"
      "v_mfma_f32_16x16x32_bf16 v[96:99], v[120:123], a[108:111], v[96:99]\n"
      "ds_read_b64_tr_b16 v[120:121], v170 offset:43072\n"
      "buffer_load_dwordx4 a[84:87], v243, s[32:35], s66 offen offset:3072 sc1\n"
      "s_add_u32 s52, s52, 130048\n"
      "v_mul_f32 v184, v68, |v68|\n"
      "v_mfma_f32_16x16x32_bf16 v[76:79], v[124:127], a[104:107], v[76:79]\n"
      "ds_read_b64_tr_b16 v[122:123], v170 offset:43840\n"
      "s_mov_b64 exec, 0x0fffff00\n"
      "v_add_u32 v253, s52, v11\n"
      "s_mov_b64 exec, -1\n"
      "v_mul_f32 v185, v69, |v69|\n"
      "v_mfma_f32_16x16x32_bf16 v[100:103], v[124:127], a[108:111], v[100:103]\n"
      "ds_read_b64_tr_b16 v[124:125], v171 offset:43072\n"
      "s_waitcnt lgkmcnt(11)\n"
      "v_mul_f32 v186, v70, |v70|\n"
      "v_mfma_f32_16x16x32_bf16 v[72:75], v[128:131], a[112:115], v[72:75]\n"
      "ds_read_b64_tr_b16 v[126:127], v171 offset:43840\n"
      "s_add_u32 s52, s31, 1\n"
      "v_mul_f32 v187, v71, |v71|\n"
      "v_mfma_f32_16x16x32_bf16 v[96:99], v[128:131], a[116:119], v[96:99]\n"
      "ds_read_b64_tr_b16 v[128:129], v170 offset:49216\n"
      "buffer_load_dwordx4 a[88:91], v243, s[32:35], s67 offen offset:0 sc1\n"
      "v_cvt_pk_bf16_f32 v188, v180, v181\n"
      "v_cvt_pk_bf16_f32 v189, v182, v183\n"
      "v_mfma_f32_16x16x32_bf16 v[76:79], v[132:135], a[112:115], v[76:79]\n"
      "ds_read_b64_tr_b16 v[130:131], v170 offset:49984\n"
      "s_and_b32 s52, s52, 3\n"
      "v_cvt_pk_bf16_f32 v190, v184, v185\n"
      "v_mfma_f32_16x16x32_bf16 v[100:103], v[132:135], a[116:119], v[100:103]\n"
      "ds_read_b64_tr_b16 v[132:133], v171 offset:49216\n"
      "s_waitcnt lgkmcnt(11)\n"
      "v_cvt_pk_bf16_f32 v191, v186, v187\n"
      "v_mfma_f32_16x16x32_bf16 v[72:75], v[136:139], a[120:123], v[72:75]\n"
      "ds_read_b64_tr_b16 v[134:135], v171 offset:49984\n"
      "s_mul_i32 s89, s52, 49152\n"
      "v_pk_max_i16 v188, v188, 0\n"
      "v_mfma_f32_16x16x32_bf16 v[96:99], v[136:139], a[124:127], v[96:99]\n"
      "ds_read_b64_tr_b16 v[136:137], v170 offset:55360\n"
      "buffer_load_dwordx4 a[92:95], v243, s[32:35], s67 offen offset:1024 sc1\n"
      "v_pk_max_i16 v189, v189, 0\n"
      "v_mfma_f32_16x16x32_bf16 v[76:79], v[140:143], a[120:123], v[76:79]\n"
      "ds_read_b64_tr_b16 v[138:139], v170 offset:56128\n"
      "s_add_u32 s52, s31, 2\n"
      "v_pk_max_i16 v190, v190, 0\n"
      "v_mfma_f32_16x16x32_bf16 v[100:103], v[140:143], a[124:127], v[100:103]\n"
      "ds_read_b64_tr_b16 v[140:141], v171 offset:55360\n"
      "s_waitcnt lgkmcnt(11)\n"
      "s_and_b32 s52, s52, 3\n"
      "v_pk_max_i16 v191, v191, 0\n"
      "v_mfma_f32_16x16x32_bf16 v[72:75], v[112:115], a[128:131], v[72:75]\n"
      "ds_read_b64_tr_b16 v[142:143], v171 offset:56128\n"
      "buffer_store_dwordx4 v[188:191], v2, s[32:35], s85 offen offset:0 sc1\n"
      "v_mfma_f32_16x16x32_bf16 v[96:99], v[112:115], a[132:135], v[96:99]\n"
      "ds_read_b64_tr_b16 v[112:113], v170 offset:12416\n"
      "s_mul_i32 s90, s52, 49152\n"
      "v_mul_f32 v192, v88, |v88|\n"
      "v_mul_f32 v193, v89, |v89|\n"
      "v_mfma_f32_16x16x32_bf16 v[76:79], v[116:119], a[128:131], v[76:79]\n"
      "ds_read_b64_tr_b16 v[114:115], v170 offset:13184\n"
      "v_mul_f32 v194, v90, |v90|\n"
      "v_mfma_f32_16x16x32_bf16 v[100:103], v[116:119], a[132:135], v[100:103]\n"
      "ds_read_b64_tr_b16 v[116:117], v171 offset:12416\n"
      "s_waitcnt lgkmcnt(11)\n"
      "s_add_u32 s52, s31, 3\n"
      "v_mul_f32 v195, v91, |v91|\n"
      "v_mfma_f32_16x16x32_bf16 v[72:75], v[120:123], a[136:139], v[72:75]\n"
      "ds_read_b64_tr_b16 v[118:119], v171 offset:13184\n"
      "v_mul_f32 v196, v92, |v92|\n"
      "v_mfma_f32_16x16x32_bf16 v[96:99], v[120:123], a[140:143], v[96:99]\n"
      "ds_read_b64_tr_b16 v[120:121], v170 offset:18560\n"
      "s_and_b32 s52, s52, 3\n"
      "v_mul_f32 v197, v93, |v93|\n"
      "v_mfma_f32_16x16x32_bf16 v[76:79], v[124:127], a[136:139], v[76:79]\n"
      "ds_read_b64_tr_b16 v[122:123], v170 offset:19328\n"
      "s_mul_i32 s91, s52, 49152\n"
      "v_mul_f32 v198, v94, |v94|\n"
      "v_mfma_f32_16x16x32_bf16 v[100:103], v[124:127], a[140:143], v[100:103]\n"
      "ds_read_b64_tr_b16 v[124:125], v171 offset:18560\n"
      "s_waitcnt lgkmcnt(11)\n"
      "v_mul_f32 v199, v95, |v95|\n"
      "v_mfma_f32_16x16x32_bf16 v[72:75], v[128:131], a[144:147], v[72:75]\n"
      "ds_read_b64_tr_b16 v[126:127], v171 offset:19328\n"
      "s_add_u32 s52, s31, 4\n"
      "v_cvt_pk_bf16_f32 v200, v192, v193\n"
      "v_cvt_pk_bf16_f32 v201, v194, v195\n"
      "v_mfma_f32_16x16x32_bf16 v[96:99], v[128:131], a[148:151], v[96:99]\n"
      "ds_read_b64_tr_b16 v[128:129], v170 offset:24704\n"
      "v_cvt_pk_bf16_f32 v202, v196, v197\n"
      "v_mfma_f32_16x16x32_bf16 v[76:79], v[132:135], a[144:147], v[76:79]\n"
      "ds_read_b64_tr_b16 v[130:131], v170 offset:25472\n"
      "s_and_b32 s52, s52, 3\n"
      "v_cvt_pk_bf16_f32 v203, v198, v199\n"
      "v_mfma_f32_16x16x32_bf16 v[100:103], v[132:135], a[148:151], v[100:103]\n"
      "ds_read_b64_tr_b16 v[132:133], v171 offset:24704\n"
      "s_waitcnt lgkmcnt(11)\n"
      "v_pk_max_i16 v200, v200, 0\n"
      "v_mfma_f32_16x16x32_bf16 v[72:75], v[136:139], a[152:155], v[72:75]\n"
      "ds_read_b64_tr_b16 v[134:135], v171 offset:25472\n"
      "s_mul_i32 s95, s52, 49152\n"
      "v_pk_max_i16 v201, v201, 0\n"
      "v_mfma_f32_16x16x32_bf16 v[96:99], v[136:139], a[156:159], v[96:99]\n"
      "ds_read_b64_tr_b16 v[136:137], v170 offset:30848\n"
      "v_pk_max_i16 v202, v202, 0\n"
      "v_mfma_f32_16x16x32_bf16 v[76:79], v[140:143], a[152:155], v[76:79]\n"
      "ds_read_b64_tr_b16 v[138:139], v170 offset:31616\n"
      "v_pk_max_i16 v203, v203, 0\n"
      "v_mfma_f32_16x16x32_bf16 v[100:103], v[140:143], a[156:159], v[100:103]\n"
      "ds_read_b64_tr_b16 v[140:141], v171 offset:30848\n"
      "s_waitcnt lgkmcnt(11)\n"
      "buffer_store_dwordx4 v[200:203], v2, s[32:35], s86 offen offset:0 sc1\n"
      "v_mfma_f32_16x16x32_bf16 v[80:83], v[112:115], a[96:99], v[80:83]\n"
      "ds_read_b64_tr_b16 v[142:143], v171 offset:31616\n"
      "v_mfma_f32_16x16x32_bf16 v[104:107], v[112:115], a[100:103], v[104:107]\n"
      "ds_read_b64_tr_b16 v[112:113], v170 offset:36992\n"
      "v_mfma_f32_16x16x32_bf16 v[84:87], v[116:119], a[96:99], v[84:87]\n"
      "ds_read_b64_tr_b16 v[114:115], v170 offset:37760\n"
      "s_mov_b64 exec, 0xff\n"
      "buffer_load_dword v246, v245, s[32:35], 0 offen sc1\n"
      "s_mov_b64 exec, -1\n"
      "v_mul_f32 v180, v72, |v72|\n"
      "v_mul_f32 v181, v73, |v73|\n"
      "v_mfma_f32_16x16x32_bf16 v[108:111], v[116:119], a[100:103], v[108:111]\n"
      "ds_read_b64_tr_b16 v[116:117], v171 offset:36992\n"
      "s_waitcnt lgkmcnt(11)\n"
      "v_mul_f32 v182, v74, |v74|\n"
      "v_mfma_f32_16x16x32_bf16 v[80:83], v[120:123], a[104:107], v[80:83]\n"
      "ds_read_b64_tr_b16 v[118:119], v171 offset:37760\n"
      "v_mul_f32 v183, v75, |v75|\n"
      "v_mfma_f32_16x16x32_bf16 v[104:107], v[120:123], a[108:111], v[104:107]\n"
      "ds_read_b64_tr_b16 v[120:121], v170 offset:43136\n"
      "v_mul_f32 v184, v76, |v76|\n"
      "v_mfma_f32_16x16x32_bf16 v[84:87], v[124:127], a[104:107], v[84:87]\n"
      "ds_read_b64_tr_b16 v[122:123], v170 offset:43904\n"
      "v_mul_f32 v185, v77, |v77|\n"
      "v_mfma_f32_16x16x32_bf16 v[108:111], v[124:127], a[108:111], v[108:111]\n"
      "ds_read_b64_tr_b16 v[124:125], v171 offset:43136\n"
      "s_waitcnt lgkmcnt(11)\n"
      "v_mul_f32 v186, v78, |v78|\n"
      "v_mfma_f32_16x16x32_bf16 v[80:83], v[128:131], a[112:115], v[80:83]\n"
      "ds_read_b64_tr_b16 v[126:127], v171 offset:43904\n"
      "v_mul_f32 v187, v79, |v79|\n"
      "v_mfma_f32_16x16x32_bf16 v[104:107], v[128:131], a[116:119], v[104:107]\n"
      "ds_read_b64_tr_b16 v[128:129], v170 offset:49280\n"
      "v_cvt_pk_bf16_f32 v188, v180, v181\n"
      "v_cvt_pk_bf16_f32 v189, v182, v183\n"
      "v_mfma_f32_16x16x32_bf16 v[84:87], v[132:135], a[112:115], v[84:87]\n"
      "ds_read_b64_tr_b16 v[130:131], v170 offset:50048\n"
      "v_cvt_pk_bf16_f32 v190, v184, v185\n"
      "v_mfma_f32_16x16x32_bf16 v[108:111], v[132:135], a[116:119], v[108:111]\n"
      "ds_read_b64_tr_b16 v[132:133], v171 offset:49280\n"
      "s_waitcnt lgkmcnt(11)\n"
      "v_cvt_pk_bf16_f32 v191, v186, v187\n"
      "v_mfma_f32_16x16x32_bf16 v[80:83], v[136:139], a[120:123], v[80:83]\n"
      "ds_read_b64_tr_b16 v[134:135], v171 offset:50048\n"
      "v_pk_max_i16 v188, v188, 0\n"
      "v_mfma_f32_16x16x32_bf16 v[104:107], v[136:139], a[124:127], v[104:107]\n"
      "ds_read_b64_tr_b16 v[136:137], v170 offset:55424\n"
      "v_pk_max_i16 v189, v189, 0\n"
      "v_mfma_f32_16x16x32_bf16 v[84:87], v[140:143], a[120:123], v[84:87]\n"
      "ds_read_b64_tr_b16 v[138:139], v170 offset:56192\n"
      "v_pk_max_i16 v190, v190, 0\n"
      "v_mfma_f32_16x16x32_bf16 v[108:111], v[140:143], a[124:127], v[108:111]\n"
      "ds_read_b64_tr_b16 v[140:141], v171 offset:55424\n"
      "s_waitcnt lgkmcnt(11)\n"
      "v_pk_max_i16 v191, v191, 0\n"
      "v_mfma_f32_16x16x32_bf16 v[80:83], v[112:115], a[128:131], v[80:83]\n"
      "ds_read_b64_tr_b16 v[142:143], v171 offset:56192\n"
      "buffer_store_dwordx4 v[188:191], v2, s[32:35], s85 offen offset:1024 sc1\n"
      "v_mfma_f32_16x16x32_bf16 v[104:107], v[112:115], a[132:135], v[104:107]\n"
      "v_mul_f32 v192, v96, |v96|\n"
      "v_mul_f32 v193, v97, |v97|\n"
      "v_mfma_f32_16x16x32_bf16 v[84:87], v[116:119], a[128:131], v[84:87]\n"
      "v_mul_f32 v194, v98, |v98|\n"
      "v_mfma_f32_16x16x32_bf16 v[108:111], v[116:119], a[132:135], v[108:111]\n"
      "s_waitcnt lgkmcnt(8)\n"
      "v_mul_f32 v195, v99, |v99|\n"
      "v_mfma_f32_16x16x32_bf16 v[80:83], v[120:123], a[136:139], v[80:83]\n"
      "v_mul_f32 v196, v100, |v100|\n"
      "v_mfma_f32_16x16x32_bf16 v[104:107], v[120:123], a[140:143], v[104:107]\n"
      "v_mul_f32 v197, v101, |v101|\n"
      "v_mfma_f32_16x16x32_bf16 v[84:87], v[124:127], a[136:139], v[84:87]\n"
      "v_mul_f32 v198, v102, |v102|\n"
      "v_mfma_f32_16x16x32_bf16 v[108:111], v[124:127], a[140:143], v[108:111]\n"
      "s_waitcnt lgkmcnt(4)\n"
      "v_mul_f32 v199, v103, |v103|\n"
      "v_mfma_f32_16x16x32_bf16 v[80:83], v[128:131], a[144:147], v[80:83]\n"
      "v_cvt_pk_bf16_f32 v200, v192, v193\n"
      "v_cvt_pk_bf16_f32 v201, v194, v195\n"
      "v_mfma_f32_16x16x32_bf16 v[104:107], v[128:131], a[148:151], v[104:107]\n"
      "v_cvt_pk_bf16_f32 v202, v196, v197\n"
      "v_mfma_f32_16x16x32_bf16 v[84:87], v[132:135], a[144:147], v[84:87]\n"
      "v_cvt_pk_bf16_f32 v203, v198, v199\n"
      "v_mfma_f32_16x16x32_bf16 v[108:111], v[132:135], a[148:151], v[108:111]\n"
      "s_waitcnt lgkmcnt(0)\n"
      "v_pk_max_i16 v200, v200, 0\n"
      "v_mfma_f32_16x16x32_bf16 v[80:83], v[136:139], a[152:155], v[80:83]\n"
      "v_pk_max_i16 v201, v201, 0\n"
      "v_mfma_f32_16x16x32_bf16 v[104:107], v[136:139], a[156:159], v[104:107]\n"
      "v_pk_max_i16 v202, v202, 0\n"
      "v_mfma_f32_16x16x32_bf16 v[84:87], v[140:143], a[152:155], v[84:87]\n"
      "v_pk_max_i16 v203, v203, 0\n"
      "v_mfma_f32_16x16x32_bf16 v[108:111], v[140:143], a[156:159], v[108:111]\n"
      "buffer_store_dwordx4 v[200:203], v2, s[32:35], s86 offen offset:1024 sc1\n"
      "s_nop 0\n"
      "s_nop 0\n"
      "v_mul_f32 v180, v80, |v80|\n"
      "v_mul_f32 v181, v81, |v81|\n"
      "v_mul_f32 v182, v82, |v82|\n"
      "v_mul_f32 v183, v83, |v83|\n"
      "v_mul_f32 v184, v84, |v84|\n"
      "v_mul_f32 v185, v85, |v85|\n"
      "v_mul_f32 v186, v86, |v86|\n"
      "v_mul_f32 v187, v87, |v87|\n"
      "v_cvt_pk_bf16_f32 v188, v180, v181\n"
      "v_cvt_pk_bf16_f32 v189, v182, v183\n"
      "v_cvt_pk_bf16_f32 v190, v184, v185\n"
      "v_cvt_pk_bf16_f32 v191, v186, v187\n"
      "v_pk_max_i16 v188, v188, 0\n"
      "v_pk_max_i16 v189, v189, 0\n"
      "v_pk_max_i16 v190, v190, 0\n"
      "v_pk_max_i16 v191, v191, 0\n"
      "buffer_store_dwordx4 v[188:191], v2, s[32:35], s85 offen offset:2048 sc1\n"
      "v_mul_f32 v192, v104, |v104|\n"
      "v_mul_f32 v193, v105, |v105|\n"
      "v_mul_f32 v194, v106, |v106|\n"
      "v_mul_f32 v195, v107, |v107|\n"
      "v_mul_f32 v196, v108, |v108|\n"
      "v_mul_f32 v197, v109, |v109|\n"
      "v_mul_f32 v198, v110, |v110|\n"
      "v_mul_f32 v199, v111, |v111|\n"
      "v_cvt_pk_bf16_f32 v200, v192, v193\n"
      "v_cvt_pk_bf16_f32 v201, v194, v195\n"
      "v_cvt_pk_bf16_f32 v202, v196, v197\n"
      "v_cvt_pk_bf16_f32 v203, v198, v199\n"
      "v_pk_max_i16 v200, v200, 0\n"
      "v_pk_max_i16 v201, v201, 0\n"
      "v_pk_max_i16 v202, v202, 0\n"
      "v_pk_max_i16 v203, v203, 0\n"
      "buffer_store_dwordx4 v[200:203], v2, s[32:35], s86 offen offset:2048 sc1\n"
      "s_barrier\n"
      "s_mov_b64 s[92:93], 3\n"
      "K2_JOIN2:\n"
      "s_mov_b32 s36, s4\n"
      "s_and_b32 s37, s5, 0xffff\n"
      "s_mov_b32 s38, 2621440\n"
      "s_mov_b32 s39, 0x20000\n"
      "s_mov_b32 s40, s26\n"
      "s_and_b32 s41, s27, 0xffff\n"
      "s_mov_b32 s42, 2621440\n"
      "s_mov_b32 s43, 0x20000\n"
      "s_lshl_b32 s52, s48, 2\n"
      "s_add_u32 s52, s52, s31\n"
      "s_mul_i32 s52, s52, 20480\n"
      "s_mul_i32 s53, s49, 160\n"
      "s_add_u32 s86, s52, s53\n"
      "v_mul_u32_u24 v13, 1280, v4\n"
      "v_lshl_add_u32 v13, v3, 3, v13\n"
      "v_lshl_add_u32 v254, v3, 3, s53\n"
      "s_waitcnt vmcnt(4)\n"
      "s_mov_b64 exec, 0xff\n"
      "v_cmp_ne_u32_e64 vcc, s56, v246\n"
      "s_mov_b64 exec, -1\n"
      "s_cbranch_vccnz K2_spin_14\n"
      "K2_cont_15:\n"
      "buffer_load_dwordx4 v[16:19], v247, s[32:35], s89 offen offset:0 sc1\n"
      "buffer_load_dwordx4 v[20:23], v247, s[32:35], s89 offen offset:1024 sc1\n"
      "buffer_load_dwordx4 v[24:27], v247, s[32:35], s89 offen offset:2048 sc1\n"
      "s_waitcnt vmcnt(3)\n"
      "v_mov_b32 v6, s56\n"
      "s_mov_b64 exec, s[92:93]\n"
      "buffer_store_dword v6, v11, s[32:35], s88 offen sc1\n"
      "s_mov_b64 exec, -1\n"
      "buffer_load_dwordx4 v[28:31], v248, s[32:35], s89 offen offset:0 sc1\n"
      "buffer_load_dwordx4 v[32:35], v248, s[32:35], s89 offen offset:1024 sc1\n"
      "buffer_load_dwordx4 v[36:39], v248, s[32:35], s89 offen offset:2048 sc1\n"
      "buffer_load_dwordx4 v[40:43], v247, s[32:35], s90 offen offset:0 sc1\n"
      "buffer_load_dwordx4 v[44:47], v247, s[32:35], s90 offen offset:1024 sc1\n"
      "buffer_load_dwordx4 v[48:51], v247, s[32:35], s90 offen offset:2048 sc1\n"
      "s_waitcnt vmcnt(9)\n"
      "v_mfma_f32_16x16x32_bf16 v[112:115], a[160:163], v[16:19], 0\n"
      "buffer_load_dwordx4 v[52:55], v248, s[32:35], s90 offen offset:0 sc1\n"
      "v_mfma_f32_16x16x32_bf16 v[116:119], a[164:167], v[16:19], 0\n"
      "v_mfma_f32_16x16x32_bf16 v[120:123], a[168:171], v[16:19], 0\n"
      "global_load_dwordx2 v[192:193], v254, s[24:25] offset:0\n"
      "global_load_dwordx2 v[194:195], v254, s[24:25] offset:32\n"
      "global_load_dwordx2 v[196:197], v254, s[24:25] offset:64\n"
      "global_load_dwordx2 v[198:199], v254, s[24:25] offset:96\n"
      "global_load_dwordx2 v[200:201], v254, s[24:25] offset:128\n"
      "v_mfma_f32_16x16x32_bf16 v[124:127], a[172:175], v[16:19], 0\n"
      "buffer_load_dwordx4 v[56:59], v248, s[32:35], s90 offen offset:1024 sc1\n"
      "s_lshl_b32 s52, s49, 6\n"
      "s_lshl1_add_u32 s52, s48, s52\n"
      "s_lshr_b32 s53, s31, 1\n"
      "s_add_u32 s52, s52, s53\n"
      "s_mul_i32 s52, s52, 5120\n"
      "s_add_u32 s87, s52, 20971520\n"
      "s_add_u32 s88, s87, 4096\n"
      "s_and_b32 s53, s31, 1\n"
      "s_lshl_b32 s53, s53, 3\n"
      "v_add_u32 v250, s53, v2\n"
      "s_mul_i32 s52, s31, 15360\n"
      "s_add_u32 s52, s52, 36864\n"
      "v_add_u32 v14, s52, v2\n"
      "v_mfma_f32_16x16x32_bf16 v[128:131], a[176:179], v[16:19], 0\n"
      "s_waitcnt vmcnt(15)\n"
      "v_mfma_f32_16x16x32_bf16 v[112:115], a[180:183], v[20:23], v[112:115]\n"
      "v_mfma_f32_16x16x32_bf16 v[116:119], a[184:187], v[20:23], v[116:119]\n"
      "buffer_load_dwordx4 v[60:63], v248, s[32:35], s90 offen offset:2048 sc1\n"
      "v_mfma_f32_16x16x32_bf16 v[120:123], a[188:191], v[20:23], v[120:123]\n"
      "v_mfma_f32_16x16x32_bf16 v[124:127], a[192:195], v[20:23], v[124:127]\n"
      "buffer_load_dwordx4 a[96:99], v243, s[32:35], s67 offen offset:2048 sc1\n"
      "v_mfma_f32_16x16x32_bf16 v[128:131], a[196:199], v[20:23], v[128:131]\n"
      "buffer_load_dwordx4 v[64:67], v247, s[32:35], s91 offen offset:0 sc1\n"
      "s_waitcnt vmcnt(17)\n"
      "v_mfma_f32_16x16x32_bf16 v[112:115], a[200:203], v[24:27], v[112:115]\n"
      "v_mfma_f32_16x16x32_bf16 v[116:119], a[204:207], v[24:27], v[116:119]\n"
      "v_mfma_f32_16x16x32_bf16 v[120:123], a[208:211], v[24:27], v[120:123]\n"
      "buffer_load_dwordx4 v[68:71], v247, s[32:35], s91 offen offset:1024 sc1\n"
      "v_mfma_f32_16x16x32_bf16 v[124:127], a[212:215], v[24:27], v[124:127]\n"
      "buffer_load_dwordx4 a[100:103], v244, s[32:35], 0 offen offset:0 sc1\n"
      "v_mfma_f32_16x16x32_bf16 v[128:131], a[216:219], v[24:27], v[128:131]\n"
      "s_waitcnt vmcnt(17)\n"
      "v_mfma_f32_16x16x32_bf16 v[112:115], a[220:223], v[28:31], v[112:115]\n"
      "buffer_load_dwordx4 v[72:75], v247, s[32:35], s91 offen offset:2048 sc1\n"
      "v_mfma_f32_16x16x32_bf16 v[116:119], a[224:227], v[28:31], v[116:119]\n"
      "v_mfma_f32_16x16x32_bf16 v[120:123], a[228:231], v[28:31], v[120:123]\n"
      "v_mfma_f32_16x16x32_bf16 v[124:127], a[232:235], v[28:31], v[124:127]\n"
      "buffer_load_dwordx4 v[76:79], v248, s[32:35], s91 offen offset:0 sc1\n"
      "buffer_load_dwordx4 a[104:107], v244, s[32:35], 0 offen offset:1024 sc1\n"
      "v_mfma_f32_16x16x32_bf16 v[128:131], a[236:239], v[28:31], v[128:131]\n"
      "s_waitcnt vmcnt(19)\n"
      "v_mfma_f32_16x16x32_bf16 v[112:115], a[0:3], v[32:35], v[112:115]\n"
      "buffer_load_dwordx2 v[222:223], v13, s[36:39], s86 offen offset:0\n"
      "buffer_load_dwordx2 v[224:225], v13, s[36:39], s86 offen offset:32\n"
      "buffer_load_dwordx2 v[226:227], v13, s[36:39], s86 offen offset:64\n"
      "buffer_load_dwordx2 v[228:229], v13, s[36:39], s86 offen offset:96\n"
      "buffer_load_dwordx2 v[230:231], v13, s[36:39], s86 offen offset:128\n"
      "v_mfma_f32_16x16x32_bf16 v[116:119], a[4:7], v[32:35], v[116:119]\n"
      "buffer_load_dwordx4 v[80:83], v248, s[32:35], s91 offen offset:1024 sc1\n"
      "v_mfma_f32_16x16x32_bf16 v[120:123], a[8:11], v[32:35], v[120:123]\n"
      "v_mfma_f32_16x16x32_bf16 v[124:127], a[12:15], v[32:35], v[124:127]\n"
      "buffer_load_dwordx4 a[108:111], v244, s[32:35], 0 offen offset:2048 sc1\n"
      "v_mfma_f32_16x16x32_bf16 v[128:131], a[16:19], v[32:35], v[128:131]\n"
      "buffer_load_dwordx4 v[84:87], v248, s[32:35], s91 offen offset:2048 sc1\n"
      "s_waitcnt vmcnt(26)\n"
      "v_mfma_f32_16x16x32_bf16 v[112:115], a[20:23], v[36:39], v[112:115]\n"
      "v_mfma_f32_16x16x32_bf16 v[116:119], a[24:27], v[36:39], v[116:119]\n"
      "v_mfma_f32_16x16x32_bf16 v[120:123], a[28:31], v[36:39], v[120:123]\n"
      "buffer_load_dwordx4 v[88:91], v247, s[32:35], s95 offen offset:0 sc1\n"
      "v_mfma_f32_16x16x32_bf16 v[124:127], a[32:35], v[36:39], v[124:127]\n"
      "buffer_load_dwordx4 a[112:115], v244, s[32:35], 0 offen offset:3072 sc1\n"
      "v_mfma_f32_16x16x32_bf16 v[128:131], a[36:39], v[36:39], v[128:131]\n"
      "s_waitcnt vmcnt(27)\n"
      "v_mfma_f32_16x16x32_bf16 v[132:135], a[160:163], v[40:43], 0\n"
      "buffer_load_dwordx4 v[92:95], v247, s[32:35], s95 offen offset:1024 sc1\n"
      "v_mfma_f32_16x16x32_bf16 v[136:139], a[164:167], v[40:43], 0\n"
      "v_mfma_f32_16x16x32_bf16 v[140:143], a[168:171], v[40:43], 0\n"
      "v_mfma_f32_16x16x32_bf16 v[144:147], a[172:175], v[40:43], 0\n"
      "buffer_load_dwordx4 v[96:99], v247, s[32:35], s95 offen offset:2048 sc1\n"
      "buffer_load_dwordx4 a[116:119], v244, s[32:35], s65 offen offset:0 sc1\n"
      "v_mfma_f32_16x16x32_bf16 v[148:151], a[176:179], v[40:43], 0\n"
      "s_waitcnt vmcnt(29)\n"
      "v_mfma_f32_16x16x32_bf16 v[132:135], a[180:183], v[44:47], v[132:135]\n"
      "v_mfma_f32_16x16x32_bf16 v[136:139], a[184:187], v[44:47], v[136:139]\n"
      "buffer_load_dwordx4 v[100:103], v248, s[32:35], s95 offen offset:0 sc1\n"
      "v_mfma_f32_16x16x32_bf16 v[140:143], a[188:191], v[44:47], v[140:143]\n"
      "v_mfma_f32_16x16x32_bf16 v[144:147], a[192:195], v[44:47], v[144:147]\n"
      "buffer_load_dwordx4 a[120:123], v244, s[32:35], s65 offen offset:1024 sc1\n"
      "v_mfma_f32_16x16x32_bf16 v[148:151], a[196:199], v[44:47], v[148:151]\n"
      "buffer_load_dwordx4 v[104:107], v248, s[32:35], s95 offen offset:1024 sc1\n"
      "s_waitcnt vmcnt(31)\n"
      "v_mfma_f32_16x16x32_bf16 v[132:135], a[200:203], v[48:51], v[132:135]\n"
      "v_mfma_f32_16x16x32_bf16 v[136:139], a[204:207], v[48:51], v[136:139]\n"
      "v_mfma_f32_16x16x32_bf16 v[140:143], a[208:211], v[48:51], v[140:143]\n"
      "buffer_load_dwordx4 v[108:111], v248, s[32:35], s95 offen offset:2048 sc1\n"
      "v_mfma_f32_16x16x32_bf16 v[144:147], a[212:215], v[48:51], v[144:147]\n"
      "buffer_load_dwordx4 a[124:127], v244, s[32:35], s65 offen offset:2048 sc1\n"
      "v_mfma_f32_16x16x32_bf16 v[148:151], a[216:219], v[48:51], v[148:151]\n"
      "s_waitcnt vmcnt(32)\n"
      "v_mfma_f32_16x16x32_bf16 v[132:135], a[220:223], v[52:55], v[132:135]\n"
      "v_mfma_f32_16x16x32_bf16 v[136:139], a[224:227], v[52:55], v[136:139]\n"
      "v_mfma_f32_16x16x32_bf16 v[140:143], a[228:231], v[52:55], v[140:143]\n"
      "v_mfma_f32_16x16x32_bf16 v[144:147], a[232:235], v[52:55], v[144:147]\n"
      "buffer_load_dwordx4 a[128:131], v244, s[32:35], s65 offen offset:3072 sc1\n"
      "v_mfma_f32_16x16x32_bf16 v[148:151], a[236:239], v[52:55], v[148:151]\n"
      "s_waitcnt vmcnt(27)\n"
      "v_mfma_f32_16x16x32_bf16 v[132:135], a[0:3], v[56:59], v[132:135]\n"
      "v_mfma_f32_16x16x32_bf16 v[136:139], a[4:7], v[56:59], v[136:139]\n"
      "v_mfma_f32_16x16x32_bf16 v[140:143], a[8:11], v[56:59], v[140:143]\n"
      "v_mfma_f32_16x16x32_bf16 v[144:147], a[12:15], v[56:59], v[144:147]\n"
      "buffer_load_dwordx4 a[132:135], v244, s[32:35], s66 offen offset:0 sc1\n"
      "v_mfma_f32_16x16x32_bf16 v[148:151], a[16:19], v[56:59], v[148:151]\n"
      "s_waitcnt vmcnt(27)\n"
      "v_mfma_f32_16x16x32_bf16 v[132:135], a[20:23], v[60:63], v[132:135]\n"
      "v_mfma_f32_16x16x32_bf16 v[136:139], a[24:27], v[60:63], v[136:139]\n"
      "v_mfma_f32_16x16x32_bf16 v[140:143], a[28:31], v[60:63], v[140:143]\n"
      "v_mfma_f32_16x16x32_bf16 v[144:147], a[32:35], v[60:63], v[144:147]\n"
      "buffer_load_dwordx4 a[136:139], v244, s[32:35], s66 offen offset:1024 sc1\n"
      "v_mfma_f32_16x16x32_bf16 v[148:151], a[36:39], v[60:63], v[148:151]\n"
      "s_waitcnt vmcnt(26)\n"
      "v_mfma_f32_16x16x32_bf16 v[152:155], a[160:163], v[64:67], 0\n"
      "v_lshlrev_b32 v202, 16, v192\n"
      "v_mfma_f32_16x16x32_bf16 v[156:159], a[164:167], v[64:67], 0\n"
      "v_and_b32 v203, s63, v192\n"
      "v_mfma_f32_16x16x32_bf16 v[160:163], a[168:171], v[64:67], 0\n"
      "v_lshlrev_b32 v204, 16, v193\n"
      "v_mfma_f32_16x16x32_bf16 v[164:167], a[172:175], v[64:67], 0\n"
      "buffer_load_dwordx4 a[140:143], v244, s[32:35], s66 offen offset:2048 sc1\n"
      "v_mfma_f32_16x16x32_bf16 v[168:171], a[176:179], v[64:67], 0\n"
      "v_and_b32 v205, s63, v193\n"
      "s_waitcnt vmcnt(26)\n"
      "v_mfma_f32_16x16x32_bf16 v[152:155], a[180:183], v[68:71], v[152:155]\n"
      "v_lshlrev_b32 v206, 16, v194\n"
      "v_mfma_f32_16x16x32_bf16 v[156:159], a[184:187], v[68:71], v[156:159]\n"
      "v_mfma_f32_16x16x32_bf16 v[160:163], a[188:191], v[68:71], v[160:163]\n"
      "v_and_b32 v207, s63, v194\n"
      "v_mfma_f32_16x16x32_bf16 v[164:167], a[192:195], v[68:71], v[164:167]\n"
      "v_lshlrev_b32 v208, 16, v195\n"
      "buffer_load_dwordx4 a[144:147], v244, s[32:35], s66 offen offset:3072 sc1\n"
      "v_mfma_f32_16x16x32_bf16 v[168:171], a[196:199], v[68:71], v[168:171]\n"
      "v_and_b32 v209, s63, v195\n"
      "s_waitcnt vmcnt(25)\n"
      "v_mfma_f32_16x16x32_bf16 v[152:155], a[200:203], v[72:75], v[152:155]\n"
      "v_mfma_f32_16x16x32_bf16 v[156:159], a[204:207], v[72:75], v[156:159]\n"
      "v_lshlrev_b32 v210, 16, v196\n"
      "v_mfma_f32_16x16x32_bf16 v[160:163], a[208:211], v[72:75], v[160:163]\n"
      "v_and_b32 v211, s63, v196\n"
      "v_mfma_f32_16x16x32_bf16 v[164:167], a[212:215], v[72:75], v[164:167]\n"
      "buffer_load_dwordx4 a[148:151], v244, s[32:35], s67 offen offset:0 sc1\n"
      "v_mfma_f32_16x16x32_bf16 v[168:171], a[216:219], v[72:75], v[168:171]\n"
      "v_lshlrev_b32 v212, 16, v197\n"
      "s_waitcnt vmcnt(25)\n"
      "v_mfma_f32_16x16x32_bf16 v[152:155], a[220:223], v[76:79], v[152:155]\n"
      "s_mov_b64 exec, 0xfffffff\n"
      "buffer_load_dword v246, v253, s[32:35], 0 offen sc1\n"
      "s_mov_b64 exec, -1\n"
      "v_and_b32 v213, s63, v197\n"
      "v_mfma_f32_16x16x32_bf16 v[156:159], a[224:227], v[76:79], v[156:159]\n"
      "v_lshlrev_b32 v214, 16, v198\n"
      "v_mfma_f32_16x16x32_bf16 v[160:163], a[228:231], v[76:79], v[160:163]\n"
      "v_mfma_f32_16x16x32_bf16 v[164:167], a[232:235], v[76:79], v[164:167]\n"
      "v_and_b32 v215, s63, v198\n"
      "buffer_load_dwordx4 a[152:155], v244, s[32:35], s67 offen offset:1024 sc1\n"
      "v_mfma_f32_16x16x32_bf16 v[168:171], a[236:239], v[76:79], v[168:171]\n"
      "v_lshlrev_b32 v216, 16, v199\n"
      "s_waitcnt vmcnt(20)\n"
      "v_mfma_f32_16x16x32_bf16 v[152:155], a[0:3], v[80:83], v[152:155]\n"
      "v_mfma_f32_16x16x32_bf16 v[156:159], a[4:7], v[80:83], v[156:159]\n"
      "v_and_b32 v217, s63, v199\n"
      "v_mfma_f32_16x16x32_bf16 v[160:163], a[8:11], v[80:83], v[160:163]\n"
      "v_lshlrev_b32 v218, 16, v200\n"
      "v_mfma_f32_16x16x32_bf16 v[164:167], a[12:15], v[80:83], v[164:167]\n"
      "v_and_b32 v219, s63, v200\n"
      "buffer_load_dwordx4 a[156:159], v244, s[32:35], s67 offen offset:2048 sc1\n"
      "v_mfma_f32_16x16x32_bf16 v[168:171], a[16:19], v[80:83], v[168:171]\n"
      "s_waitcnt vmcnt(19)\n"
      "v_mfma_f32_16x16x32_bf16 v[152:155], a[20:23], v[84:87], v[152:155]\n"
      "v_lshlrev_b32 v220, 16, v201\n"
      "v_mfma_f32_16x16x32_bf16 v[156:159], a[24:27], v[84:87], v[156:159]\n"
      "v_and_b32 v221, s63, v201\n"
      "v_mfma_f32_16x16x32_bf16 v[160:163], a[28:31], v[84:87], v[160:163]\n"
      "v_mfma_f32_16x16x32_bf16 v[164:167], a[32:35], v[84:87], v[164:167]\n"
      "v_mfma_f32_16x16x32_bf16 v[168:171], a[36:39], v[84:87], v[168:171]\n"
      "s_waitcnt vmcnt(18)\n"
      "v_mfma_f32_16x16x32_bf16 v[172:175], a[160:163], v[88:91], v[202:205]\n"
      "v_mfma_f32_16x16x32_bf16 v[176:179], a[164:167], v[88:91], v[206:209]\n"
      "v_mfma_f32_16x16x32_bf16 v[180:183], a[168:171], v[88:91], v[210:213]\n"
      "v_mfma_f32_16x16x32_bf16 v[184:187], a[172:175], v[88:91], v[214:217]\n"
      "v_mfma_f32_16x16x32_bf16 v[188:191], a[176:179], v[88:91], v[218:221]\n"
      "s_waitcnt vmcnt(16)\n"
      "v_mfma_f32_16x16x32_bf16 v[172:175], a[180:183], v[92:95], v[172:175]\n"
      "v_mfma_f32_16x16x32_bf16 v[176:179], a[184:187], v[92:95], v[176:179]\n"
      "v_mfma_f32_16x16x32_bf16 v[180:183], a[188:191], v[92:95], v[180:183]\n"
      "v_mfma_f32_16x16x32_bf16 v[184:187], a[192:195], v[92:95], v[184:187]\n"
      "v_mfma_f32_16x16x32_bf16 v[188:191], a[196:199], v[92:95], v[188:191]\n"
      "s_waitcnt vmcnt(15)\n"
      "v_mfma_f32_16x16x32_bf16 v[172:175], a[200:203], v[96:99], v[172:175]\n"
      "v_mfma_f32_16x16x32_bf16 v[176:179], a[204:207], v[96:99], v[176:179]\n"
      "v_mfma_f32_16x16x32_bf16 v[180:183], a[208:211], v[96:99], v[180:183]\n"
      "v_mfma_f32_16x16x32_bf16 v[184:187], a[212:215], v[96:99], v[184:187]\n"
      "v_mfma_f32_16x16x32_bf16 v[188:191], a[216:219], v[96:99], v[188:191]\n"
      "s_waitcnt vmcnt(2)\n"
      "s_mov_b64 exec, 0xfffffff\n"
      "v_cmp_ne_u32 vcc, s56, v246\n"
      "s_mov_b64 exec, -1\n"
      "s_cbranch_vccnz K2_spin_16\n"
      "K2_cont_17:\n"
      "buffer_load_dwordx4 v[16:19], v249, s[32:35], s89 offen offset:0 sc1\n"
      "v_mfma_f32_16x16x32_bf16 v[172:175], a[220:223], v[100:103], v[172:175]\n"
      "buffer_load_dwordx4 v[20:23], v249, s[32:35], s89 offen offset:1024 sc1\n"
      "v_mfma_f32_16x16x32_bf16 v[176:179], a[224:227], v[100:103], v[176:179]\n"
      "buffer_load_dwordx4 v[24:27], v249, s[32:35], s89 offen offset:2048 sc1\n"
      "v_mfma_f32_16x16x32_bf16 v[180:183], a[228:231], v[100:103], v[180:183]\n"
      "buffer_load_dwordx4 v[28:31], v249, s[32:35], s90 offen offset:0 sc1\n"
      "v_mfma_f32_16x16x32_bf16 v[184:187], a[232:235], v[100:103], v[184:187]\n"
      "buffer_load_dwordx4 v[32:35], v249, s[32:35], s90 offen offset:1024 sc1\n"
      "v_mfma_f32_16x16x32_bf16 v[188:191], a[236:239], v[100:103], v[188:191]\n"
      "buffer_load_dwordx4 v[100:103], v252, s[32:35], s95 offen offset:0 sc1\n"
      "buffer_load_dwordx4 v[36:39], v249, s[32:35], s90 offen offset:2048 sc1\n"
      "v_mfma_f32_16x16x32_bf16 v[172:175], a[0:3], v[104:107], v[172:175]\n"
      "buffer_load_dwordx4 v[40:43], v249, s[32:35], s91 offen offset:0 sc1\n"
      "v_mfma_f32_16x16x32_bf16 v[176:179], a[4:7], v[104:107], v[176:179]\n"
      "buffer_load_dwordx4 v[44:47], v249, s[32:35], s91 offen offset:1024 sc1\n"
      "v_mfma_f32_16x16x32_bf16 v[180:183], a[8:11], v[104:107], v[180:183]\n"
      "buffer_load_dwordx4 v[48:51], v249, s[32:35], s91 offen offset:2048 sc1\n"
      "v_mfma_f32_16x16x32_bf16 v[184:187], a[12:15], v[104:107], v[184:187]\n"
      "buffer_load_dwordx4 v[52:55], v252, s[32:35], s89 offen offset:0 sc1\n"
      "v_mfma_f32_16x16x32_bf16 v[188:191], a[16:19], v[104:107], v[188:191]\n"
      "buffer_load_dwordx4 v[104:107], v252, s[32:35], s95 offen offset:1024 sc1\n"
      "buffer_load_dwordx4 v[56:59], v252, s[32:35], s89 offen offset:1024 sc1\n"
      "v_mfma_f32_16x16x32_bf16 v[172:175], a[20:23], v[108:111], v[172:175]\n"
      "buffer_load_dwordx4 v[60:63], v252, s[32:35], s89 offen offset:2048 sc1\n"
      "v_mfma_f32_16x16x32_bf16 v[176:179], a[24:27], v[108:111], v[176:179]\n"
      "buffer_load_dwordx4 v[64:67], v252, s[32:35], s90 offen offset:0 sc1\n"
      "v_mfma_f32_16x16x32_bf16 v[180:183], a[28:31], v[108:111], v[180:183]\n"
      "buffer_load_dwordx4 v[68:71], v252, s[32:35], s90 offen offset:1024 sc1\n"
      "v_mfma_f32_16x16x32_bf16 v[184:187], a[32:35], v[108:111], v[184:187]\n"
      "buffer_load_dwordx4 v[72:75], v252, s[32:35], s90 offen offset:2048 sc1\n"
      "v_mfma_f32_16x16x32_bf16 v[188:191], a[36:39], v[108:111], v[188:191]\n"
      "buffer_load_dwordx4 v[108:111], v252, s[32:35], s95 offen offset:2048 sc1\n"
      "buffer_load_dwordx4 v[76:79], v252, s[32:35], s91 offen offset:0 sc1\n"
      "s_waitcnt vmcnt(18)\n"
      "v_mfma_f32_16x16x32_bf16 v[112:115], a[40:43], v[16:19], v[112:115]\n"
      "buffer_load_dwordx4 v[80:83], v252, s[32:35], s91 offen offset:1024 sc1\n"
      "v_mfma_f32_16x16x32_bf16 v[116:119], a[44:47], v[16:19], v[116:119]\n"
      "buffer_load_dwordx4 v[84:87], v252, s[32:35], s91 offen offset:2048 sc1\n"
      "v_mfma_f32_16x16x32_bf16 v[120:123], a[48:51], v[16:19], v[120:123]\n"
      "buffer_load_dwordx4 v[88:91], v249, s[32:35], s95 offen offset:0 sc1\n"
      "v_mfma_f32_16x16x32_bf16 v[124:127], a[52:55], v[16:19], v[124:127]\n"
      "buffer_load_dwordx4 v[92:95], v249, s[32:35], s95 offen offset:1024 sc1\n"
      "v_mfma_f32_16x16x32_bf16 v[128:131], a[56:59], v[16:19], v[128:131]\n"
      "buffer_load_dwordx4 v[96:99], v249, s[32:35], s95 offen offset:2048 sc1\n"
      "s_waitcnt vmcnt(22)\n"
      "v_mfma_f32_16x16x32_bf16 v[112:115], a[60:63], v[20:23], v[112:115]\n"
      "v_mfma_f32_16x16x32_bf16 v[116:119], a[64:67], v[20:23], v[116:119]\n"
      "v_mfma_f32_16x16x32_bf16 v[120:123], a[68:71], v[20:23], v[120:123]\n"
      "v_mfma_f32_16x16x32_bf16 v[124:127], a[72:75], v[20:23], v[124:127]\n"
      "v_mfma_f32_16x16x32_bf16 v[128:131], a[76:79], v[20:23], v[128:131]\n"
      "s_waitcnt vmcnt(21)\n"
      "v_mfma_f32_16x16x32_bf16 v[112:115], a[80:83], v[24:27], v[112:115]\n"
      "v_mfma_f32_16x16x32_bf16 v[116:119], a[84:87], v[24:27], v[116:119]\n"
      "v_mfma_f32_16x16x32_bf16 v[120:123], a[88:91], v[24:27], v[120:123]\n"
      "v_mfma_f32_16x16x32_bf16 v[124:127], a[92:95], v[24:27], v[124:127]\n"
      "v_mfma_f32_16x16x32_bf16 v[128:131], a[96:99], v[24:27], v[128:131]\n"
      "s_waitcnt vmcnt(20)\n"
      "v_mfma_f32_16x16x32_bf16 v[132:135], a[40:43], v[28:31], v[132:135]\n"
      "v_mfma_f32_16x16x32_bf16 v[136:139], a[44:47], v[28:31], v[136:139]\n"
      "v_mfma_f32_16x16x32_bf16 v[140:143], a[48:51], v[28:31], v[140:143]\n"
      "v_mfma_f32_16x16x32_bf16 v[144:147], a[52:55], v[28:31], v[144:147]\n"
      "v_mfma_f32_16x16x32_bf16 v[148:151], a[56:59], v[28:31], v[148:151]\n"
      "s_waitcnt vmcnt(19)\n"
      "v_mfma_f32_16x16x32_bf16 v[132:135], a[60:63], v[32:35], v[132:135]\n"
      "v_mfma_f32_16x16x32_bf16 v[136:139], a[64:67], v[32:35], v[136:139]\n"
      "v_mfma_f32_16x16x32_bf16 v[140:143], a[68:71], v[32:35], v[140:143]\n"
      "buffer_load_dwordx2 v[232:233], v250, s[32:35], s87 offen offset:0 sc1\n"
      "buffer_load_dwordx2 v[234:235], v250, s[32:35], s87 offen offset:1024 sc1\n"
      "buffer_load_dwordx2 v[236:237], v250, s[32:35], s87 offen offset:2048 sc1\n"
      "buffer_load_dwordx2 v[238:239], v250, s[32:35], s87 offen offset:3072 sc1\n"
      "buffer_load_dwordx2 v[240:241], v250, s[32:35], s88 offen offset:0 sc1\n"
      "v_mfma_f32_16x16x32_bf16 v[144:147], a[72:75], v[32:35], v[144:147]\n"
      "v_mfma_f32_16x16x32_bf16 v[148:151], a[76:79], v[32:35], v[148:151]\n"
      "s_waitcnt vmcnt(22)\n"
      "v_mfma_f32_16x16x32_bf16 v[132:135], a[80:83], v[36:39], v[132:135]\n"
      "v_mfma_f32_16x16x32_bf16 v[136:139], a[84:87], v[36:39], v[136:139]\n"
      "v_mfma_f32_16x16x32_bf16 v[140:143], a[88:91], v[36:39], v[140:143]\n"
      "v_mfma_f32_16x16x32_bf16 v[144:147], a[92:95], v[36:39], v[144:147]\n"
      "v_mfma_f32_16x16x32_bf16 v[148:151], a[96:99], v[36:39], v[148:151]\n"
      "s_waitcnt vmcnt(21)\n"
      "v_mfma_f32_16x16x32_bf16 v[152:155], a[40:43], v[40:43], v[152:155]\n"
      "s_lshl2_add_u32 s52, s30, s31\n"
      "s_cmp_eq_u32 s52, 1020\n"
      "s_cbranch_scc0 K2_noei_18\n"
      "s_nop 0\n"
      "s_mov_b32 s82, 65536\n"
      "s_mov_b32 s83, 73728\n"
      "s_mov_b32 s84, 81920\n"
      "s_mov_b32 s85, 90112\n"
      "buffer_load_dword v246, v11, s[32:35], s82 offen sc1\n"
      "buffer_load_dword v247, v11, s[32:35], s83 offen sc1\n"
      "buffer_load_dword v248, v11, s[32:35], s84 offen sc1\n"
      "buffer_load_dword v249, v11, s[32:35], s85 offen sc1\n"
      "K2_noei_18:\n"
      "v_mfma_f32_16x16x32_bf16 v[156:159], a[44:47], v[40:43], v[156:159]\n"
      "v_mfma_f32_16x16x32_bf16 v[160:163], a[48:51], v[40:43], v[160:163]\n"
      "v_mfma_f32_16x16x32_bf16 v[164:167], a[52:55], v[40:43], v[164:167]\n"
      "v_mfma_f32_16x16x32_bf16 v[168:171], a[56:59], v[40:43], v[168:171]\n"
      "s_waitcnt vmcnt(20)\n"
      "v_mfma_f32_16x16x32_bf16 v[152:155], a[60:63], v[44:47], v[152:155]\n"
      "v_mfma_f32_16x16x32_bf16 v[156:159], a[64:67], v[44:47], v[156:159]\n"
      "v_mfma_f32_16x16x32_bf16 v[160:163], a[68:71], v[44:47], v[160:163]\n"
      "v_mfma_f32_16x16x32_bf16 v[164:167], a[72:75], v[44:47], v[164:167]\n"
      "v_mfma_f32_16x16x32_bf16 v[168:171], a[76:79], v[44:47], v[168:171]\n"
      "s_waitcnt vmcnt(19)\n"
      "v_mfma_f32_16x16x32_bf16 v[152:155], a[80:83], v[48:51], v[152:155]\n"
      "v_mfma_f32_16x16x32_bf16 v[156:159], a[84:87], v[48:51], v[156:159]\n"
      "v_mfma_f32_16x16x32_bf16 v[160:163], a[88:91], v[48:51], v[160:163]\n"
      "v_mfma_f32_16x16x32_bf16 v[164:167], a[92:95], v[48:51], v[164:167]\n"
      "v_mfma_f32_16x16x32_bf16 v[168:171], a[96:99], v[48:51], v[168:171]\n"
      "s_waitcnt vmcnt(18)\n"
      "v_mfma_f32_16x16x32_bf16 v[112:115], a[100:103], v[52:55], v[112:115]\n"
      "v_mfma_f32_16x16x32_bf16 v[116:119], a[104:107], v[52:55], v[116:119]\n"
      "v_mfma_f32_16x16x32_bf16 v[120:123], a[108:111], v[52:55], v[120:123]\n"
      "v_mfma_f32_16x16x32_bf16 v[124:127], a[112:115], v[52:55], v[124:127]\n"
      "v_mfma_f32_16x16x32_bf16 v[128:131], a[116:119], v[52:55], v[128:131]\n"
      "s_waitcnt vmcnt(16)\n"
      "v_mfma_f32_16x16x32_bf16 v[112:115], a[120:123], v[56:59], v[112:115]\n"
      "v_mfma_f32_16x16x32_bf16 v[116:119], a[124:127], v[56:59], v[116:119]\n"
      "v_mfma_f32_16x16x32_bf16 v[120:123], a[128:131], v[56:59], v[120:123]\n"
      "v_mfma_f32_16x16x32_bf16 v[124:127], a[132:135], v[56:59], v[124:127]\n"
      "v_mfma_f32_16x16x32_bf16 v[128:131], a[136:139], v[56:59], v[128:131]\n"
      "s_waitcnt vmcnt(15)\n"
      "v_mfma_f32_16x16x32_bf16 v[112:115], a[140:143], v[60:63], v[112:115]\n"
      "v_mfma_f32_16x16x32_bf16 v[116:119], a[144:147], v[60:63], v[116:119]\n"
      "v_mfma_f32_16x16x32_bf16 v[120:123], a[148:151], v[60:63], v[120:123]\n"
      "v_mfma_f32_16x16x32_bf16 v[124:127], a[152:155], v[60:63], v[124:127]\n"
      "v_mfma_f32_16x16x32_bf16 v[128:131], a[156:159], v[60:63], v[128:131]\n"
      "s_waitcnt vmcnt(14)\n"
      "v_mfma_f32_16x16x32_bf16 v[132:135], a[100:103], v[64:67], v[132:135]\n"
      "v_mfma_f32_16x16x32_bf16 v[136:139], a[104:107], v[64:67], v[136:139]\n"
      "v_mfma_f32_16x16x32_bf16 v[140:143], a[108:111], v[64:67], v[140:143]\n"
      "s_add_u32 s52, s31, 1\n"
      "s_and_b32 s52, s52, 3\n"
      "s_mul_i32 s52, s52, 15360\n"
      "s_add_u32 s52, s52, 36864\n"
      "v_add_u32 v251, s52, v2\n"
      "v_mfma_f32_16x16x32_bf16 v[144:147], a[112:115], v[64:67], v[144:147]\n"
      "ds_write_b128 v251, v[112:115] offset:0\n"
      "v_mfma_f32_16x16x32_bf16 v[148:151], a[116:119], v[64:67], v[148:151]\n"
      "ds_write_b128 v251, v[116:119] offset:1024\n"
      "s_waitcnt vmcnt(13)\n"
      "v_mfma_f32_16x16x32_bf16 v[132:135], a[120:123], v[68:71], v[132:135]\n"
      "ds_write_b128 v251, v[120:123] offset:2048\n"
      "v_mfma_f32_16x16x32_bf16 v[136:139], a[124:127], v[68:71], v[136:139]\n"
      "ds_write_b128 v251, v[124:127] offset:3072\n"
      "v_mfma_f32_16x16x32_bf16 v[140:143], a[128:131], v[68:71], v[140:143]\n"
      "ds_write_b128 v251, v[128:131] offset:4096\n"
      "v_mfma_f32_16x16x32_bf16 v[144:147], a[132:135], v[68:71], v[144:147]\n"
      "v_mfma_f32_16x16x32_bf16 v[148:151], a[136:139], v[68:71], v[148:151]\n"
      "v_lshlrev_b32 v112, 16, v222\n"
      "v_and_b32 v113, s63, v222\n"
      "s_waitcnt vmcnt(12)\n"
      "v_mfma_f32_16x16x32_bf16 v[132:135], a[140:143], v[72:75], v[132:135]\n"
      "v_lshlrev_b32 v114, 16, v223\n"
      "v_mfma_f32_16x16x32_bf16 v[136:139], a[144:147], v[72:75], v[136:139]\n"
      "v_and_b32 v115, s63, v223\n"
      "v_lshlrev_b32 v116, 16, v224\n"
      "v_mfma_f32_16x16x32_bf16 v[140:143], a[148:151], v[72:75], v[140:143]\n"
      "v_and_b32 v117, s63, v224\n"
      "v_mfma_f32_16x16x32_bf16 v[144:147], a[152:155], v[72:75], v[144:147]\n"
      "v_lshlrev_b32 v118, 16, v225\n"
      "v_and_b32 v119, s63, v225\n"
      "v_mfma_f32_16x16x32_bf16 v[148:151], a[156:159], v[72:75], v[148:151]\n"
      "v_lshlrev_b32 v120, 16, v226\n"
      "s_waitcnt vmcnt(10)\n"
      "v_mfma_f32_16x16x32_bf16 v[152:155], a[100:103], v[76:79], v[152:155]\n"
      "v_and_b32 v121, s63, v226\n"
      "v_mfma_f32_16x16x32_bf16 v[156:159], a[104:107], v[76:79], v[156:159]\n"
      "v_lshlrev_b32 v122, 16, v227\n"
      "v_and_b32 v123, s63, v227\n"
      "v_mfma_f32_16x16x32_bf16 v[160:163], a[108:111], v[76:79], v[160:163]\n"
      "v_lshlrev_b32 v124, 16, v228\n"
      "s_add_u32 s52, s31, 2\n"
      "s_and_b32 s52, s52, 3\n"
      "s_mul_i32 s52, s52, 15360\n"
      "s_add_u32 s52, s52, 41984\n"
      "v_add_u32 v251, s52, v2\n"
      "v_mfma_f32_16x16x32_bf16 v[164:167], a[112:115], v[76:79], v[164:167]\n"
      "v_and_b32 v125, s63, v228\n"
      "v_lshlrev_b32 v126, 16, v229\n"
      "ds_write_b128 v251, v[132:135] offset:0\n"
      "v_mfma_f32_16x16x32_bf16 v[168:171], a[116:119], v[76:79], v[168:171]\n"
      "v_and_b32 v127, s63, v229\n"
      "ds_write_b128 v251, v[136:139] offset:1024\n"
      "s_waitcnt vmcnt(9)\n"
      "v_mfma_f32_16x16x32_bf16 v[152:155], a[120:123], v[80:83], v[152:155]\n"
      "v_lshlrev_b32 v128, 16, v230\n"
      "v_and_b32 v129, s63, v230\n"
      "ds_write_b128 v251, v[140:143] offset:2048\n"
      "v_mfma_f32_16x16x32_bf16 v[156:159], a[124:127], v[80:83], v[156:159]\n"
      "v_lshlrev_b32 v130, 16, v231\n"
      "ds_write_b128 v251, v[144:147] offset:3072\n"
      "v_mfma_f32_16x16x32_bf16 v[160:163], a[128:131], v[80:83], v[160:163]\n"
      "v_and_b32 v131, s63, v231\n"
      "ds_write_b128 v251, v[148:151] offset:4096\n"
      "v_mfma_f32_16x16x32_bf16 v[164:167], a[132:135], v[80:83], v[164:167]\n"
      "v_mfma_f32_16x16x32_bf16 v[168:171], a[136:139], v[80:83], v[168:171]\n"
      "s_waitcnt vmcnt(4)\n"
      "v_lshlrev_b32 v132, 16, v232\n"
      "v_and_b32 v133, s63, v232\n"
      "v_mfma_f32_16x16x32_bf16 v[152:155], a[140:143], v[84:87], v[152:155]\n"
      "v_lshlrev_b32 v134, 16, v233\n"
      "v_mfma_f32_16x16x32_bf16 v[156:159], a[144:147], v[84:87], v[156:159]\n"
      "v_and_b32 v135, s63, v233\n"
      "s_waitcnt vmcnt(3)\n"
      "v_lshlrev_b32 v136, 16, v234\n"
      "v_mfma_f32_16x16x32_bf16 v[160:163], a[148:151], v[84:87], v[160:163]\n"
      "v_and_b32 v137, s63, v234\n"
      "v_mfma_f32_16x16x32_bf16 v[164:167], a[152:155], v[84:87], v[164:167]\n"
      "v_lshlrev_b32 v138, 16, v235\n"
      "v_and_b32 v139, s63, v235\n"
      "v_mfma_f32_16x16x32_bf16 v[168:171], a[156:159], v[84:87], v[168:171]\n"
      "s_waitcnt vmcnt(2)\n"
      "v_lshlrev_b32 v140, 16, v236\n"
      "v_mfma_f32_16x16x32_bf16 v[172:175], a[40:43], v[88:91], v[172:175]\n"
      "v_and_b32 v141, s63, v236\n"
      "v_mfma_f32_16x16x32_bf16 v[176:179], a[44:47], v[88:91], v[176:179]\n"
      "v_lshlrev_b32 v142, 16, v237\n"
      "v_and_b32 v143, s63, v237\n"
      "v_mfma_f32_16x16x32_bf16 v[180:183], a[48:51], v[88:91], v[180:183]\n"
      "s_waitcnt vmcnt(1)\n"
      "v_lshlrev_b32 v144, 16, v238\n"
      "s_add_u32 s52, s31, 3\n"
      "s_and_b32 s52, s52, 3\n"
      "s_mul_i32 s52, s52, 15360\n"
      "s_add_u32 s52, s52, 47104\n"
      "v_add_u32 v251, s52, v2\n"
      "v_mfma_f32_16x16x32_bf16 v[184:187], a[52:55], v[88:91], v[184:187]\n"
      "v_and_b32 v145, s63, v238\n"
      "v_lshlrev_b32 v146, 16, v239\n"
      "ds_write_b128 v251, v[152:155] offset:0\n"
      "v_mfma_f32_16x16x32_bf16 v[188:191], a[56:59], v[88:91], v[188:191]\n"
      "v_and_b32 v147, s63, v239\n"
      "ds_write_b128 v251, v[156:159] offset:1024\n"
      "v_mfma_f32_16x16x32_bf16 v[172:175], a[60:63], v[92:95], v[172:175]\n"
      "s_waitcnt vmcnt(0)\n"
      "v_lshlrev_b32 v148, 16, v240\n"
      "v_and_b32 v149, s63, v240\n"
      "ds_write_b128 v251, v[160:163] offset:2048\n"
      "v_mfma_f32_16x16x32_bf16 v[176:179], a[64:67], v[92:95], v[176:179]\n"
      "v_lshlrev_b32 v150, 16, v241\n"
      "ds_write_b128 v251, v[164:167] offset:3072\n"
      "v_mfma_f32_16x16x32_bf16 v[180:183], a[68:71], v[92:95], v[180:183]\n"
      "v_and_b32 v151, s63, v241\n"
      "ds_write_b128 v251, v[168:171] offset:4096\n"
      "v_mfma_f32_16x16x32_bf16 v[184:187], a[72:75], v[92:95], v[184:187]\n"
      "v_mfma_f32_16x16x32_bf16 v[188:191], a[76:79], v[92:95], v[188:191]\n"
      "s_waitcnt lgkmcnt(0)\n"
      "s_barrier\n"
      "v_mfma_f32_16x16x32_bf16 v[172:175], a[80:83], v[96:99], v[172:175]\n"
      "ds_read_b128 v[16:19], v14 offset:0\n"
      "v_mfma_f32_16x16x32_bf16 v[176:179], a[84:87], v[96:99], v[176:179]\n"
      "ds_read_b128 v[36:39], v14 offset:5120\n"
      "v_mfma_f32_16x16x32_bf16 v[180:183], a[88:91], v[96:99], v[180:183]\n"
      "ds_read_b128 v[56:59], v14 offset:10240\n"
      "v_mfma_f32_16x16x32_bf16 v[184:187], a[92:95], v[96:99], v[184:187]\n"
      "ds_read_b128 v[20:23], v14 offset:1024\n"
      "v_mfma_f32_16x16x32_bf16 v[188:191], a[96:99], v[96:99], v[188:191]\n"
      "ds_read_b128 v[40:43], v14 offset:6144\n"
      "v_mfma_f32_16x16x32_bf16 v[172:175], a[100:103], v[100:103], v[172:175]\n"
      "ds_read_b128 v[60:63], v14 offset:11264\n"
      "v_mfma_f32_16x16x32_bf16 v[176:179], a[104:107], v[100:103], v[176:179]\n"
      "ds_read_b128 v[24:27], v14 offset:2048\n"
      "v_mfma_f32_16x16x32_bf16 v[180:183], a[108:111], v[100:103], v[180:183]\n"
      "ds_read_b128 v[44:47], v14 offset:7168\n"
      "v_mfma_f32_16x16x32_bf16 v[184:187], a[112:115], v[100:103], v[184:187]\n"
      "ds_read_b128 v[64:67], v14 offset:12288\n"
      "v_mfma_f32_16x16x32_bf16 v[188:191], a[116:119], v[100:103], v[188:191]\n"
      "ds_read_b128 v[28:31], v14 offset:3072\n"
      "v_mfma_f32_16x16x32_bf16 v[172:175], a[120:123], v[104:107], v[172:175]\n"
      "ds_read_b128 v[48:51], v14 offset:8192\n"
      "v_mfma_f32_16x16x32_bf16 v[176:179], a[124:127], v[104:107], v[176:179]\n"
      "ds_read_b128 v[68:71], v14 offset:13312\n"
      "v_mfma_f32_16x16x32_bf16 v[180:183], a[128:131], v[104:107], v[180:183]\n"
      "ds_read_b128 v[32:35], v14 offset:4096\n"
      "v_mfma_f32_16x16x32_bf16 v[184:187], a[132:135], v[104:107], v[184:187]\n"
      "ds_read_b128 v[52:55], v14 offset:9216\n"
      "v_mfma_f32_16x16x32_bf16 v[188:191], a[136:139], v[104:107], v[188:191]\n"
      "ds_read_b128 v[72:75], v14 offset:14336\n"
      "v_mfma_f32_16x16x32_bf16 v[172:175], a[140:143], v[108:111], v[172:175]\n"
      "v_mfma_f32_16x16x32_bf16 v[176:179], a[144:147], v[108:111], v[176:179]\n"
      "v_mfma_f32_16x16x32_bf16 v[180:183], a[148:151], v[108:111], v[180:183]\n"
      "v_mfma_f32_16x16x32_bf16 v[184:187], a[152:155], v[108:111], v[184:187]\n"
      "v_mfma_f32_16x16x32_bf16 v[188:191], a[156:159], v[108:111], v[188:191]\n"
      "s_mov_b32 s70, 0xbfb8aa3b\n"
      "s_mov_b32 s71, 0xbfb8aa3b\n"
      "s_mov_b32 s72, 1.0\n"
      "s_mov_b32 s73, 1.0\n"
      "s_waitcnt lgkmcnt(14)\n"
      "v_pk_add_f32 v[172:173], v[172:173], v[16:17]\n"
      "v_pk_add_f32 v[174:175], v[174:175], v[18:19]\n"
      "s_waitcnt lgkmcnt(11)\n"
      "v_pk_add_f32 v[176:177], v[176:177], v[20:21]\n"
      "v_pk_add_f32 v[178:179], v[178:179], v[22:23]\n"
      "v_pk_add_f32 v[172:173], v[172:173], v[36:37]\n"
      "v_pk_add_f32 v[174:175], v[174:175], v[38:39]\n"
      "s_waitcnt lgkmcnt(8)\n"
      "v_pk_add_f32 v[180:181], v[180:181], v[24:25]\n"
      "v_pk_add_f32 v[182:183], v[182:183], v[26:27]\n"
      "v_pk_add_f32 v[176:177], v[176:177], v[40:41]\n"
      "v_pk_add_f32 v[178:179], v[178:179], v[42:43]\n"
      "s_waitcnt lgkmcnt(5)\n"
      "v_pk_add_f32 v[184:185], v[184:185], v[28:29]\n"
      "v_pk_add_f32 v[186:187], v[186:187], v[30:31]\n"
      "v_pk_add_f32 v[172:173], v[172:173], v[56:57]\n"
      "v_pk_add_f32 v[174:175], v[174:175], v[58:59]\n"
      "v_pk_add_f32 v[180:181], v[180:181], v[44:45]\n"
      "v_pk_add_f32 v[182:183], v[182:183], v[46:47]\n"
      "s_waitcnt lgkmcnt(2)\n"
      "s_nop 0\n"
      "v_pk_add_f32 v[188:189], v[188:189], v[32:33]\n"
      "v_pk_add_f32 v[190:191], v[190:191], v[34:35]\n"
      "v_pk_add_f32 v[176:177], v[176:177], v[60:61]\n"
      "v_pk_add_f32 v[178:179], v[178:179], v[62:63]\n"
      "v_pk_add_f32 v[184:185], v[184:185], v[48:49]\n"
      "v_pk_add_f32 v[186:187], v[186:187], v[50:51]\n"
      "v_pk_mul_f32 v[152:153], v[172:173], s[70:71]\n"
      "v_pk_mul_f32 v[154:155], v[174:175], s[70:71]\n"
      "v_pk_add_f32 v[180:181], v[180:181], v[64:65]\n"
      "v_pk_add_f32 v[182:183], v[182:183], v[66:67]\n"
      "s_waitcnt lgkmcnt(1)\n"
      "s_nop 0\n"
      "v_pk_add_f32 v[188:189], v[188:189], v[52:53]\n"
      "v_pk_add_f32 v[190:191], v[190:191], v[54:55]\n"
      "v_pk_mul_f32 v[156:157], v[176:177], s[70:71]\n"
      "v_pk_mul_f32 v[158:159], v[178:179], s[70:71]\n"
      "v_pk_add_f32 v[184:185], v[184:185], v[68:69]\n"
      "v_pk_add_f32 v[186:187], v[186:187], v[70:71]\n"
      "v_exp_f32 v152, v152\n"
      "v_exp_f32 v153, v153\n"
      "v_exp_f32 v154, v154\n"
      "v_exp_f32 v155, v155\n"
      "v_pk_mul_f32 v[160:161], v[180:181], s[70:71]\n"
      "v_pk_mul_f32 v[162:163], v[182:183], s[70:71]\n"
      "s_waitcnt lgkmcnt(0)\n"
      "v_pk_add_f32 v[188:189], v[188:189], v[72:73]\n"
      "v_pk_add_f32 v[190:191], v[190:191], v[74:75]\n"
      "v_exp_f32 v156, v156\n"
      "v_exp_f32 v157, v157\n"
      "v_exp_f32 v158, v158\n"
      "v_exp_f32 v159, v159\n"
      "v_pk_mul_f32 v[164:165], v[184:185], s[70:71]\n"
      "v_pk_mul_f32 v[166:167], v[186:187], s[70:71]\n"
      "v_exp_f32 v160, v160\n"
      "v_exp_f32 v161, v161\n"
      "v_exp_f32 v162, v162\n"
      "v_exp_f32_e64 v163, v163\n"
      "v_pk_mul_f32 v[168:169], v[188:189], s[70:71]\n"
      "v_pk_mul_f32 v[170:171], v[190:191], s[70:71]\n"
      "v_pk_add_f32 v[152:153], v[152:153], s[72:73]\n"
      "v_pk_add_f32 v[154:155], v[154:155], s[72:73]\n"
      "v_exp_f32 v164, v164\n"
      "v_exp_f32 v165, v165\n"
      "v_exp_f32 v166, v166\n"
      "v_exp_f32 v167, v167\n"
      "v_pk_add_f32 v[156:157], v[156:157], s[72:73]\n"
      "v_pk_add_f32 v[158:159], v[158:159], s[72:73]\n"
      "v_exp_f32 v168, v168\n"
      "v_exp_f32 v169, v169\n"
      "v_exp_f32 v170, v170\n"
      "v_exp_f32 v171, v171\n"
      "v_rcp_f32 v152, v152\n"
      "v_rcp_f32 v153, v153\n"
      "v_rcp_f32 v154, v154\n"
      "v_rcp_f32 v155, v155\n"
      "v_pk_add_f32 v[160:161], v[160:161], s[72:73]\n"
      "v_pk_add_f32 v[162:163], v[162:163], s[72:73]\n"
      "v_rcp_f32 v156, v156\n"
      "v_rcp_f32 v157, v157\n"
      "v_rcp_f32 v158, v158\n"
      "v_rcp_f32 v159, v159\n"
      "v_pk_add_f32 v[164:165], v[164:165], s[72:73]\n"
      "v_pk_add_f32 v[166:167], v[166:167], s[72:73]\n"
      "v_rcp_f32 v160, v160\n"
      "v_rcp_f32 v161, v161\n"
      "v_rcp_f32 v162, v162\n"
      "v_rcp_f32 v163, v163\n"
      "v_pk_add_f32 v[168:169], v[168:169], s[72:73]\n"
      "v_pk_add_f32 v[170:171], v[170:171], s[72:73]\n"
      "v_pk_mul_f32 v[152:153], v[152:153], v[172:173]\n"
      "v_pk_mul_f32 v[154:155], v[154:155], v[174:175]\n"
      "v_rcp_f32 v164, v164\n"
      "v_rcp_f32 v165, v165\n"
      "v_rcp_f32 v166, v166\n"
      "v_rcp_f32 v167, v167\n"
      "v_pk_mul_f32 v[156:157], v[156:157], v[176:177]\n"
      "v_pk_mul_f32 v[158:159], v[158:159], v[178:179]\n"
      "v_rcp_f32 v168, v168\n"
      "v_rcp_f32 v169, v169\n"
      "v_rcp_f32 v170, v170\n"
      "v_rcp_f32 v171, v171\n"
      "v_pk_fma_f32 v[152:153], v[152:153], v[132:133], v[112:113]\n"
      "v_pk_fma_f32 v[154:155], v[154:155], v[134:135], v[114:115]\n"
      "v_pk_mul_f32 v[160:161], v[160:161], v[180:181]\n"
      "v_pk_mul_f32 v[162:163], v[162:163], v[182:183]\n"
      "v_pk_fma_f32 v[156:157], v[156:157], v[136:137], v[116:117]\n"
      "v_pk_fma_f32 v[158:159], v[158:159], v[138:139], v[118:119]\n"
      "v_pk_mul_f32 v[164:165], v[164:165], v[184:185]\n"
      "v_pk_mul_f32 v[166:167], v[166:167], v[186:187]\n"
      "v_cvt_pk_bf16_f32 v76, v152, v153\n"
      "v_cvt_pk_bf16_f32 v77, v154, v155\n"
      "v_pk_fma_f32 v[160:161], v[160:161], v[140:141], v[120:121]\n"
      "v_pk_fma_f32 v[162:163], v[162:163], v[142:143], v[122:123]\n"
      "v_pk_mul_f32 v[168:169], v[168:169], v[188:189]\n"
      "v_pk_mul_f32 v[170:171], v[170:171], v[190:191]\n"
      "v_cvt_pk_bf16_f32 v78, v156, v157\n"
      "v_cvt_pk_bf16_f32 v79, v158, v159\n"
      "v_pk_fma_f32 v[164:165], v[164:165], v[144:145], v[124:125]\n"
      "v_pk_fma_f32 v[166:167], v[166:167], v[146:147], v[126:127]\n"
      "buffer_store_dwordx2 v[76:77], v13, s[40:43], s86 offen offset:0 nt\n"
      "v_cvt_pk_bf16_f32 v80, v160, v161\n"
      "v_cvt_pk_bf16_f32 v81, v162, v163\n"
      "v_pk_fma_f32 v[168:169], v[168:169], v[148:149], v[128:129]\n"
      "v_pk_fma_f32 v[170:171], v[170:171], v[150:151], v[130:131]\n"
      "buffer_store_dwordx2 v[78:79], v13, s[40:43], s86 offen offset:32 nt\n"
      "v_cvt_pk_bf16_f32 v82, v164, v165\n"
      "v_cvt_pk_bf16_f32 v83, v166, v167\n"
      "buffer_store_dwordx2 v[80:81], v13, s[40:43], s86 offen offset:64 nt\n"
      "v_cvt_pk_bf16_f32 v84, v168, v169\n"
      "v_cvt_pk_bf16_f32 v85, v170, v171\n"
      "buffer_store_dwordx2 v[82:83], v13, s[40:43], s86 offen offset:96 nt\n"
      "buffer_store_dwordx2 v[84:85], v13, s[40:43], s86 offen offset:128 nt\n"
      "s_lshl2_add_u32 s52, s30, s31\n"
      "s_cmp_eq_u32 s52, 1020\n"
      "s_cbranch_scc0 K2_noep_19\n"
      "v_xor_b32 v246, s56, v246\n"
      "v_xor_b32 v247, s56, v247\n"
      "v_xor_b32 v248, s56, v248\n"
      "v_xor_b32 v249, s56, v249\n"
      "v_or_b32 v246, v246, v247\n"
      "v_or_b32 v246, v246, v248\n"
      "v_or_b32 v246, v246, v249\n"
      "v_cmp_ne_u32 vcc, 0, v246\n"
      "s_and_b64 vcc, exec, vcc\n"
      "s_cbranch_vccnz K2_spin_20\n"
      "K2_cont_21:\n"
      "v_mov_b32 v6, s56\n"
      "s_mov_b64 exec, 1\n"
      "buffer_store_dword v6, off, s[32:35], 0 sc1\n"
      "s_mov_b64 exec, -1\n"
      "K2_noep_19:\n"
      "K2_FIN:\n"
      "s_waitcnt vmcnt(0)\n"
      "s_endpgm\n"
      "K2_spin_1:\n"
      "s_mov_b32 s3, 20000\n"
      "s_mov_b64 exec, 3\n"
      "K2_spin_1_l:\n"
      "s_sleep 1\n"
      "buffer_load_dword v240, v11, s[32:35], s93 offen sc1\n"
      "s_waitcnt vmcnt(0)\n"
      "v_cmp_ne_u32 vcc, s56, v240\n"
      "s_cbranch_vccz K2_spin_1_x\n"
      "s_sub_u32 s3, s3, 1\n"
      "s_cbranch_scc0 K2_spin_1_l\n"
      "K2_spin_1_x:\n"
      "s_mov_b64 exec, -1\n"
      "s_branch K2_cont_2\n"
      "K2_spin_4:\n"
      "s_mov_b32 s3, 20000\n"
      "K2_spin_4_l:\n"
      "s_sleep 1\n"
      "buffer_load_dword v104, v11, s[32:35], s85 offen sc1\n"
      "s_waitcnt vmcnt(0)\n"
      "v_cmp_ne_u32 vcc, s56, v104\n"
      "s_and_b64 vcc, exec, vcc\n"
      "s_cbranch_vccz K2_cont_5\n"
      "s_sub_u32 s3, s3, 1\n"
      "s_cbranch_scc0 K2_spin_4_l\n"
      "s_branch K2_cont_5\n"
      "K2_spin_6:\n"
      "s_mov_b32 s3, 20000\n"
      "s_mov_b32 exec_hi, 0\n"
      "K2_spin_6_l:\n"
      "s_sleep 1\n"
      "buffer_load_dword v254, v11, s[32:35], s92 offen sc1\n"
      "s_waitcnt vmcnt(0)\n"
      "v_cmp_ne_u32 vcc, s56, v254\n"
      "s_cbranch_vccz K2_spin_6_x\n"
      "s_sub_u32 s3, s3, 1\n"
      "s_cbranch_scc0 K2_spin_6_l\n"
      "K2_spin_6_x:\n"
      "s_mov_b32 exec_hi, -1\n"
      "s_branch K2_cont_7\n"
      "K2_spin_10:\n"
      "s_mov_b32 s3, 20000\n"
      "s_mov_b64 exec, 255\n"
      "K2_spin_10_l:\n"
      "s_sleep 1\n"
      "buffer_load_dword v250, v251, s[32:35], s87 offen sc1\n"
      "s_waitcnt vmcnt(0)\n"
      "v_cmp_ne_u32 vcc, s56, v250\n"
      "s_cbranch_vccz K2_spin_10_x\n"
      "s_sub_u32 s3, s3, 1\n"
      "s_cbranch_scc0 K2_spin_10_l\n"
      "K2_spin_10_x:\n"
      "s_mov_b64 exec, -1\n"
      "s_branch K2_cont_11\n"
      "K2_spin_12:\n"
      "s_mov_b32 s3, 20000\n"
      "s_mov_b32 exec_hi, 0\n"
      "K2_spin_12_l:\n"
      "s_sleep 1\n"
      "buffer_load_dword v207, v11, s[32:35], s92 offen sc1\n"
      "s_waitcnt vmcnt(0)\n"
      "v_cmp_ne_u32 vcc, s56, v207\n"
      "s_cbranch_vccz K2_spin_12_x\n"
      "s_sub_u32 s3, s3, 1\n"
      "s_cbranch_scc0 K2_spin_12_l\n"
      "K2_spin_12_x:\n"
      "s_mov_b32 exec_hi, -1\n"
      "s_branch K2_cont_13\n"
      "K2_spin_14:\n"
      "s_mov_b32 s3, 20000\n"
      "s_mov_b64 exec, 0xff\n"
      "K2_spin_14_l:\n"
      "s_sleep 1\n"
      "buffer_load_dword v246, v245, s[32:35], 0 offen sc1\n"
      "s_waitcnt vmcnt(0)\n"
      "v_cmp_ne_u32 vcc, s56, v246\n"
      "s_cbranch_vccz K2_spin_14_x\n"
      "s_sub_u32 s3, s3, 1\n"
      "s_cbranch_scc0 K2_spin_14_l\n"
      "K2_spin_14_x:\n"
      "s_mov_b64 exec, -1\n"
      "s_branch K2_cont_15\n"
      "K2_spin_16:\n"
      "s_mov_b32 s3, 20000\n"
      "s_mov_b64 exec, 0xfffffff\n"
      "K2_spin_16_l:\n"
      "s_sleep 1\n"
      "buffer_load_dword v246, v253, s[32:35], 0 offen sc1\n"
      "s_waitcnt vmcnt(0)\n"
      "v_cmp_ne_u32 vcc, s56, v246\n"
      "s_cbranch_vccz K2_spin_16_x\n"
      "s_sub_u32 s3, s3, 1\n"
      "s_cbranch_scc0 K2_spin_16_l\n"
      "K2_spin_16_x:\n"
      "s_mov_b64 exec, -1\n"
      "s_branch K2_cont_17\n"
      "K2_spin_20:\n"
      "s_mov_b32 s3, 20000\n"
      "K2_spin_20_l:\n"
      "s_sleep 1\n"
      "buffer_load_dword v246, v11, s[32:35], s82 offen sc1\n"
      "buffer_load_dword v247, v11, s[32:35], s83 offen sc1\n"
      "buffer_load_dword v248, v11, s[32:35], s84 offen sc1\n"
      "buffer_load_dword v249, v11, s[32:35], s85 offen sc1\n"
      "s_waitcnt vmcnt(0)\n"
      "v_xor_b32 v246, s56, v246\n"
      "v_xor_b32 v247, s56, v247\n"
      "v_xor_b32 v248, s56, v248\n"
      "v_xor_b32 v249, s56, v249\n"
      "v_or_b32 v246, v246, v247\n"
      "v_or_b32 v246, v246, v248\n"
      "v_or_b32 v246, v246, v249\n"
      "v_cmp_ne_u32 vcc, 0, v246\n"
      "s_and_b64 vcc, exec, vcc\n"
      "s_cbranch_vccz K2_cont_21\n"
      "s_sub_u32 s3, s3, 1\n"
      "s_cbranch_scc0 K2_spin_20_l\n"
      "s_branch K2_cont_21\n"
      "K2_END:\n"
      "s_nop 0\n"
      :
      : "s"(kernarg), "s"(bid), "v"(tid)
      : "v1", "v2", "v3", "v4", "v5", "v6", "v7", "v8", "v9", "v10", "v11", "v12", "v13", "v14", "v15", "v16",
        "v17", "v18", "v19", "v20", "v21", "v22", "v23", "v24", "v25", "v26", "v27", "v28", "v29", "v30", "v31", "v32",
        "v33", "v34", "v35", "v36", "v37", "v38", "v39", "v40", "v41", "v42", "v43", "v44", "v45", "v46", "v47", "v48",
        "v49", "v50", "v51", "v52", "v53", "v54", "v55", "v56", "v57", "v58", "v59", "v60", "v61", "v62", "v63", "v64",
        "v65", "v66", "v67", "v68", "v69", "v70", "v71", "v72", "v73", "v74", "v75", "v76", "v77", "v78", "v79", "v80",
        "v81", "v82", "v83", "v84", "v85", "v86", "v87", "v88", "v89", "v90", "v91", "v92", "v93", "v94", "v95", "v96",
        "v97", "v98", "v99", "v100", "v101", "v102", "v103", "v104", "v105", "v106", "v107", "v108", "v109", "v110", "v111", "v112",
        "v113", "v114", "v115", "v116", "v117", "v118", "v119", "v120", "v121", "v122", "v123", "v124", "v125", "v126", "v127", "v128",
        "v129", "v130", "v131", "v132", "v133", "v134", "v135", "v136", "v137", "v138", "v139", "v140", "v141", "v142", "v143", "v144",
        "v145", "v146", "v147", "v148", "v149", "v150", "v151", "v152", "v153", "v154", "v155", "v156", "v157", "v158", "v159", "v160",
        "v161", "v162", "v163", "v164", "v165", "v166", "v167", "v168", "v169", "v170", "v171", "v172", "v173", "v174", "v175", "v176",
        "v177", "v178", "v179", "v180", "v181", "v182", "v183", "v184", "v185", "v186", "v187", "v188", "v189", "v190", "v191", "v192",
        "v193", "v194", "v195", "v196", "v197", "v198", "v199", "v200", "v201", "v202", "v203", "v204", "v205", "v206", "v207", "v208",
        "v209", "v210", "v211", "v212", "v213", "v214", "v215", "v216", "v217", "v218", "v219", "v220", "v221", "v222", "v223", "v224",
        "v225", "v226", "v227", "v228", "v229", "v230", "v231", "v232", "v233", "v234", "v235", "v236", "v237", "v238", "v239", "v240",
        "v241", "v242", "v243", "v244", "v245", "v246", "v247", "v248", "v249", "v250", "v251", "v252", "v253", "v254", "v255", "a0",
        "a1", "a2", "a3", "a4", "a5", "a6", "a7", "a8", "a9", "a10", "a11", "a12", "a13", "a14", "a15", "a16",
        "a17", "a18", "a19", "a20", "a21", "a22", "a23", "a24", "a25", "a26", "a27", "a28", "a29", "a30", "a31", "a32",
        "a33", "a34", "a35", "a36", "a37", "a38", "a39", "a40", "a41", "a42", "a43", "a44", "a45", "a46", "a47", "a48",
        "a49", "a50", "a51", "a52", "a53", "a54", "a55", "a56", "a57", "a58", "a59", "a60", "a61", "a62", "a63", "a64",
        "a65", "a66", "a67", "a68", "a69", "a70", "a71", "a72", "a73", "a74", "a75", "a76", "a77", "a78", "a79", "a80",
        "a81", "a82", "a83", "a84", "a85", "a86", "a87", "a88", "a89", "a90", "a91", "a92", "a93", "a94", "a95", "a96",
        "a97", "a98", "a99", "a100", "a101", "a102", "a103", "a104", "a105", "a106", "a107", "a108", "a109", "a110", "a111", "a112",
        "a113", "a114", "a115", "a116", "a117", "a118", "a119", "a120", "a121", "a122", "a123", "a124", "a125", "a126", "a127", "a128",
        "a129", "a130", "a131", "a132", "a133", "a134", "a135", "a136", "a137", "a138", "a139", "a140", "a141", "a142", "a143", "a144",
        "a145", "a146", "a147", "a148", "a149", "a150", "a151", "a152", "a153", "a154", "a155", "a156", "a157", "a158", "a159", "a160",
        "a161", "a162", "a163", "a164", "a165", "a166", "a167", "a168", "a169", "a170", "a171", "a172", "a173", "a174", "a175", "a176",
        "a177", "a178", "a179", "a180", "a181", "a182", "a183", "a184", "a185", "a186", "a187", "a188", "a189", "a190", "a191", "a192",
        "a193", "a194", "a195", "a196", "a197", "a198", "a199", "a200", "a201", "a202", "a203", "a204", "a205", "a206", "a207", "a208",
        "a209", "a210", "a211", "a212", "a213", "a214", "a215", "a216", "a217", "a218", "a219", "a220", "a221", "a222", "a223", "a224",
        "a225", "a226", "a227", "a228", "a229", "a230", "a231", "a232", "a233", "a234", "a235", "a236", "a237", "a238", "a239", "a240",
        "a241", "a242", "a243", "a244", "a245", "a246", "a247", "a248", "a249", "a250", "a251", "a252", "a253", "a254", "a255", "s3",
        "s4", "s5", "s6", "s7", "s8", "s9", "s10", "s11", "s12", "s13", "s14", "s15", "s16", "s17", "s18", "s19",
        "s20", "s21", "s22", "s23", "s24", "s25", "s26", "s27", "s28", "s29", "s30", "s31", "s32", "s33", "s34", "s35",
        "s36", "s37", "s38", "s39", "s40", "s41", "s42", "s43", "s44", "s45", "s46", "s47", "s48", "s49", "s50", "s51",
        "s52", "s53", "s54", "s55", "s56", "s57", "s58", "s59", "s60", "s61", "s62", "s63", "s64", "s65", "s66", "s67",
        "s68", "s69", "s70", "s71", "s72", "s73", "s74", "s75", "s76", "s77", "s78", "s79", "s80", "s81", "s82", "s83",
        "s84", "s85", "s86", "s87", "s88", "s89", "s90", "s91", "s92", "s93", "s94", "s95", "s96", "s97", "s98", "s99",
        "s100", "s101", "vcc", "scc", "memory");
}

extern "C" void kernel_launch(void* const* d_in, const int* in_sizes, int n_in, void* d_out, int out_size, void* d_ws,
                              size_t ws_size, hipStream_t stream) {
  Args a;
  a.x = d_in[0]; a.logA = d_in[1]; a.aim = d_in[2]; a.cre = d_in[3]; a.cim = d_in[4]; a.logdt = d_in[5];
  a.dskip = d_in[6]; a.w1 = d_in[8]; a.b1 = d_in[9]; a.w2 = d_in[10]; a.b2 = d_in[11]; a.out = d_out; a.ws = d_ws;
  fused_kernel<<<256, 256, 0, stream>>>(a);
}
```
